```python
import torch

def _softmax(x, axis=-1):
    return torch.softmax(x, dim=axis)
def _gelu(x):
    return torch.nn.functional.gelu(x, approximate="tanh")
def _silu(x):
    return torch.nn.functional.silu(x)
def _sigmoid(x):
    return torch.sigmoid(x)
def _layernorm(x, axis=-1, eps=1e-5):
    m = x.mean(dim=axis, keepdim=True)
    v = x.var(dim=axis, keepdim=True, unbiased=False)
    return (x - m) / torch.sqrt(v + eps)
def _scatter_add(tgt, idx, src, axis=0):
    return tgt.clone().index_add_(axis, idx, src)
def _segment_sum(src, seg, n):
    out = torch.zeros((n,) + src.shape[1:], dtype=src.dtype)
    return out.index_add_(0, seg, src)
def _pad(x, pw):
    flat = tuple(v for pair in reversed(pw) for v in pair)
    return torch.nn.functional.pad(x, flat)

kernel_name = 'gated_mix__grouped_kv_mqa__glu_gated_geglu__prompt'

def setup_inputs():

    _w = (lambda a: (lambda t: t.to(torch.bfloat16) if t.is_floating_point() else t)(torch.as_tensor(a)))
    def _A_setup():
        B, S, D, H = 1, 2048, 576, 32
        Dh = D // H
        return {
            "x": _w((lambda *s: torch.randn(*s, dtype=torch.bfloat16) * 0.05)(B, S, D)),
            "wq": _w((lambda *s: torch.randn(*s, dtype=torch.bfloat16) * 0.8)(D, H * Dh)),
            "wk": _w((lambda *s: torch.randn(*s, dtype=torch.bfloat16) * 0.8)(D, Dh)),
            "wv": _w((lambda *s: torch.randn(*s, dtype=torch.bfloat16) * 0.05)(D, Dh)),
            "wo": _w((lambda *s: torch.randn(*s, dtype=torch.bfloat16) * 0.05)(H * Dh, D)),
        }
    _A_in = _A_setup()

    def _B_setup():
        B, S, Dm, Df = 1, 2048, 576, 4608
        return {
            "x": _w((lambda *s: torch.randn(*s, dtype=torch.bfloat16) * 0.05)(B, S, Dm)),
            "w_gate": _w((lambda *s: torch.randn(*s, dtype=torch.bfloat16) * 0.8)(Dm, Df) * (Dm ** -0.5)),
            "w_up": _w((lambda *s: torch.randn(*s, dtype=torch.bfloat16) * 0.8)(Dm, Df) * (Dm ** -0.5)),
            "w_down": _w((lambda *s: torch.randn(*s, dtype=torch.bfloat16) * 0.8)(Df, Dm) * (Df ** -0.5)),
        }
    _B_in = _B_setup()

    return {
        "A_x": _A_in["x"],
        "A_wq": _A_in["wq"],
        "A_wk": _A_in["wk"],
        "A_wv": _A_in["wv"],
        "A_wo": _A_in["wo"],
        "B_x": _B_in["x"],
        "B_w_gate": _B_in["w_gate"],
        "B_w_up": _B_in["w_up"],
        "B_w_down": _B_in["w_down"],
        "gate_w": _w((lambda *s: torch.randn(*s, dtype=torch.bfloat16) * 0.05)(576) * 16.0),
    }

def reference(A_x, A_wq, A_wk, A_wv, A_wo, B_x, B_w_gate, B_w_up, B_w_down, gate_w):
    def _A_ref(x, wq, wk, wv, wo):
        B, S, D = x.shape
        H = 32
        Dh = D // H
        q = (lambda x, *d: x.permute(*d))((x @ wq).reshape(B, S, H, Dh), 0, 2, 1, 3)
        k = x @ wk
        v = x @ wv
        scores = torch.einsum("bhqd,bkd->bhqk", q, k) * (Dh ** -0.5)
        p = _softmax(scores, axis=-1)
        o = torch.einsum("bhqk,bkd->bhqd", p, v)
        return (lambda x, *d: x.permute(*d))(o, 0, 2, 1, 3).reshape(B, S, H * Dh) @ wo

    def _B_ref(x, w_gate, w_up, w_down):
        g = torch.einsum("bsd,df->bsf", x, w_gate)
        u = torch.einsum("bsd,df->bsf", x, w_up)
        h = _gelu(g) * u
        return torch.einsum("bsf,fd->bsd", h, w_down)

    x = A_x
    g = _sigmoid((x * gate_w).sum(-1, keepdims=True))
    a = _A_ref(x, A_wq, A_wk, A_wv, A_wo)
    b = _B_ref(x, B_w_gate, B_w_up, B_w_down)
    return g * a + (1 - g) * b
```

```cpp
#include <hip/hip_runtime.h>
#include <hip/hip_bf16.h>
typedef __hip_bfloat16 bf16;
constexpr int S=2048, D=576, H=32, DH=18, F=4608;
__device__ inline float b2f(bf16 x){return __bfloat162float(x);}
__device__ inline bf16 f2b(float x){return __float2bfloat16(x);}
__global__ void gemm_naive(const bf16* A, const bf16* B, float* C, int M, int N, int K){
  __shared__ float As[16][17], Bs[16][17];
  int r = blockIdx.y*16+threadIdx.y, c = blockIdx.x*16+threadIdx.x;
  float acc=0;
  for(int k0=0;k0<K;k0+=16){
    As[threadIdx.y][threadIdx.x] = b2f(A[(size_t)r*K+k0+threadIdx.x]);
    Bs[threadIdx.y][threadIdx.x] = c<N ? b2f(B[(size_t)(k0+threadIdx.y)*N+c]) : 0.f;
    __syncthreads();
    for(int k=0;k<16;k++) acc += As[threadIdx.y][k]*Bs[k][threadIdx.x];
    __syncthreads();
  }
  if(c<N) C[(size_t)r*N+c]=acc;
}
__global__ void geglu(const float* g,const float* u,bf16* h,int n){
  int i=blockIdx.x*256+threadIdx.x; if(i>=n) return;
  float x=g[i]; float t=tanhf(0.7978845608f*(x+0.044715f*x*x*x));
  h[i]=f2b(0.5f*x*(1.f+t)*u[i]);
}
__global__ void attn(const float* q,const float* k,const float* v,bf16* o){
  int idx=blockIdx.x*64+threadIdx.x; int h=idx/S, s=idx%S;
  float qq[DH]; for(int d=0;d<DH;d++) qq[d]=b2f(f2b(q[s*D+h*DH+d]));
  float m=-1e30f,l=0,acc[DH]; for(int d=0;d<DH;d++) acc[d]=0;
  for(int j=0;j<S;j++){
    float sc=0; for(int d=0;d<DH;d++) sc+=qq[d]*b2f(f2b(k[j*DH+d]));
    sc*=0.23570226f;
    float mn=fmaxf(m,sc); float a=__expf(m-mn), p=__expf(sc-mn);
    l=l*a+p; for(int d=0;d<DH;d++) acc[d]=acc[d]*a+p*b2f(f2b(v[j*DH+d])); m=mn;
  }
  for(int d=0;d<DH;d++) o[s*D+h*DH+d]=f2b(acc[d]/l);
}
__global__ void fin(const bf16* x,const bf16* gw,const float* a,const float* b,bf16* out){
  int s=blockIdx.x; __shared__ float red[64];
  float t=0; for(int d=threadIdx.x;d<D;d+=64) t+=b2f(x[s*D+d])*b2f(gw[d]);
  red[threadIdx.x]=t; __syncthreads();
  float z=0; for(int i=0;i<64;i++) z+=red[i];
  float g=1.f/(1.f+__expf(-z));
  for(int d=threadIdx.x;d<D;d+=64) out[s*D+d]=f2b(g*a[s*D+d]+(1.f-g)*b[s*D+d]);
}
extern "C" void kernel_launch(void* const* d_in, const int* in_sizes, int n_in, void* d_out, int out_size, void* d_ws, size_t ws_size, hipStream_t stream){
  const bf16* x=(const bf16*)d_in[0]; const bf16* wq=(const bf16*)d_in[1]; const bf16* wk=(const bf16*)d_in[2]; const bf16* wv=(const bf16*)d_in[3];
  const bf16* wo=(const bf16*)d_in[4]; const bf16* wg=(const bf16*)d_in[6]; const bf16* wu=(const bf16*)d_in[7]; const bf16* wd=(const bf16*)d_in[8]; const bf16* gw=(const bf16*)d_in[9];
  float* q=(float*)d_ws; float* k=q+S*D; float* v=k+S*DH; float* g=v+S*DH; float* u=g+(size_t)S*F; float* a=u+(size_t)S*F; float* b=a+S*D;
  bf16* h=(bf16*)(b+S*D); bf16* o=h+(size_t)S*F;
  dim3 t(16,16);
  gemm_naive<<<dim3(D/16,S/16),t,0,stream>>>(x,wq,q,S,D,D);
  gemm_naive<<<dim3(2,S/16),t,0,stream>>>(x,wk,k,S,DH,D);
  gemm_naive<<<dim3(2,S/16),t,0,stream>>>(x,wv,v,S,DH,D);
  gemm_naive<<<dim3(F/16,S/16),t,0,stream>>>(x,wg,g,S,F,D);
  gemm_naive<<<dim3(F/16,S/16),t,0,stream>>>(x,wu,u,S,F,D);
  geglu<<<S*F/256,256,0,stream>>>(g,u,h,S*F);
  attn<<<H*S/64,64,0,stream>>>(q,k,v,o);
  gemm_naive<<<dim3(D/16,S/16),t,0,stream>>>(o,wo,a,S,D,D);
  gemm_naive<<<dim3(D/16,S/16),t,0,stream>>>(h,wd,b,S,D,F);
  fin<<<S,64,0,stream>>>(x,gw,a,b,(bf16*)d_out);
}
```

```cpp
#include <hip/hip_runtime.h>
#include <hip/hip_bf16.h>

__global__ void __launch_bounds__(256, 1)
fused_mix_kernel(const void* x, const void* wq, const void* wk, const void* wv, const void* wo, const void* wg,
                 const void* wu, const void* wd, const void* gw, void* out, void* ws) {
  __shared__ __attribute__((aligned(16))) char smem[163840];
  const unsigned wave = __builtin_amdgcn_readfirstlane(threadIdx.x >> 6);
  asm volatile(
    ".Lstart:\n"
    "s_load_dwordx16 s[64:79], %0, 0\n"
    "s_load_dwordx4 s[80:83], %0, 64\n"
    "s_load_dwordx2 s[84:85], %0, 80\n"
    "s_getpc_b64 s[96:97]\n"
    "s_mov_b32 s8, %1\n"
    "s_mov_b32 s9, %2\n"
    "v_mbcnt_lo_u32_b32 v192, -1, 0\n"
    "v_mbcnt_hi_u32_b32 v192, -1, v192\n"
    "s_and_b32 s11, s8, 3\n"
    "s_bfe_u32 s12, s8, 0x20003\n"
    "s_lshr_b32 s10, s8, 5\n"
    "s_and_b32 s15, s8, 4\n"
    "s_lshl_b32 s15, s15, 1\n"
    "s_add_u32 s10, s10, s15\n"
    "s_lshr_b32 s13, s9, 1\n"
    "s_and_b32 s14, s9, 1\n"
    "s_lshr_b32 s15, s8, 3\n"
    "s_lshl_b32 s15, s15, 2\n"
    "s_add_u32 s15, s15, s9\n"
    "s_mul_i32 s15, s15, 640\n"
    "v_and_b32 v187, 7, v192\n"
    "v_lshlrev_b32 v187, 7, v187\n"
    "v_add_u32 v187, s15, v187\n"
    "v_min_u32 v187, 70992, v187\n"
    "global_load_dword v252, v187, s[96:97]\n"
    "v_lshrrev_b32 v184, 3, v192\n"
    "v_and_b32 v185, 7, v192\n"
    "v_lshrrev_b32 v186, 1, v184\n"
    "v_xor_b32 v187, v185, v186\n"
    "v_lshlrev_b32 v187, 4, v187\n"
    "v_mul_u32_u24 v199, 1152, v184\n"
    "v_xor_b32 v200, 64, v187\n"
    "v_add_u32 v200, v200, v199\n"
    "v_add_u32 v199, v199, v187\n"
    "v_and_b32 v187, 1, v184\n"
    "v_lshl_add_u32 v187, v186, 2, v187\n"
    "s_lshl_b32 s15, s14, 1\n"
    "v_add_u32 v248, s15, v187\n"
    "v_mul_u32_u24 v198, 9216, v248\n"
    "v_lshl_add_u32 v198, v185, 4, v198\n"
    "s_mul_i32 s56, s9, 1056\n"
    "s_lshl_b32 s58, s9, 12\n"
    "s_mul_i32 s59, s9, 36864\n"
    "s_mul_i32 s5, s11, 2304\n"
    "s_lshl_b32 s15, s12, 7\n"
    "s_add_u32 s5, s5, s15\n"
    "s_lshr_b32 s4, s10, 2\n"
    "s_and_b32 s4, s4, 2\n"
    "s_lshr_b32 s0, s10, 1\n"
    "s_xor_b32 s0, s0, s12\n"
    "s_and_b32 s0, s0, 3\n"
    "s_mul_i32 s15, s0, 589824\n"
    "s_add_u32 s5, s5, s15\n"
    "s_lshl_b32 s15, s0, 7\n"
    "s_add_u32 s59, s59, s15\n"
    "s_lshl_b32 s15, s0, 6\n"
    "v_add_u32 v248, s15, v248\n"
    "s_waitcnt lgkmcnt(0)\n"
    "s_mul_i32 s15, s10, 147456\n"
    "s_add_u32 s16, s64, s15\n"
    "s_addc_u32 s17, s65, 0\n"
    "s_and_b32 s17, s17, 0xffff\n"
    "s_mov_b32 s18, 147456\n"
    "s_mov_b32 s19, 0x20000\n"
    "s_cmp_eq_u32 s13, 0\n"
    "s_cselect_b64 s[54:55], s[74:75], s[76:77]\n"
    "s_mov_b32 s20, s54\n"
    "s_and_b32 s21, s55, 0xffff\n"
    "s_mov_b32 s22, 5308416\n"
    "s_mov_b32 s23, 0x20000\n"
    "s_add_u32 m0, s58, 0x0\n"
    "s_nop 0\n"
    "buffer_load_dwordx4 v199, s[16:19], s59 offen lds\n"
    "s_add_u32 m0, s58, 0x400\n"
    "s_add_u32 s59, s59, 0x2400\n"
    "buffer_load_dwordx4 v200, s[16:19], s59 offen lds\n"
    "s_add_u32 m0, s58, 0x800\n"
    "s_add_u32 s59, s59, 0x2400\n"
    "buffer_load_dwordx4 v199, s[16:19], s59 offen lds\n"
    "s_add_u32 m0, s58, 0xc00\n"
    "s_add_u32 s59, s59, 0x2400\n"
    "buffer_load_dwordx4 v200, s[16:19], s59 offen lds\n"
    "s_xor_b32 s57, s4, 0\n"
    "s_mul_i32 s57, s57, 512\n"
    "s_add_u32 s57, s57, s5\n"
    "s_add_u32 m0, s56, 0x4200\n"
    "s_nop 0\n"
    "buffer_load_dwordx4 v198, s[20:23], s57 offen lds\n"
    "s_add_u32 m0, s56, 0x5280\n"
    "s_add_u32 s57, s57, 0x24000\n"
    "buffer_load_dwordx4 v198, s[20:23], s57 offen lds\n"
    "s_add_u32 m0, s56, 0x6300\n"
    "s_add_u32 s57, s57, 0x24000\n"
    "buffer_load_dwordx4 v198, s[20:23], s57 offen lds\n"
    "s_add_u32 m0, s56, 0x7380\n"
    "s_add_u32 s57, s57, 0x24000\n"
    "buffer_load_dwordx4 v198, s[20:23], s57 offen lds\n"
    "s_add_u32 m0, s58, 0x8400\n"
    "s_add_u32 s59, s59, 0xffff9480\n"
    "s_xor_b32 s1, s0, 1\n"
    "s_xor_b32 s100, s0, 0\n"
    "s_sub_u32 s1, s1, s100\n"
    "s_sub_u32 s1, s1, 1\n"
    "s_mul_i32 s1, s1, 128\n"
    "s_add_u32 s59, s59, s1\n"
    "buffer_load_dwordx4 v199, s[16:19], s59 offen lds\n"
    "v_lshrrev_b32 v188, 5, v192\n"
    "v_bfe_u32 v189, v192, 4, 1\n"
    "v_bfe_u32 v190, v192, 2, 2\n"
    "s_add_u32 m0, s58, 0x8800\n"
    "s_add_u32 s59, s59, 0x2400\n"
    "buffer_load_dwordx4 v200, s[16:19], s59 offen lds\n"
    "v_and_b32 v191, 3, v192\n"
    "v_lshrrev_b32 v184, 1, v190\n"
    "v_mul_u32_u24 v184, 1056, v184\n"
    "s_add_u32 m0, s58, 0x8c00\n"
    "s_add_u32 s59, s59, 0x2400\n"
    "buffer_load_dwordx4 v199, s[16:19], s59 offen lds\n"
    "v_and_b32 v185, 1, v190\n"
    "v_lshl_add_u32 v184, v185, 7, v184\n"
    "v_lshl_add_u32 v184, v188, 9, v184\n"
    "s_add_u32 m0, s58, 0x9000\n"
    "s_add_u32 s59, s59, 0x2400\n"
    "buffer_load_dwordx4 v200, s[16:19], s59 offen lds\n"
    "s_lshl_b32 s15, s14, 5\n"
    "v_add_u32 v225, s15, v184\n"
    "v_lshrrev_b32 v185, 1, v191\n"
    "s_add_u32 m0, s56, 0xc600\n"
    "s_add_u32 s57, s57, 0x24000\n"
    "s_xor_b32 s1, s0, 1\n"
    "s_xor_b32 s100, s0, 0\n"
    "s_sub_u32 s1, s1, s100\n"
    "s_sub_u32 s1, s1, 1\n"
    "s_mul_i32 s1, s1, 589824\n"
    "s_add_u32 s57, s57, s1\n"
    "buffer_load_dwordx4 v198, s[20:23], s57 offen lds\n"
    "v_and_b32 v187, 1, v191\n"
    "v_lshl_add_u32 v186, v187, 4, v225\n"
    "v_lshl_add_u32 v186, v189, 3, v186\n"
    "s_add_u32 m0, s56, 0xd680\n"
    "s_add_u32 s57, s57, 0x24000\n"
    "buffer_load_dwordx4 v198, s[20:23], s57 offen lds\n"
    "v_lshl_add_u32 v250, v185, 6, v186\n"
    "v_mul_u32_u24 v184, 2112, v185\n"
    "v_add_u32 v193, v184, v186\n"
    "s_add_u32 m0, s56, 0xe700\n"
    "s_add_u32 s57, s57, 0x24000\n"
    "buffer_load_dwordx4 v198, s[20:23], s57 offen lds\n"
    "v_add_u32 v194, 0x10000, v193\n"
    "v_add_u32 v195, 0x20000, v193\n"
    "v_and_b32 v184, 31, v192\n"
    "s_add_u32 m0, s56, 0xf780\n"
    "s_add_u32 s57, s57, 0x24000\n"
    "buffer_load_dwordx4 v198, s[20:23], s57 offen lds\n"
    "v_bfe_u32 v185, v184, 1, 3\n"
    "v_xor_b32 v185, v185, v188\n"
    "v_lshlrev_b32 v186, 7, v184\n"
    "s_add_u32 m0, s58, 0x10800\n"
    "s_add_u32 s59, s59, 0xffff9480\n"
    "s_xor_b32 s1, s0, 2\n"
    "s_xor_b32 s100, s0, 1\n"
    "s_sub_u32 s1, s1, s100\n"
    "s_sub_u32 s1, s1, 1\n"
    "s_mul_i32 s1, s1, 128\n"
    "s_add_u32 s59, s59, s1\n"
    "buffer_load_dwordx4 v199, s[16:19], s59 offen lds\n"
    "s_lshl_b32 s15, s13, 13\n"
    "v_add_u32 v186, s15, v186\n"
    "v_xor_b32 v187, 0, v185\n"
    "s_add_u32 m0, s58, 0x10c00\n"
    "s_add_u32 s59, s59, 0x2400\n"
    "buffer_load_dwordx4 v200, s[16:19], s59 offen lds\n"
    "v_lshl_add_u32 v201, v187, 4, v186\n"
    "v_xor_b32 v187, 2, v185\n"
    "v_lshl_add_u32 v202, v187, 4, v186\n"
    "s_add_u32 m0, s58, 0x11000\n"
    "s_add_u32 s59, s59, 0x2400\n"
    "buffer_load_dwordx4 v199, s[16:19], s59 offen lds\n"
    "v_xor_b32 v187, 4, v185\n"
    "v_lshl_add_u32 v203, v187, 4, v186\n"
    "v_xor_b32 v187, 6, v185\n"
    "s_add_u32 m0, s58, 0x11400\n"
    "s_add_u32 s59, s59, 0x2400\n"
    "buffer_load_dwordx4 v200, s[16:19], s59 offen lds\n"
    "v_lshl_add_u32 v204, v187, 4, v186\n"
    "v_lshlrev_b32 v246, 4, v192\n"
    "s_mov_b64 s[50:51], s[84:85]\n"
    "v_mov_b32 v249, 0\n"
    "global_load_dwordx2 v[226:227], v249, s[50:51] sc1\n"
    "s_mov_b32 s92, s80\n"
    "s_and_b32 s93, s81, 0xffff\n"
    "s_mov_b32 s94, 1152\n"
    "s_mov_b32 s95, 0x20000\n"
    "s_lshl_b32 s15, s14, 10\n"
    "s_add_u32 m0, s15, 160512\n"
    "s_lshl_b32 s54, s0, 7\n"
    "s_cmp_eq_u32 s14, 0\n"
    "s_cselect_b32 s54, s54, 0\n"
    "v_add_u32 v217, s15, v246\n"
    "v_xor_b32 v217, s54, v217\n"
    "buffer_load_dwordx4 v217, s[92:95], 0 offen lds\n"
    "s_waitcnt vmcnt(14)\n"
    "s_barrier\n"
    "ds_read_b64_tr_b16 v[128:129], v193 offset:16896\n"
    "ds_read_b64_tr_b16 v[130:131], v193 offset:17152\n"
    "ds_read_b128 a[0:3], v201 offset:0\n"
    "ds_read_b64_tr_b16 v[132:133], v193 offset:16960\n"
    "ds_read_b64_tr_b16 v[134:135], v193 offset:17216\n"
    "ds_read_b128 a[4:7], v201 offset:4096\n"
    "ds_read_b64_tr_b16 v[136:137], v193 offset:21120\n"
    "ds_read_b64_tr_b16 v[138:139], v193 offset:21376\n"
    "ds_read_b128 a[8:11], v202 offset:0\n"
    "ds_read_b64_tr_b16 v[140:141], v193 offset:21184\n"
    "ds_read_b64_tr_b16 v[142:143], v193 offset:21440\n"
    "ds_read_b128 a[12:15], v202 offset:4096\n"
    "s_waitcnt lgkmcnt(6)\n"
    "v_mfma_f32_32x32x16_bf16 v[0:15], v[128:131], a[0:3], 0\n"
    "s_add_u32 m0, s56, 0x14a00\n"
    "s_add_u32 s57, s57, 0x24000\n"
    "s_xor_b32 s1, s0, 2\n"
    "s_xor_b32 s100, s0, 1\n"
    "s_sub_u32 s1, s1, s100\n"
    "s_sub_u32 s1, s1, 1\n"
    "s_mul_i32 s1, s1, 589824\n"
    "s_add_u32 s57, s57, s1\n"
    "ds_read_b64_tr_b16 v[144:145], v193 offset:25344\n"
    "ds_read_b64_tr_b16 v[146:147], v193 offset:25600\n"
    "v_mfma_f32_32x32x16_bf16 v[16:31], v[128:131], a[4:7], 0\n"
    "buffer_load_dwordx4 v198, s[20:23], s57 offen lds\n"
    "ds_read_b128 a[16:19], v203 offset:0\n"
    "s_add_u32 m0, s56, 0x15a80\n"
    "s_add_u32 s57, s57, 0x24000\n"
    "v_mfma_f32_32x32x16_bf16 v[48:63], v[132:135], a[4:7], 0\n"
    "buffer_load_dwordx4 v198, s[20:23], s57 offen lds\n"
    "ds_read_b64_tr_b16 v[148:149], v193 offset:25408\n"
    "ds_read_b64_tr_b16 v[150:151], v193 offset:25664\n"
    "s_add_u32 m0, s56, 0x16b00\n"
    "s_add_u32 s57, s57, 0x24000\n"
    "v_mfma_f32_32x32x16_bf16 v[32:47], v[132:135], a[0:3], 0\n"
    "buffer_load_dwordx4 v198, s[20:23], s57 offen lds\n"
    "ds_read_b128 a[20:23], v203 offset:4096\n"
    "s_lshl_b32 s2, s11, 3\n"
    "s_waitcnt lgkmcnt(6)\n"
    "v_mfma_f32_32x32x16_bf16 v[0:15], v[136:139], a[8:11], v[0:15]\n"
    "s_add_u32 m0, s56, 0x17b80\n"
    "s_add_u32 s57, s57, 0x24000\n"
    "ds_read_b64_tr_b16 v[128:129], v193 offset:29568\n"
    "ds_read_b64_tr_b16 v[130:131], v193 offset:29824\n"
    "s_lshl_b32 s15, s12, 1\n"
    "s_add_u32 s7, s2, s15\n"
    "v_and_b32 v185, 7, v192\n"
    "s_mul_i32 s15, s11, 2304\n"
    "s_lshl_b32 s62, s12, 6\n"
    "v_mfma_f32_32x32x16_bf16 v[16:31], v[136:139], a[12:15], v[16:31]\n"
    "buffer_load_dwordx4 v198, s[20:23], s57 offen lds\n"
    "ds_read_b128 a[24:27], v204 offset:0\n"
    "s_add_u32 m0, s58, 0x18c00\n"
    "s_add_u32 s59, s59, 0xffff9480\n"
    "s_xor_b32 s1, s0, 3\n"
    "s_xor_b32 s100, s0, 2\n"
    "s_sub_u32 s1, s1, s100\n"
    "s_sub_u32 s1, s1, 1\n"
    "s_mul_i32 s1, s1, 128\n"
    "s_add_u32 s59, s59, s1\n"
    "v_mfma_f32_32x32x16_bf16 v[48:63], v[140:143], a[12:15], v[48:63]\n"
    "buffer_load_dwordx4 v199, s[16:19], s59 offen lds\n"
    "ds_read_b64_tr_b16 v[132:133], v193 offset:29632\n"
    "ds_read_b64_tr_b16 v[134:135], v193 offset:29888\n"
    "s_add_u32 m0, s58, 0x19000\n"
    "s_add_u32 s59, s59, 0x2400\n"
    "v_mfma_f32_32x32x16_bf16 v[32:47], v[140:143], a[8:11], v[32:47]\n"
    "buffer_load_dwordx4 v200, s[16:19], s59 offen lds\n"
    "ds_read_b128 a[28:31], v204 offset:4096\n"
    "s_waitcnt vmcnt(12)\n"
    "s_barrier\n"
    "s_waitcnt lgkmcnt(6)\n"
    "v_mfma_f32_32x32x16_bf16 v[0:15], v[144:147], a[16:19], v[0:15]\n"
    "s_add_u32 m0, s58, 0x19400\n"
    "s_add_u32 s59, s59, 0x2400\n"
    "ds_read_b64_tr_b16 v[136:137], v193 offset:50688\n"
    "ds_read_b64_tr_b16 v[138:139], v193 offset:50944\n"
    "s_add_u32 s15, s15, s62\n"
    "s_add_u32 s2, s15, 2048\n"
    "s_mul_i32 s15, s7, 36\n"
    "s_cmp_eq_u32 s13, 0\n"
    "s_cselect_b64 s[60:61], s[74:75], s[66:67]\n"
    "s_cselect_b64 s[62:63], s[76:77], s[66:67]\n"
    "s_cselect_b32 s15, s2, s15\n"
    "s_cselect_b32 s2, 9216, 64\n"
    "s_cselect_b64 s[90:91], 0, -1\n"
    "v_mfma_f32_32x32x16_bf16 v[16:31], v[144:147], a[20:23], v[16:31]\n"
    "buffer_load_dwordx4 v199, s[16:19], s59 offen lds\n"
    "ds_read_b128 a[32:35], v201 offset:33792\n"
    "s_add_u32 m0, s58, 0x19800\n"
    "s_add_u32 s59, s59, 0x2400\n"
    "v_mfma_f32_32x32x16_bf16 v[48:63], v[148:151], a[20:23], v[48:63]\n"
    "buffer_load_dwordx4 v200, s[16:19], s59 offen lds\n"
    "ds_read_b64_tr_b16 v[140:141], v193 offset:50752\n"
    "ds_read_b64_tr_b16 v[142:143], v193 offset:51008\n"
    "s_add_u32 m0, s56, 0x1ce00\n"
    "s_add_u32 s57, s57, 0x24000\n"
    "s_xor_b32 s1, s0, 3\n"
    "s_xor_b32 s100, s0, 2\n"
    "s_sub_u32 s1, s1, s100\n"
    "s_sub_u32 s1, s1, 1\n"
    "s_mul_i32 s1, s1, 589824\n"
    "s_add_u32 s57, s57, s1\n"
    "v_mfma_f32_32x32x16_bf16 v[32:47], v[148:151], a[16:19], v[32:47]\n"
    "buffer_load_dwordx4 v198, s[20:23], s57 offen lds\n"
    "ds_read_b128 a[36:39], v201 offset:37888\n"
    "v_cmp_le_u32 vcc, 4, v185\n"
    "s_waitcnt lgkmcnt(6)\n"
    "v_mfma_f32_32x32x16_bf16 v[0:15], v[128:131], a[24:27], v[0:15]\n"
    "s_add_u32 m0, s56, 0x1de80\n"
    "s_add_u32 s57, s57, 0x24000\n"
    "ds_read_b64_tr_b16 v[144:145], v193 offset:54912\n"
    "ds_read_b64_tr_b16 v[146:147], v193 offset:55168\n"
    "v_mov_b32 v206, s60\n"
    "v_mov_b32 v184, s62\n"
    "v_cndmask_b32 v206, v206, v184, vcc\n"
    "v_mov_b32 v207, s61\n"
    "v_mov_b32 v184, s63\n"
    "v_mfma_f32_32x32x16_bf16 v[16:31], v[128:131], a[28:31], v[16:31]\n"
    "buffer_load_dwordx4 v198, s[20:23], s57 offen lds\n"
    "ds_read_b128 a[40:43], v202 offset:33792\n"
    "s_add_u32 m0, s56, 0x1ef00\n"
    "s_add_u32 s57, s57, 0x24000\n"
    "v_mfma_f32_32x32x16_bf16 v[48:63], v[132:135], a[28:31], v[48:63]\n"
    "buffer_load_dwordx4 v198, s[20:23], s57 offen lds\n"
    "ds_read_b64_tr_b16 v[148:149], v193 offset:54976\n"
    "ds_read_b64_tr_b16 v[150:151], v193 offset:55232\n"
    "s_add_u32 m0, s56, 0x1ff80\n"
    "s_add_u32 s57, s57, 0x24000\n"
    "v_mfma_f32_32x32x16_bf16 v[32:47], v[132:135], a[24:27], v[32:47]\n"
    "buffer_load_dwordx4 v198, s[20:23], s57 offen lds\n"
    "ds_read_b128 a[44:47], v202 offset:37888\n"
    "v_cndmask_b32 v207, v207, v184, vcc\n"
    "s_waitcnt lgkmcnt(6)\n"
    "v_mfma_f32_32x32x16_bf16 v[0:15], v[136:139], a[32:35], v[0:15]\n"
    "s_add_u32 m0, s58, 0x21000\n"
    "s_add_u32 s59, s59, 0xffff9480\n"
    "s_xor_b32 s1, s0, 4\n"
    "s_xor_b32 s100, s0, 3\n"
    "s_sub_u32 s1, s1, s100\n"
    "s_sub_u32 s1, s1, 1\n"
    "s_mul_i32 s1, s1, 128\n"
    "s_add_u32 s59, s59, s1\n"
    "ds_read_b64_tr_b16 v[128:129], v193 offset:59136\n"
    "ds_read_b64_tr_b16 v[130:131], v193 offset:59392\n"
    "v_mfma_f32_32x32x16_bf16 v[16:31], v[136:139], a[36:39], v[16:31]\n"
    "buffer_load_dwordx4 v199, s[16:19], s59 offen lds\n"
    "ds_read_b128 a[48:51], v203 offset:33792\n"
    "s_add_u32 m0, s58, 0x21400\n"
    "s_add_u32 s59, s59, 0x2400\n"
    "v_mfma_f32_32x32x16_bf16 v[48:63], v[140:143], a[36:39], v[48:63]\n"
    "buffer_load_dwordx4 v200, s[16:19], s59 offen lds\n"
    "ds_read_b64_tr_b16 v[132:133], v193 offset:59200\n"
    "ds_read_b64_tr_b16 v[134:135], v193 offset:59456\n"
    "s_add_u32 m0, s58, 0x21800\n"
    "s_add_u32 s59, s59, 0x2400\n"
    "v_mfma_f32_32x32x16_bf16 v[32:47], v[140:143], a[32:35], v[32:47]\n"
    "buffer_load_dwordx4 v199, s[16:19], s59 offen lds\n"
    "ds_read_b128 a[52:55], v203 offset:37888\n"
    "v_mov_b32 v208, 1152\n"
    "s_waitcnt lgkmcnt(6)\n"
    "v_mfma_f32_32x32x16_bf16 v[0:15], v[144:147], a[40:43], v[0:15]\n"
    "s_add_u32 m0, s58, 0x21c00\n"
    "s_add_u32 s59, s59, 0x2400\n"
    "ds_read_b64_tr_b16 v[136:137], v193 offset:63360\n"
    "ds_read_b64_tr_b16 v[138:139], v193 offset:63616\n"
    "v_max_u32 v208, s2, v208\n"
    "v_and_b32 v186, 3, v185\n"
    "v_lshlrev_b32 v186, 4, v186\n"
    "v_bfe_u32 v184, v185, 1, 1\n"
    "v_mul_u32_u24 v184, 36, v184\n"
    "v_mfma_f32_32x32x16_bf16 v[16:31], v[144:147], a[44:47], v[16:31]\n"
    "buffer_load_dwordx4 v200, s[16:19], s59 offen lds\n"
    "ds_read_b128 a[56:59], v204 offset:33792\n"
    "s_add_u32 m0, s56, 0x25200\n"
    "s_add_u32 s57, s57, 0x24000\n"
    "s_xor_b32 s1, s0, 4\n"
    "s_xor_b32 s100, s0, 3\n"
    "s_sub_u32 s1, s1, s100\n"
    "s_sub_u32 s1, s1, 1\n"
    "s_mul_i32 s1, s1, 589824\n"
    "s_add_u32 s57, s57, s1\n"
    "v_mfma_f32_32x32x16_bf16 v[48:63], v[148:151], a[44:47], v[48:63]\n"
    "buffer_load_dwordx4 v198, s[20:23], s57 offen lds\n"
    "ds_read_b64_tr_b16 v[140:141], v193 offset:63424\n"
    "ds_read_b64_tr_b16 v[142:143], v193 offset:63680\n"
    "s_add_u32 m0, s56, 0x26280\n"
    "s_add_u32 s57, s57, 0x24000\n"
    "v_mfma_f32_32x32x16_bf16 v[32:47], v[148:151], a[40:43], v[32:47]\n"
    "buffer_load_dwordx4 v198, s[20:23], s57 offen lds\n"
    "ds_read_b128 a[60:63], v204 offset:37888\n"
    "s_waitcnt vmcnt(14)\n"
    "s_barrier\n"
    "s_waitcnt lgkmcnt(6)\n"
    "v_mfma_f32_32x32x16_bf16 v[0:15], v[128:131], a[48:51], v[0:15]\n"
    "s_add_u32 m0, s56, 0x0\n"
    "s_add_u32 s57, s57, 0x24000\n"
    "ds_read_b64_tr_b16 v[144:145], v194 offset:18944\n"
    "ds_read_b64_tr_b16 v[146:147], v194 offset:19200\n"
    "v_and_b32 v187, 1, v185\n"
    "v_lshlrev_b32 v187, 4, v187\n"
    "v_cndmask_b32 v187, v187, 20, vcc\n"
    "v_add_u32 v184, v184, v187\n"
    "v_cndmask_b32 v186, v186, v184, s[90:91]\n"
    "v_mfma_f32_32x32x16_bf16 v[16:31], v[128:131], a[52:55], v[16:31]\n"
    "buffer_load_dwordx4 v198, s[20:23], s57 offen lds\n"
    "v_add_u32 v201, 0xe800, v201\n"
    "ds_read_b128 a[64:67], v201 offset:8192\n"
    "v_mfma_f32_32x32x16_bf16 v[48:63], v[132:135], a[52:55], v[48:63]\n"
    "s_add_u32 m0, s56, 0x1080\n"
    "s_add_u32 s57, s57, 0x24000\n"
    "ds_read_b64_tr_b16 v[148:149], v194 offset:19008\n"
    "ds_read_b64_tr_b16 v[150:151], v194 offset:19264\n"
    "v_add_u32 v186, s15, v186\n"
    "s_add_u32 s2, s7, 0\n"
    "s_cmp_ge_u32 s2, 3\n"
    "s_cselect_b64 s[54:55], s[70:71], s[68:69]\n"
    "s_cselect_b32 s15, 3, 0\n"
    "v_mfma_f32_32x32x16_bf16 v[32:47], v[132:135], a[48:51], v[32:47]\n"
    "buffer_load_dwordx4 v198, s[20:23], s57 offen lds\n"
    "ds_read_b128 a[68:71], v201 offset:12288\n"
    "s_sub_u32 s60, s2, s15\n"
    "s_waitcnt lgkmcnt(6)\n"
    "v_mfma_f32_32x32x16_bf16 v[0:15], v[136:139], a[56:59], v[0:15]\n"
    "s_add_u32 m0, s58, 0x2100\n"
    "s_add_u32 s59, s59, 0xffff9480\n"
    "s_xor_b32 s1, s0, 5\n"
    "s_xor_b32 s100, s0, 4\n"
    "s_sub_u32 s1, s1, s100\n"
    "s_sub_u32 s1, s1, 1\n"
    "s_mul_i32 s1, s1, 128\n"
    "s_add_u32 s59, s59, s1\n"
    "ds_read_b64_tr_b16 v[128:129], v194 offset:23168\n"
    "ds_read_b64_tr_b16 v[130:131], v194 offset:23424\n"
    "v_mfma_f32_32x32x16_bf16 v[16:31], v[136:139], a[60:63], v[16:31]\n"
    "buffer_load_dwordx4 v199, s[16:19], s59 offen lds\n"
    "v_add_u32 v202, 0xe800, v202\n"
    "ds_read_b128 a[72:75], v202 offset:8192\n"
    "v_mfma_f32_32x32x16_bf16 v[48:63], v[140:143], a[60:63], v[48:63]\n"
    "s_add_u32 m0, s58, 0x2500\n"
    "s_add_u32 s59, s59, 0x2400\n"
    "ds_read_b64_tr_b16 v[132:133], v194 offset:23232\n"
    "ds_read_b64_tr_b16 v[134:135], v194 offset:23488\n"
    "s_lshl_b32 s15, s60, 4\n"
    "s_cmp_eq_u32 s60, 2\n"
    "s_cselect_b32 s15, 20, s15\n"
    "s_cmp_ge_u32 s2, 6\n"
    "s_cselect_b32 s2, 0, s13\n"
    "v_mfma_f32_32x32x16_bf16 v[32:47], v[140:143], a[56:59], v[32:47]\n"
    "buffer_load_dwordx4 v200, s[16:19], s59 offen lds\n"
    "ds_read_b128 a[76:79], v202 offset:12288\n"
    "s_lshl_b32 s2, s2, 3\n"
    "s_waitcnt lgkmcnt(6)\n"
    "v_mfma_f32_32x32x16_bf16 v[0:15], v[144:147], a[64:67], v[0:15]\n"
    "s_add_u32 m0, s58, 0x2900\n"
    "s_add_u32 s59, s59, 0x2400\n"
    "ds_read_b64_tr_b16 v[136:137], v194 offset:27392\n"
    "ds_read_b64_tr_b16 v[138:139], v194 offset:27648\n"
    "v_add_u32 v184, s2, v185\n"
    "v_cmp_eq_u32 vcc, 13, v184\n"
    "v_mov_b32 v184, s54\n"
    "s_nop 0\n"
    "v_cndmask_b32 v206, v206, v184, vcc\n"
    "v_mov_b32 v184, s55\n"
    "v_mfma_f32_32x32x16_bf16 v[16:31], v[144:147], a[68:71], v[16:31]\n"
    "buffer_load_dwordx4 v199, s[16:19], s59 offen lds\n"
    "v_add_u32 v203, 0xe800, v203\n"
    "ds_read_b128 a[80:83], v203 offset:8192\n"
    "v_mfma_f32_32x32x16_bf16 v[48:63], v[148:151], a[68:71], v[48:63]\n"
    "s_add_u32 m0, s58, 0x2d00\n"
    "s_add_u32 s59, s59, 0x2400\n"
    "ds_read_b64_tr_b16 v[140:141], v194 offset:27456\n"
    "ds_read_b64_tr_b16 v[142:143], v194 offset:27712\n"
    "v_cndmask_b32 v207, v207, v184, vcc\n"
    "v_mov_b32 v184, s15\n"
    "v_cndmask_b32 v208, v208, 36, vcc\n"
    "v_cndmask_b32 v186, v186, v184, vcc\n"
    "s_add_u32 s2, s7, 1\n"
    "v_mfma_f32_32x32x16_bf16 v[32:47], v[148:151], a[64:67], v[32:47]\n"
    "buffer_load_dwordx4 v200, s[16:19], s59 offen lds\n"
    "ds_read_b128 a[84:87], v203 offset:12288\n"
    "s_cmp_ge_u32 s2, 3\n"
    "s_cselect_b64 s[54:55], s[70:71], s[68:69]\n"
    "s_cselect_b32 s15, 3, 0\n"
    "s_waitcnt lgkmcnt(6)\n"
    "v_mfma_f32_32x32x16_bf16 v[0:15], v[128:131], a[72:75], v[0:15]\n"
    "s_add_u32 m0, s56, 0x6300\n"
    "s_add_u32 s57, s57, 0x24000\n"
    "s_xor_b32 s1, s0, 5\n"
    "s_xor_b32 s100, s0, 4\n"
    "s_sub_u32 s1, s1, s100\n"
    "s_sub_u32 s1, s1, 1\n"
    "s_mul_i32 s1, s1, 589824\n"
    "s_add_u32 s57, s57, s1\n"
    "ds_read_b64_tr_b16 v[144:145], v194 offset:31616\n"
    "ds_read_b64_tr_b16 v[146:147], v194 offset:31872\n"
    "v_mfma_f32_32x32x16_bf16 v[16:31], v[128:131], a[76:79], v[16:31]\n"
    "buffer_load_dwordx4 v198, s[20:23], s57 offen lds\n"
    "v_add_u32 v204, 0xe800, v204\n"
    "ds_read_b128 a[88:91], v204 offset:8192\n"
    "v_mfma_f32_32x32x16_bf16 v[48:63], v[132:135], a[76:79], v[48:63]\n"
    "s_add_u32 m0, s56, 0x7380\n"
    "s_add_u32 s57, s57, 0x24000\n"
    "ds_read_b64_tr_b16 v[148:149], v194 offset:31680\n"
    "ds_read_b64_tr_b16 v[150:151], v194 offset:31936\n"
    "s_sub_u32 s60, s2, s15\n"
    "s_lshl_b32 s15, s60, 4\n"
    "s_cmp_eq_u32 s60, 2\n"
    "s_cselect_b32 s15, 20, s15\n"
    "s_cmp_ge_u32 s2, 6\n"
    "s_cselect_b32 s2, 0, s13\n"
    "v_mfma_f32_32x32x16_bf16 v[32:47], v[132:135], a[72:75], v[32:47]\n"
    "buffer_load_dwordx4 v198, s[20:23], s57 offen lds\n"
    "ds_read_b128 a[92:95], v204 offset:12288\n"
    "s_waitcnt vmcnt(14)\n"
    "s_barrier\n"
    "s_waitcnt lgkmcnt(6)\n"
    "v_mfma_f32_32x32x16_bf16 v[0:15], v[136:139], a[80:83], v[0:15]\n"
    "s_add_u32 m0, s56, 0x8400\n"
    "s_add_u32 s57, s57, 0x24000\n"
    "ds_read_b64_tr_b16 v[128:129], v194 offset:52736\n"
    "ds_read_b64_tr_b16 v[130:131], v194 offset:52992\n"
    "s_lshl_b32 s2, s2, 3\n"
    "v_add_u32 v184, s2, v185\n"
    "v_cmp_eq_u32 vcc, 15, v184\n"
    "v_mov_b32 v184, s54\n"
    "s_nop 0\n"
    "v_cndmask_b32 v206, v206, v184, vcc\n"
    "v_mfma_f32_32x32x16_bf16 v[16:31], v[136:139], a[84:87], v[16:31]\n"
    "buffer_load_dwordx4 v198, s[20:23], s57 offen lds\n"
    "ds_read_b128 a[96:99], v201 offset:41984\n"
    "v_mov_b32 v184, s55\n"
    "v_mfma_f32_32x32x16_bf16 v[48:63], v[140:143], a[84:87], v[48:63]\n"
    "s_add_u32 m0, s56, 0x9480\n"
    "s_add_u32 s57, s57, 0x24000\n"
    "ds_read_b64_tr_b16 v[132:133], v194 offset:52800\n"
    "ds_read_b64_tr_b16 v[134:135], v194 offset:53056\n"
    "v_cndmask_b32 v207, v207, v184, vcc\n"
    "v_mov_b32 v184, s15\n"
    "v_cndmask_b32 v208, v208, 36, vcc\n"
    "v_cndmask_b32 v186, v186, v184, vcc\n"
    "v_mad_u32_u24 v186, v208, v248, v186\n"
    "v_mfma_f32_32x32x16_bf16 v[32:47], v[140:143], a[80:83], v[32:47]\n"
    "buffer_load_dwordx4 v198, s[20:23], s57 offen lds\n"
    "ds_read_b128 a[100:103], v201 offset:46080\n"
    "v_mov_b32 v187, 0\n"
    "s_waitcnt lgkmcnt(6)\n"
    "v_mfma_f32_32x32x16_bf16 v[0:15], v[144:147], a[88:91], v[0:15]\n"
    "s_add_u32 m0, s58, 0xa500\n"
    "s_add_u32 s59, s59, 0xffff9480\n"
    "s_xor_b32 s1, s0, 6\n"
    "s_xor_b32 s100, s0, 5\n"
    "s_sub_u32 s1, s1, s100\n"
    "s_sub_u32 s1, s1, 1\n"
    "s_mul_i32 s1, s1, 128\n"
    "s_add_u32 s59, s59, s1\n"
    "ds_read_b64_tr_b16 v[136:137], v194 offset:56960\n"
    "ds_read_b64_tr_b16 v[138:139], v194 offset:57216\n"
    "v_mfma_f32_32x32x16_bf16 v[16:31], v[144:147], a[92:95], v[16:31]\n"
    "buffer_load_dwordx4 v199, s[16:19], s59 offen lds\n"
    "ds_read_b128 a[104:107], v202 offset:41984\n"
    "v_lshl_add_u64 v[206:207], v[186:187], 0, v[206:207]\n"
    "v_mfma_f32_32x32x16_bf16 v[48:63], v[148:151], a[92:95], v[48:63]\n"
    "s_add_u32 m0, s58, 0xa900\n"
    "s_add_u32 s59, s59, 0x2400\n"
    "ds_read_b64_tr_b16 v[140:141], v194 offset:57024\n"
    "ds_read_b64_tr_b16 v[142:143], v194 offset:57280\n"
    "v_lshlrev_b32 v208, 4, v208\n"
    "v_mov_b32 v209, 0\n"
    "v_lshrrev_b32 v185, 1, v191\n"
    "v_and_b32 v187, 1, v191\n"
    "v_lshlrev_b32 v187, 4, v187\n"
    "v_mfma_f32_32x32x16_bf16 v[32:47], v[148:151], a[88:91], v[32:47]\n"
    "buffer_load_dwordx4 v200, s[16:19], s59 offen lds\n"
    "ds_read_b128 a[108:111], v202 offset:46080\n"
    "v_lshl_add_u32 v187, v185, 3, v187\n"
    "s_waitcnt lgkmcnt(6)\n"
    "v_mfma_f32_32x32x16_bf16 v[0:15], v[128:131], a[96:99], v[0:15]\n"
    "s_add_u32 m0, s58, 0xad00\n"
    "s_add_u32 s59, s59, 0x2400\n"
    "ds_read_b64_tr_b16 v[144:145], v194 offset:61184\n"
    "ds_read_b64_tr_b16 v[146:147], v194 offset:61440\n"
    "v_cmp_eq_u32 vcc, 0, v191\n"
    "v_lshlrev_b32 v186, 3, v191\n"
    "v_lshlrev_b32 v197, 4, v188\n"
    "v_cndmask_b32 v186, v186, 8, vcc\n"
    "v_cmp_eq_u32 vcc, 1, v189\n"
    "v_mfma_f32_32x32x16_bf16 v[16:31], v[128:131], a[100:103], v[16:31]\n"
    "buffer_load_dwordx4 v199, s[16:19], s59 offen lds\n"
    "ds_read_b128 a[112:115], v203 offset:41984\n"
    "v_add_u32 v186, 64, v186\n"
    "v_mfma_f32_32x32x16_bf16 v[48:63], v[132:135], a[100:103], v[48:63]\n"
    "s_add_u32 m0, s58, 0xb100\n"
    "s_add_u32 s59, s59, 0x2400\n"
    "ds_read_b64_tr_b16 v[148:149], v194 offset:61248\n"
    "ds_read_b64_tr_b16 v[150:151], v194 offset:61504\n"
    "v_add_u32 v197, 98304, v197\n"
    "v_cndmask_b32 v187, v187, v186, vcc\n"
    "v_add_u32 v196, v225, v187\n"
    "v_add_u32 v196, 2112, v196\n"
    "s_mov_b32 s52, 0x110000\n"
    "v_mfma_f32_32x32x16_bf16 v[32:47], v[132:135], a[96:99], v[32:47]\n"
    "buffer_load_dwordx4 v200, s[16:19], s59 offen lds\n"
    "ds_read_b128 a[116:119], v203 offset:46080\n"
    "s_mov_b32 s53, 0x110000\n"
    "s_waitcnt lgkmcnt(6)\n"
    "v_mfma_f32_32x32x16_bf16 v[0:15], v[136:139], a[104:107], v[0:15]\n"
    "s_add_u32 m0, s56, 0xe700\n"
    "s_add_u32 s57, s57, 0x24000\n"
    "s_xor_b32 s1, s0, 6\n"
    "s_xor_b32 s100, s0, 5\n"
    "s_sub_u32 s1, s1, s100\n"
    "s_sub_u32 s1, s1, 1\n"
    "s_mul_i32 s1, s1, 589824\n"
    "s_add_u32 s57, s57, s1\n"
    "ds_read_b64_tr_b16 v[128:129], v194 offset:65408\n"
    "ds_read_b64_tr_b16 v[130:131], v195 offset:128\n"
    "v_mfma_f32_32x32x16_bf16 v[16:31], v[136:139], a[108:111], v[16:31]\n"
    "buffer_load_dwordx4 v198, s[20:23], s57 offen lds\n"
    "ds_read_b128 a[120:123], v204 offset:41984\n"
    "s_mov_b32 s88, 0\n"
    "v_mfma_f32_32x32x16_bf16 v[48:63], v[140:143], a[108:111], v[48:63]\n"
    "s_add_u32 m0, s56, 0xf780\n"
    "s_add_u32 s57, s57, 0x24000\n"
    "ds_read_b64_tr_b16 v[132:133], v194 offset:65472\n"
    "ds_read_b64_tr_b16 v[134:135], v195 offset:192\n"
    "s_mov_b32 s89, -1\n"
    "v_lshl_add_u32 v184, v188, 2, v190\n"
    "v_mul_u32_u24 v184, 48, v184\n"
    "v_lshlrev_b32 v186, 3, v191\n"
    "v_lshl_add_u32 v187, v185, 3, 32\n"
    "v_mfma_f32_32x32x16_bf16 v[32:47], v[140:143], a[104:107], v[32:47]\n"
    "buffer_load_dwordx4 v198, s[20:23], s57 offen lds\n"
    "ds_read_b128 a[124:127], v204 offset:46080\n"
    "s_waitcnt vmcnt(14)\n"
    "s_barrier\n"
    "s_waitcnt lgkmcnt(6)\n"
    "v_mfma_f32_32x32x16_bf16 v[0:15], v[144:147], a[112:115], v[0:15]\n"
    "s_add_u32 m0, s56, 0x10800\n"
    "s_add_u32 s57, s57, 0x24000\n"
    "ds_read_b64_tr_b16 v[136:137], v195 offset:20992\n"
    "ds_read_b64_tr_b16 v[138:139], v195 offset:21248\n"
    "v_cndmask_b32 v186, v186, v187, vcc\n"
    "v_add_u32 v230, v184, v186\n"
    "v_mov_b32 v228, v246\n"
    "v_lshlrev_b32 v229, 3, v192\n"
    "v_lshrrev_b32 v184, 2, v192\n"
    "v_mfma_f32_32x32x16_bf16 v[16:31], v[144:147], a[116:119], v[16:31]\n"
    "buffer_load_dwordx4 v198, s[20:23], s57 offen lds\n"
    "v_add_u32 v201, 0x9800, v201\n"
    "ds_read_b128 a[128:131], v201 offset:36864\n"
    "v_mfma_f32_32x32x16_bf16 v[48:63], v[148:151], a[116:119], v[48:63]\n"
    "s_add_u32 m0, s56, 0x11880\n"
    "s_add_u32 s57, s57, 0x24000\n"
    "ds_read_b64_tr_b16 v[140:141], v195 offset:21056\n"
    "ds_read_b64_tr_b16 v[142:143], v195 offset:21312\n"
    "v_mul_u32_u24 v184, 288, v184\n"
    "v_lshl_add_u32 v233, v191, 3, v184\n"
    "v_add_u32 v234, 0x10000, v233\n"
    "v_add_u32 v235, 0x20000, v233\n"
    "v_and_b32 v184, 31, v192\n"
    "v_mfma_f32_32x32x16_bf16 v[32:47], v[148:151], a[112:115], v[32:47]\n"
    "buffer_load_dwordx4 v198, s[20:23], s57 offen lds\n"
    "ds_read_b128 a[132:135], v201 offset:40960\n"
    "v_lshrrev_b32 v185, 4, v184\n"
    "s_waitcnt lgkmcnt(6)\n"
    "v_mfma_f32_32x32x16_bf16 v[0:15], v[128:131], a[120:123], v[0:15]\n"
    "s_add_u32 m0, s58, 0x12900\n"
    "s_add_u32 s59, s59, 0xffff9480\n"
    "s_xor_b32 s1, s0, 7\n"
    "s_xor_b32 s100, s0, 6\n"
    "s_sub_u32 s1, s1, s100\n"
    "s_sub_u32 s1, s1, 1\n"
    "s_mul_i32 s1, s1, 128\n"
    "s_add_u32 s59, s59, s1\n"
    "ds_read_b64_tr_b16 v[144:145], v195 offset:25216\n"
    "ds_read_b64_tr_b16 v[146:147], v195 offset:25472\n"
    "v_mfma_f32_32x32x16_bf16 v[16:31], v[128:131], a[124:127], v[16:31]\n"
    "buffer_load_dwordx4 v199, s[16:19], s59 offen lds\n"
    "v_add_u32 v202, 0x9800, v202\n"
    "ds_read_b128 a[136:139], v202 offset:36864\n"
    "v_mfma_f32_32x32x16_bf16 v[48:63], v[132:135], a[124:127], v[48:63]\n"
    "s_add_u32 m0, s58, 0x12d00\n"
    "s_add_u32 s59, s59, 0x2400\n"
    "ds_read_b64_tr_b16 v[148:149], v195 offset:25280\n"
    "ds_read_b64_tr_b16 v[150:151], v195 offset:25536\n"
    "s_lshl_b32 s15, s13, 2\n"
    "v_add_u32 v185, s15, v185\n"
    "v_mul_u32_u24 v185, 41984, v185\n"
    "v_and_b32 v186, 15, v184\n"
    "v_lshl_add_u32 v187, v186, 4, v185\n"
    "v_mfma_f32_32x32x16_bf16 v[32:47], v[132:135], a[120:123], v[32:47]\n"
    "buffer_load_dwordx4 v200, s[16:19], s59 offen lds\n"
    "ds_read_b128 a[140:143], v202 offset:40960\n"
    "v_lshl_add_u32 v187, v188, 8, v187\n"
    "s_waitcnt lgkmcnt(6)\n"
    "v_mfma_f32_32x32x16_bf16 v[0:15], v[136:139], a[128:131], v[0:15]\n"
    "s_add_u32 m0, s58, 0x13100\n"
    "s_add_u32 s59, s59, 0x2400\n"
    "ds_read_b64_tr_b16 v[128:129], v193 offset:0\n"
    "ds_read_b64_tr_b16 v[130:131], v193 offset:256\n"
    "s_lshl_b32 s15, s14, 9\n"
    "v_add_u32 v210, s15, v187\n"
    "v_add_u32 v211, 83968, v210\n"
    "v_lshl_add_u32 v187, v186, 3, v185\n"
    "s_lshl_b32 s15, s12, 7\n"
    "v_mfma_f32_32x32x16_bf16 v[16:31], v[136:139], a[132:135], v[16:31]\n"
    "buffer_load_dwordx4 v199, s[16:19], s59 offen lds\n"
    "v_add_u32 v203, 0x9800, v203\n"
    "ds_read_b128 a[144:147], v203 offset:36864\n"
    "v_mfma_f32_32x32x16_bf16 v[48:63], v[140:143], a[132:135], v[48:63]\n"
    "s_add_u32 m0, s58, 0x13500\n"
    "s_add_u32 s59, s59, 0x2400\n"
    "ds_read_b64_tr_b16 v[132:133], v193 offset:64\n"
    "ds_read_b64_tr_b16 v[134:135], v193 offset:320\n"
    "s_lshl_b32 s54, s14, 2\n"
    "s_add_u32 s15, s15, s54\n"
    "s_add_u32 s15, s15, 40960\n"
    "v_add_u32 v245, s15, v187\n"
    "s_mul_i32 s15, s9, 83968\n"
    "v_mfma_f32_32x32x16_bf16 v[32:47], v[140:143], a[128:131], v[32:47]\n"
    "buffer_load_dwordx4 v200, s[16:19], s59 offen lds\n"
    "ds_read_b128 a[148:151], v203 offset:40960\n"
    "v_add_u32 v231, s15, v246\n"
    "s_waitcnt lgkmcnt(6)\n"
    "v_mfma_f32_32x32x16_bf16 v[0:15], v[144:147], a[136:139], v[0:15]\n"
    "s_add_u32 m0, s56, 0x16b00\n"
    "s_add_u32 s57, s57, 0x24000\n"
    "s_xor_b32 s1, s0, 7\n"
    "s_xor_b32 s100, s0, 6\n"
    "s_sub_u32 s1, s1, s100\n"
    "s_sub_u32 s1, s1, 1\n"
    "s_mul_i32 s1, s1, 589824\n"
    "s_add_u32 s57, s57, s1\n"
    "ds_read_b64_tr_b16 v[136:137], v193 offset:4224\n"
    "ds_read_b64_tr_b16 v[138:139], v193 offset:4480\n"
    "v_mfma_f32_32x32x16_bf16 v[16:31], v[144:147], a[140:143], v[16:31]\n"
    "buffer_load_dwordx4 v198, s[20:23], s57 offen lds\n"
    "v_add_u32 v204, 0x9800, v204\n"
    "ds_read_b128 a[152:155], v204 offset:36864\n"
    "v_mfma_f32_32x32x16_bf16 v[48:63], v[148:151], a[140:143], v[48:63]\n"
    "s_add_u32 m0, s56, 0x17b80\n"
    "s_add_u32 s57, s57, 0x24000\n"
    "ds_read_b64_tr_b16 v[140:141], v193 offset:4288\n"
    "ds_read_b64_tr_b16 v[142:143], v193 offset:4544\n"
    "v_add_u32 v232, 41984, v231\n"
    "s_lshl_b32 s6, s12, 11\n"
    "s_add_u32 s2, s7, s14\n"
    "s_cmp_ge_u32 s2, 3\n"
    "s_cselect_b32 s15, 3, 0\n"
    "v_mfma_f32_32x32x16_bf16 v[32:47], v[148:151], a[136:139], v[32:47]\n"
    "buffer_load_dwordx4 v198, s[20:23], s57 offen lds\n"
    "ds_read_b128 a[156:159], v204 offset:40960\n"
    "s_waitcnt vmcnt(14)\n"
    "s_barrier\n"
    "s_waitcnt lgkmcnt(6)\n"
    "v_mfma_f32_32x32x16_bf16 v[0:15], v[128:131], a[144:147], v[0:15]\n"
    "s_add_u32 m0, s56, 0x18c00\n"
    "s_add_u32 s57, s57, 0x24000\n"
    "ds_read_b64_tr_b16 v[144:145], v193 offset:25344\n"
    "ds_read_b64_tr_b16 v[146:147], v193 offset:25600\n"
    "s_sub_u32 s3, s2, s15\n"
    "s_lshl_b32 s15, s13, 6\n"
    "v_add_u32 v185, s15, v184\n"
    "s_lshl_b32 s15, s13, 11\n"
    "s_lshl_b32 s54, s3, 9\n"
    "v_mfma_f32_32x32x16_bf16 v[16:31], v[128:131], a[148:151], v[16:31]\n"
    "buffer_load_dwordx4 v198, s[20:23], s57 offen lds\n"
    "v_add_u32 v201, 0xfffe8100, v201\n"
    "ds_read_b128 a[160:163], v201 offset:8192\n"
    "v_mfma_f32_32x32x16_bf16 v[48:63], v[132:135], a[148:151], v[48:63]\n"
    "s_add_u32 m0, s56, 0x19c80\n"
    "s_add_u32 s57, s57, 0x24000\n"
    "ds_read_b64_tr_b16 v[148:149], v193 offset:25408\n"
    "ds_read_b64_tr_b16 v[150:151], v193 offset:25664\n"
    "s_add_u32 s15, s15, s54\n"
    "v_lshlrev_b32 v186, 4, v184\n"
    "v_lshl_add_u32 v186, v188, 3, v186\n"
    "v_add_u32 v224, s15, v186\n"
    "s_mov_b32 s87, 1024\n"
    "v_mfma_f32_32x32x16_bf16 v[32:47], v[132:135], a[144:147], v[32:47]\n"
    "buffer_load_dwordx4 v198, s[20:23], s57 offen lds\n"
    "ds_read_b128 a[164:167], v201 offset:12288\n"
    "v_lshlrev_b32 v186, 10, v188\n"
    "s_waitcnt lgkmcnt(6)\n"
    "v_mfma_f32_32x32x16_bf16 v[0:15], v[136:139], a[152:155], v[0:15]\n"
    "s_add_u32 m0, s58, 0x1ad00\n"
    "s_add_u32 s59, s59, 0xffff9480\n"
    "s_mov_b32 s1, 8\n"
    "s_xor_b32 s100, s0, 7\n"
    "s_sub_u32 s1, s1, s100\n"
    "s_sub_u32 s1, s1, 1\n"
    "s_mul_i32 s1, s1, 128\n"
    "s_add_u32 s59, s59, s1\n"
    "ds_read_b64_tr_b16 v[128:129], v193 offset:29568\n"
    "ds_read_b64_tr_b16 v[130:131], v193 offset:29824\n"
    "v_mfma_f32_32x32x16_bf16 v[16:31], v[136:139], a[156:159], v[16:31]\n"
    "buffer_load_dwordx4 v199, s[16:19], s59 offen lds\n"
    "v_add_u32 v202, 0xfffe8100, v202\n"
    "ds_read_b128 a[168:171], v202 offset:8192\n"
    "v_mfma_f32_32x32x16_bf16 v[48:63], v[140:143], a[156:159], v[48:63]\n"
    "s_add_u32 m0, s58, 0x1b100\n"
    "s_add_u32 s59, s59, 0x2400\n"
    "ds_read_b64_tr_b16 v[132:133], v193 offset:29632\n"
    "ds_read_b64_tr_b16 v[134:135], v193 offset:29888\n"
    "v_sub_u32 v186, 11264, v186\n"
    "v_lshl_add_u32 v186, v185, 3, v186\n"
    "s_cmp_eq_u32 s2, 2\n"
    "s_cselect_b64 vcc, -1, 0\n"
    "s_cselect_b32 s87, 256, s87\n"
    "v_mfma_f32_32x32x16_bf16 v[32:47], v[140:143], a[152:155], v[32:47]\n"
    "buffer_load_dwordx4 v200, s[16:19], s59 offen lds\n"
    "ds_read_b128 a[172:175], v202 offset:12288\n"
    "v_cndmask_b32 v224, v224, v186, vcc\n"
    "s_waitcnt lgkmcnt(6)\n"
    "v_mfma_f32_32x32x16_bf16 v[0:15], v[144:147], a[160:163], v[0:15]\n"
    "s_add_u32 m0, s58, 0x1b500\n"
    "s_add_u32 s59, s59, 0x2400\n"
    "ds_read_b64_tr_b16 v[136:137], v193 offset:33792\n"
    "ds_read_b64_tr_b16 v[138:139], v193 offset:34048\n"
    "v_lshlrev_b32 v186, 3, v188\n"
    "s_lshl_b32 s15, s3, 4\n"
    "v_add_u32 v187, s15, v186\n"
    "v_sub_u32 v186, 40, v186\n"
    "s_cmp_eq_u32 s3, 2\n"
    "s_cselect_b64 s[90:91], -1, 0\n"
    "v_mfma_f32_32x32x16_bf16 v[16:31], v[144:147], a[164:167], v[16:31]\n"
    "buffer_load_dwordx4 v199, s[16:19], s59 offen lds\n"
    "v_add_u32 v203, 0xfffe8100, v203\n"
    "ds_read_b128 a[176:179], v203 offset:8192\n"
    "v_mfma_f32_32x32x16_bf16 v[48:63], v[148:151], a[164:167], v[48:63]\n"
    "s_add_u32 m0, s58, 0x1b900\n"
    "s_add_u32 s59, s59, 0x2400\n"
    "ds_read_b64_tr_b16 v[140:141], v193 offset:33856\n"
    "ds_read_b64_tr_b16 v[142:143], v193 offset:34112\n"
    "v_cndmask_b32 v187, v187, v186, s[90:91]\n"
    "v_mul_u32_u24 v186, 48, v185\n"
    "v_add_u32 v186, v186, v187\n"
    "v_add_u32 v186, 4096, v186\n"
    "s_cmp_ge_u32 s2, 3\n"
    "s_cselect_b64 vcc, -1, 0\n"
    "s_cselect_b32 s87, 1536, s87\n"
    "s_cselect_b32 s15, 0x3f80, 0\n"
    "v_mfma_f32_32x32x16_bf16 v[32:47], v[148:151], a[160:163], v[32:47]\n"
    "buffer_load_dwordx4 v200, s[16:19], s59 offen lds\n"
    "ds_read_b128 a[180:183], v203 offset:12288\n"
    "v_cndmask_b32 v224, v224, v186, vcc\n"
    "s_waitcnt lgkmcnt(6)\n"
    "v_mfma_f32_32x32x16_bf16 v[0:15], v[128:131], a[168:171], v[0:15]\n"
    "s_add_u32 m0, s56, 0x1ef00\n"
    "s_add_u32 s57, s57, 0x24000\n"
    "s_mov_b32 s1, 8\n"
    "s_xor_b32 s100, s0, 7\n"
    "s_sub_u32 s1, s1, s100\n"
    "s_sub_u32 s1, s1, 1\n"
    "s_mul_i32 s1, s1, 589824\n"
    "s_add_u32 s57, s57, s1\n"
    "ds_read_b64_tr_b16 v[144:145], v193 offset:38016\n"
    "ds_read_b64_tr_b16 v[146:147], v193 offset:38272\n"
    "v_mfma_f32_32x32x16_bf16 v[16:31], v[128:131], a[172:175], v[16:31]\n"
    "buffer_load_dwordx4 v198, s[20:23], s57 offen lds\n"
    "v_add_u32 v204, 0xfffe8100, v204\n"
    "ds_read_b128 a[184:187], v204 offset:8192\n"
    "v_mfma_f32_32x32x16_bf16 v[48:63], v[132:135], a[172:175], v[48:63]\n"
    "s_add_u32 m0, s56, 0x1ff80\n"
    "s_add_u32 s57, s57, 0x24000\n"
    "ds_read_b64_tr_b16 v[148:149], v193 offset:38080\n"
    "ds_read_b64_tr_b16 v[150:151], v193 offset:38336\n"
    "v_mul_u32_u24 v247, s15, v188\n"
    "s_mul_i32 s15, s10, 12288\n"
    "s_cmp_ge_u32 s2, 6\n"
    "s_cselect_b32 s15, 0x80000000, s15\n"
    "v_add_u32 v224, s15, v224\n"
    "v_mfma_f32_32x32x16_bf16 v[32:47], v[132:135], a[168:171], v[32:47]\n"
    "buffer_load_dwordx4 v198, s[20:23], s57 offen lds\n"
    "ds_read_b128 a[188:191], v204 offset:12288\n"
    "s_waitcnt vmcnt(14)\n"
    "s_barrier\n"
    "s_waitcnt lgkmcnt(6)\n"
    "v_mfma_f32_32x32x16_bf16 v[0:15], v[136:139], a[176:179], v[0:15]\n"
    "s_add_u32 m0, s56, 0x21000\n"
    "s_add_u32 s57, s57, 0x24000\n"
    "ds_read_b64_tr_b16 v[128:129], v193 offset:59136\n"
    "ds_read_b64_tr_b16 v[130:131], v193 offset:59392\n"
    "v_mul_u32_u24 v184, 3641, v192\n"
    "v_add_u32 v185, 0, v184\n"
    "v_bfe_u32 v186, v185, 18, 2\n"
    "v_bfe_u32 v185, v185, 16, 2\n"
    "v_mul_u32_u24 v186, 8064, v186\n"
    "v_mfma_f32_32x32x16_bf16 v[16:31], v[136:139], a[180:183], v[16:31]\n"
    "buffer_load_dwordx4 v198, s[20:23], s57 offen lds\n"
    "ds_read_b128 a[192:195], v201 offset:41984\n"
    "v_mul_u32_u24 v185, 864, v185\n"
    "v_mfma_f32_32x32x16_bf16 v[48:63], v[140:143], a[180:183], v[48:63]\n"
    "s_add_u32 m0, s56, 0x22080\n"
    "s_add_u32 s57, s57, 0x24000\n"
    "ds_read_b64_tr_b16 v[132:133], v193 offset:59200\n"
    "ds_read_b64_tr_b16 v[134:135], v193 offset:59456\n"
    "v_add_u32 v187, 0, v246\n"
    "v_add3_u32 v236, v185, v186, v187\n"
    "v_add_u32 v185, 233024, v184\n"
    "v_bfe_u32 v186, v185, 18, 2\n"
    "v_bfe_u32 v185, v185, 16, 2\n"
    "v_mfma_f32_32x32x16_bf16 v[32:47], v[140:143], a[176:179], v[32:47]\n"
    "buffer_load_dwordx4 v198, s[20:23], s57 offen lds\n"
    "ds_read_b128 a[196:199], v201 offset:46080\n"
    "v_mul_u32_u24 v186, 8064, v186\n"
    "s_waitcnt lgkmcnt(6)\n"
    "v_mfma_f32_32x32x16_bf16 v[0:15], v[144:147], a[184:187], v[0:15]\n"
    "s_xor_b32 s57, s4, 1\n"
    "s_mul_i32 s57, s57, 512\n"
    "s_add_u32 s57, s57, s5\n"
    "s_add_u32 m0, s56, 0x23100\n"
    "ds_read_b64_tr_b16 v[136:137], v193 offset:63360\n"
    "ds_read_b64_tr_b16 v[138:139], v193 offset:63616\n"
    "v_mul_u32_u24 v185, 864, v185\n"
    "v_add3_u32 v237, v185, v186, v187\n"
    "v_add_u32 v185, 466048, v184\n"
    "v_mfma_f32_32x32x16_bf16 v[16:31], v[144:147], a[188:191], v[16:31]\n"
    "buffer_load_dwordx4 v198, s[20:23], s57 offen lds\n"
    "ds_read_b128 a[200:203], v202 offset:41984\n"
    "v_bfe_u32 v186, v185, 18, 2\n"
    "v_mfma_f32_32x32x16_bf16 v[48:63], v[148:151], a[188:191], v[48:63]\n"
    "ds_read_b64_tr_b16 v[140:141], v193 offset:63424\n"
    "ds_read_b64_tr_b16 v[142:143], v193 offset:63680\n"
    "v_bfe_u32 v185, v185, 16, 2\n"
    "v_mul_u32_u24 v186, 8064, v186\n"
    "v_mul_u32_u24 v185, 864, v185\n"
    "v_add3_u32 v238, v185, v186, v187\n"
    "v_add_u32 v185, 699072, v184\n"
    "v_bfe_u32 v186, v185, 18, 2\n"
    "v_mfma_f32_32x32x16_bf16 v[32:47], v[148:151], a[184:187], v[32:47]\n"
    "ds_read_b128 a[204:207], v202 offset:46080\n"
    "v_bfe_u32 v185, v185, 16, 2\n"
    "v_mul_u32_u24 v186, 8064, v186\n"
    "v_mul_u32_u24 v185, 864, v185\n"
    "v_add3_u32 v239, v185, v186, v187\n"
    "v_add_u32 v185, 932096, v184\n"
    "v_bfe_u32 v186, v185, 18, 2\n"
    "v_bfe_u32 v185, v185, 16, 2\n"
    "s_waitcnt lgkmcnt(6)\n"
    "v_mfma_f32_32x32x16_bf16 v[0:15], v[128:131], a[192:195], v[0:15]\n"
    "s_add_u32 m0, s56, 0x24180\n"
    "s_add_u32 s57, s57, 0x24000\n"
    "ds_read_b64_tr_b16 v[144:145], v194 offset:2048\n"
    "ds_read_b64_tr_b16 v[146:147], v194 offset:2304\n"
    "v_mul_u32_u24 v186, 8064, v186\n"
    "v_mul_u32_u24 v185, 864, v185\n"
    "v_add_u32 v187, 4096, v246\n"
    "v_add3_u32 v240, v185, v186, v187\n"
    "v_add_u32 v185, 1165120, v184\n"
    "v_mfma_f32_32x32x16_bf16 v[16:31], v[128:131], a[196:199], v[16:31]\n"
    "buffer_load_dwordx4 v198, s[20:23], s57 offen lds\n"
    "ds_read_b128 a[208:211], v203 offset:41984\n"
    "v_bfe_u32 v186, v185, 18, 2\n"
    "v_mfma_f32_32x32x16_bf16 v[48:63], v[132:135], a[196:199], v[48:63]\n"
    "s_add_u32 m0, s56, 0x25200\n"
    "s_add_u32 s57, s57, 0x24000\n"
    "ds_read_b64_tr_b16 v[148:149], v194 offset:2112\n"
    "ds_read_b64_tr_b16 v[150:151], v194 offset:2368\n"
    "v_bfe_u32 v185, v185, 16, 2\n"
    "v_mul_u32_u24 v186, 8064, v186\n"
    "v_mul_u32_u24 v185, 864, v185\n"
    "v_add3_u32 v241, v185, v186, v187\n"
    "v_add_u32 v185, 1398144, v184\n"
    "v_mfma_f32_32x32x16_bf16 v[32:47], v[132:135], a[192:195], v[32:47]\n"
    "buffer_load_dwordx4 v198, s[20:23], s57 offen lds\n"
    "ds_read_b128 a[212:215], v203 offset:46080\n"
    "v_bfe_u32 v186, v185, 18, 2\n"
    "s_waitcnt lgkmcnt(6)\n"
    "v_mfma_f32_32x32x16_bf16 v[0:15], v[136:139], a[200:203], v[0:15]\n"
    "s_add_u32 m0, s56, 0x26280\n"
    "s_add_u32 s57, s57, 0x24000\n"
    "ds_read_b64_tr_b16 v[128:129], v194 offset:6272\n"
    "ds_read_b64_tr_b16 v[130:131], v194 offset:6528\n"
    "v_bfe_u32 v185, v185, 16, 2\n"
    "v_mul_u32_u24 v186, 8064, v186\n"
    "v_mul_u32_u24 v185, 864, v185\n"
    "v_add3_u32 v242, v185, v186, v187\n"
    "v_add_u32 v185, 1631168, v184\n"
    "v_mfma_f32_32x32x16_bf16 v[16:31], v[136:139], a[204:207], v[16:31]\n"
    "buffer_load_dwordx4 v198, s[20:23], s57 offen lds\n"
    "ds_read_b128 a[216:219], v204 offset:41984\n"
    "v_bfe_u32 v186, v185, 18, 2\n"
    "v_mfma_f32_32x32x16_bf16 v[48:63], v[140:143], a[204:207], v[48:63]\n"
    "ds_read_b64_tr_b16 v[132:133], v194 offset:6336\n"
    "ds_read_b64_tr_b16 v[134:135], v194 offset:6592\n"
    "v_bfe_u32 v185, v185, 16, 2\n"
    "v_mul_u32_u24 v186, 8064, v186\n"
    "v_mul_u32_u24 v185, 864, v185\n"
    "v_add3_u32 v243, v185, v186, v187\n"
    "v_add_u32 v185, 1864192, v184\n"
    "v_bfe_u32 v186, v185, 18, 2\n"
    "v_mfma_f32_32x32x16_bf16 v[32:47], v[140:143], a[200:203], v[32:47]\n"
    "ds_read_b128 a[220:223], v204 offset:46080\n"
    "v_bfe_u32 v185, v185, 16, 2\n"
    "v_mul_u32_u24 v186, 8064, v186\n"
    "v_mul_u32_u24 v185, 864, v185\n"
    "v_add_u32 v187, 8192, v246\n"
    "s_waitcnt vmcnt(12)\n"
    "s_barrier\n"
    "s_waitcnt lgkmcnt(6)\n"
    "v_mfma_f32_32x32x16_bf16 v[0:15], v[144:147], a[208:211], v[0:15]\n"
    "s_add_u32 m0, s56, 0x0\n"
    "s_add_u32 s57, s57, 0x24000\n"
    "s_xor_b32 s1, s0, 1\n"
    "s_xor_b32 s100, s0, 0\n"
    "s_sub_u32 s1, s1, s100\n"
    "s_sub_u32 s1, s1, 1\n"
    "s_mul_i32 s1, s1, 589824\n"
    "s_add_u32 s57, s57, s1\n"
    "ds_read_b64_tr_b16 v[136:137], v194 offset:27392\n"
    "ds_read_b64_tr_b16 v[138:139], v194 offset:27648\n"
    "v_mfma_f32_32x32x16_bf16 v[16:31], v[144:147], a[212:215], v[16:31]\n"
    "buffer_load_dwordx4 v198, s[20:23], s57 offen lds\n"
    "v_add_u32 v201, 0x10800, v201\n"
    "ds_read_b128 a[224:227], v201 offset:8192\n"
    "v_mfma_f32_32x32x16_bf16 v[48:63], v[148:151], a[212:215], v[48:63]\n"
    "s_add_u32 m0, s56, 0x1080\n"
    "s_add_u32 s57, s57, 0x24000\n"
    "ds_read_b64_tr_b16 v[140:141], v194 offset:27456\n"
    "ds_read_b64_tr_b16 v[142:143], v194 offset:27712\n"
    "v_add3_u32 v244, v185, v186, v187\n"
    "s_lshl_b32 s15, s10, 2\n"
    "s_add_u32 s15, s15, s11\n"
    "s_lshl_b32 s64, s15, 2\n"
    "s_add_u32 s64, s64, s12\n"
    "v_mfma_f32_32x32x16_bf16 v[32:47], v[148:151], a[208:211], v[32:47]\n"
    "buffer_load_dwordx4 v198, s[20:23], s57 offen lds\n"
    "ds_read_b128 a[228:231], v201 offset:12288\n"
    "s_mul_i32 s15, s15, 335872\n"
    "s_waitcnt lgkmcnt(6)\n"
    "v_mfma_f32_32x32x16_bf16 v[0:15], v[128:131], a[216:219], v[0:15]\n"
    "s_add_u32 m0, s56, 0x2100\n"
    "s_add_u32 s57, s57, 0x24000\n"
    "ds_read_b64_tr_b16 v[144:145], v194 offset:31616\n"
    "ds_read_b64_tr_b16 v[146:147], v194 offset:31872\n"
    "s_add_u32 s15, s15, 10485760\n"
    "s_add_u32 s24, s84, s15\n"
    "s_addc_u32 s25, s85, 0\n"
    "s_and_b32 s25, s25, 0xffff\n"
    "s_mov_b32 s26, 335872\n"
    "v_mfma_f32_32x32x16_bf16 v[16:31], v[128:131], a[220:223], v[16:31]\n"
    "buffer_load_dwordx4 v198, s[20:23], s57 offen lds\n"
    "v_add_u32 v202, 0x10800, v202\n"
    "ds_read_b128 a[232:235], v202 offset:8192\n"
    "v_mfma_f32_32x32x16_bf16 v[48:63], v[132:135], a[220:223], v[48:63]\n"
    "ds_read_b64_tr_b16 v[148:149], v194 offset:31680\n"
    "ds_read_b64_tr_b16 v[150:151], v194 offset:31936\n"
    "s_mov_b32 s27, 0x20000\n"
    "s_add_u32 s28, s84, 655360\n"
    "s_addc_u32 s29, s85, 0\n"
    "s_and_b32 s29, s29, 0xffff\n"
    "s_mov_b32 s30, 196608\n"
    "s_mov_b32 s31, 0x20000\n"
    "v_mfma_f32_32x32x16_bf16 v[32:47], v[132:135], a[216:219], v[32:47]\n"
    "ds_read_b128 a[236:239], v202 offset:12288\n"
    "s_add_u32 s44, s84, 1048576\n"
    "s_addc_u32 s45, s85, 0\n"
    "s_and_b32 s45, s45, 0xffff\n"
    "s_mov_b32 s46, 9437184\n"
    "s_mov_b32 s47, 0x20000\n"
    "s_mul_i32 s15, s11, 1327104\n"
    "s_mul_i32 s54, s12, 288\n"
    "s_waitcnt lgkmcnt(6)\n"
    "v_mfma_f32_32x32x16_bf16 v[0:15], v[136:139], a[224:227], v[0:15]\n"
    "s_add_u32 m0, s56, 0x3180\n"
    "s_add_u32 s57, s57, 0x24000\n"
    "ds_read_b64_tr_b16 v[128:129], v194 offset:35840\n"
    "ds_read_b64_tr_b16 v[130:131], v194 offset:36096\n"
    "s_add_u32 s15, s15, s54\n"
    "s_sub_u32 s55, 5308416, s15\n"
    "s_add_u32 s32, s78, s15\n"
    "s_addc_u32 s33, s79, 0\n"
    "s_and_b32 s33, s33, 0xffff\n"
    "v_mfma_f32_32x32x16_bf16 v[16:31], v[136:139], a[228:231], v[16:31]\n"
    "buffer_load_dwordx4 v198, s[20:23], s57 offen lds\n"
    "v_add_u32 v203, 0x10800, v203\n"
    "ds_read_b128 a[240:243], v203 offset:8192\n"
    "v_mfma_f32_32x32x16_bf16 v[48:63], v[140:143], a[228:231], v[48:63]\n"
    "s_add_u32 m0, s56, 0x4200\n"
    "s_add_u32 s57, s57, 0x24000\n"
    "s_xor_b32 s1, s0, 2\n"
    "s_xor_b32 s100, s0, 1\n"
    "s_sub_u32 s1, s1, s100\n"
    "s_sub_u32 s1, s1, 1\n"
    "s_mul_i32 s1, s1, 589824\n"
    "s_add_u32 s57, s57, s1\n"
    "ds_read_b64_tr_b16 v[132:133], v194 offset:35904\n"
    "ds_read_b64_tr_b16 v[134:135], v194 offset:36160\n"
    "v_mfma_f32_32x32x16_bf16 v[32:47], v[140:143], a[224:227], v[32:47]\n"
    "buffer_load_dwordx4 v198, s[20:23], s57 offen lds\n"
    "ds_read_b128 a[244:247], v203 offset:12288\n"
    "s_mov_b32 s34, s55\n"
    "s_waitcnt lgkmcnt(6)\n"
    "v_mfma_f32_32x32x16_bf16 v[0:15], v[144:147], a[232:235], v[0:15]\n"
    "s_add_u32 m0, s56, 0x5280\n"
    "s_add_u32 s57, s57, 0x24000\n"
    "ds_read_b64_tr_b16 v[136:137], v194 offset:40064\n"
    "ds_read_b64_tr_b16 v[138:139], v194 offset:40320\n"
    "s_mov_b32 s35, 0x20000\n"
    "s_mul_i32 s15, s11, 165888\n"
    "s_add_u32 s15, s15, s54\n"
    "s_sub_u32 s55, 663552, s15\n"
    "s_add_u32 s36, s72, s15\n"
    "s_addc_u32 s37, s73, 0\n"
    "v_mfma_f32_32x32x16_bf16 v[16:31], v[144:147], a[236:239], v[16:31]\n"
    "buffer_load_dwordx4 v198, s[20:23], s57 offen lds\n"
    "v_add_u32 v204, 0x10800, v204\n"
    "ds_read_b128 a[248:251], v204 offset:8192\n"
    "v_mfma_f32_32x32x16_bf16 v[48:63], v[148:151], a[236:239], v[48:63]\n"
    "ds_read_b64_tr_b16 v[140:141], v194 offset:40128\n"
    "ds_read_b64_tr_b16 v[142:143], v194 offset:40384\n"
    "s_and_b32 s37, s37, 0xffff\n"
    "s_mov_b32 s38, s55\n"
    "s_mov_b32 s39, 0x20000\n"
    "s_lshl_b32 s15, s10, 2\n"
    "s_add_u32 s15, s15, s11\n"
    "s_mul_i32 s15, s15, 36864\n"
    "v_mfma_f32_32x32x16_bf16 v[32:47], v[148:151], a[232:235], v[32:47]\n"
    "ds_read_b128 a[252:255], v204 offset:12288\n"
    "s_add_u32 s15, s15, s54\n"
    "s_sub_u32 s55, 2359296, s15\n"
    "s_add_u32 s40, s82, s15\n"
    "s_addc_u32 s41, s83, 0\n"
    "s_waitcnt vmcnt(10)\n"
    "s_barrier\n"
    "s_waitcnt lgkmcnt(6)\n"
    "v_mfma_f32_32x32x16_bf16 v[0:15], v[128:131], a[240:243], v[0:15]\n"
    "s_add_u32 m0, s56, 0x6300\n"
    "s_add_u32 s57, s57, 0x24000\n"
    "ds_read_b64_tr_b16 v[144:145], v194 offset:61184\n"
    "ds_read_b64_tr_b16 v[146:147], v194 offset:61440\n"
    "s_and_b32 s41, s41, 0xffff\n"
    "s_mov_b32 s42, s55\n"
    "s_mov_b32 s43, 0x20000\n"
    "s_mov_b32 s68, s84\n"
    "s_and_b32 s69, s85, 0xffff\n"
    "v_mfma_f32_32x32x16_bf16 v[16:31], v[128:131], a[244:247], v[16:31]\n"
    "buffer_load_dwordx4 v198, s[20:23], s57 offen lds\n"
    "ds_read_b128 v[152:155], v201 offset:41984\n"
    "s_mov_b32 s70, 16384\n"
    "v_mfma_f32_32x32x16_bf16 v[48:63], v[132:135], a[244:247], v[48:63]\n"
    "s_add_u32 m0, s56, 0x7380\n"
    "s_add_u32 s57, s57, 0x24000\n"
    "ds_read_b64_tr_b16 v[148:149], v194 offset:61248\n"
    "ds_read_b64_tr_b16 v[150:151], v194 offset:61504\n"
    "s_mov_b32 s71, 0x20000\n"
    "s_mov_b32 s76, s66\n"
    "s_and_b32 s77, s67, 0xffff\n"
    "s_mov_b32 s78, 663552\n"
    "s_mov_b32 s79, 0x20000\n"
    "v_mfma_f32_32x32x16_bf16 v[32:47], v[132:135], a[240:243], v[32:47]\n"
    "buffer_load_dwordx4 v198, s[20:23], s57 offen lds\n"
    "ds_read_b128 v[156:159], v201 offset:46080\n"
    "s_mul_i32 s60, s9, 3072\n"
    "s_waitcnt lgkmcnt(6)\n"
    "v_mfma_f32_32x32x16_bf16 v[0:15], v[136:139], a[248:251], v[0:15]\n"
    "s_add_u32 m0, s56, 0x8400\n"
    "s_add_u32 s57, s57, 0x24000\n"
    "s_xor_b32 s1, s0, 3\n"
    "s_xor_b32 s100, s0, 2\n"
    "s_sub_u32 s1, s1, s100\n"
    "s_sub_u32 s1, s1, 1\n"
    "s_mul_i32 s1, s1, 589824\n"
    "s_add_u32 s57, s57, s1\n"
    "ds_read_b64_tr_b16 v[128:129], v194 offset:65408\n"
    "ds_read_b64_tr_b16 v[130:131], v195 offset:128\n"
    "v_mfma_f32_32x32x16_bf16 v[16:31], v[136:139], a[252:255], v[16:31]\n"
    "buffer_load_dwordx4 v198, s[20:23], s57 offen lds\n"
    "ds_read_b128 v[160:163], v202 offset:41984\n"
    "s_mov_b32 s61, s60\n"
    "v_mfma_f32_32x32x16_bf16 v[48:63], v[140:143], a[252:255], v[48:63]\n"
    "ds_read_b64_tr_b16 v[132:133], v194 offset:65472\n"
    "ds_read_b64_tr_b16 v[134:135], v195 offset:192\n"
    "s_mul_i32 s62, s9, 9216\n"
    "s_mul_i32 s63, s9, 36864\n"
    "s_mov_b32 s72, 0\n"
    "v_mov_b32 v205, 0xbdd2d3e8\n"
    "v_mov_b32 v221, 0\n"
    "v_readfirstlane_b32 s48, v226\n"
    "v_mfma_f32_32x32x16_bf16 v[32:47], v[140:143], a[248:251], v[32:47]\n"
    "ds_read_b128 v[164:167], v202 offset:46080\n"
    "v_readfirstlane_b32 s49, v227\n"
    "s_nop 1\n"
    "s_add_u32 s48, s48, 0x7f4a7c15\n"
    "s_addc_u32 s49, s49, 0x9e3779b9\n"
    "s_waitcnt lgkmcnt(6)\n"
    "v_mfma_f32_32x32x16_bf16 v[0:15], v[144:147], v[152:155], v[0:15]\n"
    "s_add_u32 m0, s56, 0x9480\n"
    "s_add_u32 s57, s57, 0x24000\n"
    "ds_read_b64_tr_b16 v[136:137], v195 offset:4096\n"
    "ds_read_b64_tr_b16 v[138:139], v195 offset:4352\n"
    "v_mfma_f32_32x32x16_bf16 v[16:31], v[144:147], v[156:159], v[16:31]\n"
    "buffer_load_dwordx4 v198, s[20:23], s57 offen lds\n"
    "ds_read_b128 v[168:171], v203 offset:41984\n"
    "v_mfma_f32_32x32x16_bf16 v[48:63], v[148:151], v[156:159], v[48:63]\n"
    "s_add_u32 m0, s56, 0xa500\n"
    "s_add_u32 s57, s57, 0x24000\n"
    "ds_read_b64_tr_b16 v[140:141], v195 offset:4160\n"
    "ds_read_b64_tr_b16 v[142:143], v195 offset:4416\n"
    "v_mfma_f32_32x32x16_bf16 v[32:47], v[148:151], v[152:155], v[32:47]\n"
    "buffer_load_dwordx4 v198, s[20:23], s57 offen lds\n"
    "ds_read_b128 v[172:175], v203 offset:46080\n"
    "s_waitcnt lgkmcnt(6)\n"
    "v_mfma_f32_32x32x16_bf16 v[0:15], v[128:131], v[160:163], v[0:15]\n"
    "s_add_u32 m0, s56, 0xb580\n"
    "s_add_u32 s57, s57, 0x24000\n"
    "ds_read_b64_tr_b16 v[144:145], v195 offset:8320\n"
    "ds_read_b64_tr_b16 v[146:147], v195 offset:8576\n"
    "v_mfma_f32_32x32x16_bf16 v[16:31], v[128:131], v[164:167], v[16:31]\n"
    "buffer_load_dwordx4 v198, s[20:23], s57 offen lds\n"
    "ds_read_b128 v[176:179], v204 offset:41984\n"
    "v_mfma_f32_32x32x16_bf16 v[48:63], v[132:135], v[164:167], v[48:63]\n"
    "ds_read_b64_tr_b16 v[148:149], v195 offset:8384\n"
    "ds_read_b64_tr_b16 v[150:151], v195 offset:8640\n"
    "v_mfma_f32_32x32x16_bf16 v[32:47], v[132:135], v[160:163], v[32:47]\n"
    "ds_read_b128 v[180:183], v204 offset:46080\n"
    "s_waitcnt vmcnt(4)\n"
    "s_barrier\n"
    "s_waitcnt lgkmcnt(6)\n"
    "v_mfma_f32_32x32x16_bf16 v[0:15], v[136:139], v[168:171], v[0:15]\n"
    "s_add_u32 m0, s56, 0xc600\n"
    "s_add_u32 s57, s57, 0x24000\n"
    "s_xor_b32 s1, s0, 4\n"
    "s_xor_b32 s100, s0, 3\n"
    "s_sub_u32 s1, s1, s100\n"
    "s_sub_u32 s1, s1, 1\n"
    "s_mul_i32 s1, s1, 589824\n"
    "s_add_u32 s57, s57, s1\n"
    "ds_read_b64_tr_b16 v[128:129], v195 offset:12544\n"
    "ds_read_b64_tr_b16 v[130:131], v195 offset:12800\n"
    "v_mfma_f32_32x32x16_bf16 v[16:31], v[136:139], v[172:175], v[16:31]\n"
    "buffer_load_dwordx4 v198, s[20:23], s57 offen lds\n"
    "v_mfma_f32_32x32x16_bf16 v[48:63], v[140:143], v[172:175], v[48:63]\n"
    "s_add_u32 m0, s56, 0xd680\n"
    "s_add_u32 s57, s57, 0x24000\n"
    "ds_read_b64_tr_b16 v[132:133], v195 offset:12608\n"
    "v_mfma_f32_32x32x16_bf16 v[32:47], v[140:143], v[168:171], v[32:47]\n"
    "buffer_load_dwordx4 v198, s[20:23], s57 offen lds\n"
    "ds_read_b64_tr_b16 v[134:135], v195 offset:12864\n"
    "s_waitcnt lgkmcnt(4)\n"
    "v_mfma_f32_32x32x16_bf16 v[0:15], v[144:147], v[176:179], v[0:15]\n"
    "s_add_u32 m0, s56, 0xe700\n"
    "s_add_u32 s57, s57, 0x24000\n"
    "ds_read_b64_tr_b16 v[136:137], v195 offset:16768\n"
    "ds_read_b64_tr_b16 v[138:139], v195 offset:17024\n"
    "v_mfma_f32_32x32x16_bf16 v[16:31], v[144:147], v[180:183], v[16:31]\n"
    "buffer_load_dwordx4 v198, s[20:23], s57 offen lds\n"
    "v_mfma_f32_32x32x16_bf16 v[48:63], v[148:151], v[180:183], v[48:63]\n"
    "ds_read_b64_tr_b16 v[140:141], v195 offset:16832\n"
    "v_mfma_f32_32x32x16_bf16 v[32:47], v[148:151], v[176:179], v[32:47]\n"
    "ds_read_b64_tr_b16 v[142:143], v195 offset:17088\n"
    "s_waitcnt lgkmcnt(4)\n"
    "v_mfma_f32_32x32x16_bf16 v[64:79], v[128:131], a[0:3], 0\n"
    "s_add_u32 m0, s56, 0xf780\n"
    "s_add_u32 s57, s57, 0x24000\n"
    "ds_read_b64_tr_b16 v[144:145], v195 offset:20992\n"
    "ds_read_b64_tr_b16 v[146:147], v195 offset:21248\n"
    "v_mfma_f32_32x32x16_bf16 v[80:95], v[128:131], a[4:7], 0\n"
    "buffer_load_dwordx4 v198, s[20:23], s57 offen lds\n"
    "v_mfma_f32_32x32x16_bf16 v[112:127], v[132:135], a[4:7], 0\n"
    "s_add_u32 m0, s56, 0x10800\n"
    "s_add_u32 s57, s57, 0x24000\n"
    "s_xor_b32 s1, s0, 5\n"
    "s_xor_b32 s100, s0, 4\n"
    "s_sub_u32 s1, s1, s100\n"
    "s_sub_u32 s1, s1, 1\n"
    "s_mul_i32 s1, s1, 589824\n"
    "s_add_u32 s57, s57, s1\n"
    "ds_read_b64_tr_b16 v[148:149], v195 offset:21056\n"
    "v_mfma_f32_32x32x16_bf16 v[96:111], v[132:135], a[0:3], 0\n"
    "buffer_load_dwordx4 v198, s[20:23], s57 offen lds\n"
    "ds_read_b64_tr_b16 v[150:151], v195 offset:21312\n"
    "s_lshr_b32 s15, s8, 3\n"
    "s_lshl_b32 s15, s15, 2\n"
    "s_add_u32 s15, s15, s9\n"
    "s_lshr_b32 s2, s15, 2\n"
    "s_lshl_b32 s2, s2, 1\n"
    "s_add_u32 s15, s2, s14\n"
    "s_mul_i32 s15, s15, 18\n"
    "s_waitcnt lgkmcnt(4)\n"
    "v_mfma_f32_32x32x16_bf16 v[64:79], v[136:139], a[8:11], v[64:79]\n"
    "s_add_u32 m0, s56, 0x11880\n"
    "s_add_u32 s57, s57, 0x24000\n"
    "ds_read_b64_tr_b16 v[128:129], v195 offset:25216\n"
    "ds_read_b64_tr_b16 v[130:131], v195 offset:25472\n"
    "v_add_u32 v249, 0, v192\n"
    "v_min_u32 v249, 17, v249\n"
    "v_add_u32 v249, s15, v249\n"
    "v_mfma_f32_32x32x16_bf16 v[80:95], v[136:139], a[12:15], v[80:95]\n"
    "buffer_load_dwordx4 v198, s[20:23], s57 offen lds\n"
    "v_mfma_f32_32x32x16_bf16 v[112:127], v[140:143], a[12:15], v[112:127]\n"
    "ds_read_b64_tr_b16 v[132:133], v195 offset:25280\n"
    "v_and_b32 v225, 1, v249\n"
    "v_lshrrev_b32 v249, 1, v249\n"
    "v_mul_u32_u24 v249, 9216, v249\n"
    "v_lshl_add_u32 v249, v225, 7, v249\n"
    "v_mfma_f32_32x32x16_bf16 v[96:111], v[140:143], a[8:11], v[96:111]\n"
    "s_mul_i32 s15, s11, 2304\n"
    "s_add_u32 s15, s15, 2048\n"
    "ds_read_b64_tr_b16 v[134:135], v195 offset:25536\n"
    "v_add_u32 v249, s15, v249\n"
    "s_waitcnt lgkmcnt(4)\n"
    "v_mfma_f32_32x32x16_bf16 v[64:79], v[144:147], a[16:19], v[64:79]\n"
    "buffer_load_dword v204, v249, s[20:23], 0 offen\n"
    "ds_read_b64_tr_b16 v[136:137], v193 offset:0\n"
    "ds_read_b64_tr_b16 v[138:139], v193 offset:256\n"
    "s_add_u32 m0, s56, 0x12900\n"
    "s_add_u32 s57, s57, 0x24000\n"
    "s_lshr_b32 s15, s8, 3\n"
    "s_lshl_b32 s15, s15, 2\n"
    "s_add_u32 s15, s15, s9\n"
    "s_mul_i32 s15, s15, 14\n"
    "v_mfma_f32_32x32x16_bf16 v[80:95], v[144:147], a[20:23], v[80:95]\n"
    "buffer_load_dwordx4 v198, s[20:23], s57 offen lds\n"
    "v_mfma_f32_32x32x16_bf16 v[112:127], v[148:151], a[20:23], v[112:127]\n"
    "s_add_u32 m0, s56, 0x13980\n"
    "s_add_u32 s57, s57, 0x24000\n"
    "ds_read_b64_tr_b16 v[140:141], v193 offset:64\n"
    "v_add_u32 v249, 0, v192\n"
    "v_min_u32 v249, 13, v249\n"
    "v_add_u32 v249, s15, v249\n"
    "v_mul_u32_u24 v225, 21846, v249\n"
    "v_mfma_f32_32x32x16_bf16 v[96:111], v[148:151], a[16:19], v[96:111]\n"
    "buffer_load_dwordx4 v198, s[20:23], s57 offen lds\n"
    "ds_read_b64_tr_b16 v[142:143], v193 offset:320\n"
    "s_waitcnt lgkmcnt(4)\n"
    "v_mfma_f32_32x32x16_bf16 v[64:79], v[128:131], a[24:27], v[64:79]\n"
    "s_add_u32 m0, s56, 0x1ff80\n"
    "s_add_u32 s57, s57, 0x24000\n"
    "s_xor_b32 s1, s0, 6\n"
    "s_xor_b32 s100, s0, 5\n"
    "s_sub_u32 s1, s1, s100\n"
    "s_sub_u32 s1, s1, 1\n"
    "s_mul_i32 s1, s1, 589824\n"
    "s_add_u32 s57, s57, s1\n"
    "ds_read_b64_tr_b16 v[144:145], v193 offset:4224\n"
    "ds_read_b64_tr_b16 v[146:147], v193 offset:4480\n"
    "v_mfma_f32_32x32x16_bf16 v[80:95], v[128:131], a[28:31], v[80:95]\n"
    "buffer_load_dwordx4 v198, s[20:23], s57 offen lds\n"
    "v_mfma_f32_32x32x16_bf16 v[112:127], v[132:135], a[28:31], v[112:127]\n"
    "ds_read_b64_tr_b16 v[148:149], v193 offset:4288\n"
    "v_lshrrev_b32 v225, 16, v225\n"
    "v_mul_u32_u24 v225, 768, v225\n"
    "v_lshl_add_u32 v249, v249, 7, v225\n"
    "v_mfma_f32_32x32x16_bf16 v[96:111], v[132:135], a[24:27], v[96:111]\n"
    "s_mul_i32 s15, s11, 288\n"
    "ds_read_b64_tr_b16 v[150:151], v193 offset:4544\n"
    "v_add_u32 v249, s15, v249\n"
    "s_waitcnt lgkmcnt(4)\n"
    "v_mfma_f32_32x32x16_bf16 v[64:79], v[136:139], a[32:35], v[64:79]\n"
    "buffer_load_dword v203, v249, s[76:79], 0 offen\n"
    "ds_read_b64_tr_b16 v[128:129], v193 offset:8448\n"
    "ds_read_b64_tr_b16 v[130:131], v193 offset:8704\n"
    "s_add_u32 m0, s56, 0x1ef00\n"
    "s_add_u32 s57, s57, 0x24000\n"
    "s_xor_b32 s86, s4, 0\n"
    "s_mul_i32 s86, s86, 8192\n"
    "s_add_u32 s86, s86, s6\n"
    "v_mfma_f32_32x32x16_bf16 v[80:95], v[136:139], a[36:39], v[80:95]\n"
    "buffer_load_dwordx4 v198, s[20:23], s57 offen lds\n"
    "v_mfma_f32_32x32x16_bf16 v[112:127], v[140:143], a[36:39], v[112:127]\n"
    "s_add_u32 m0, s56, 0x19c80\n"
    "s_add_u32 s57, s57, 0x24000\n"
    "ds_read_b64_tr_b16 v[132:133], v193 offset:8512\n"
    "v_mul_f32 v184, v0, v0\n"
    "v_mul_f32 v185, v1, v1\n"
    "v_mul_f32 v186, v2, v2\n"
    "v_mul_f32 v187, v3, v3\n"
    "v_mfma_f32_32x32x16_bf16 v[96:111], v[140:143], a[32:35], v[96:111]\n"
    "buffer_load_dwordx4 v198, s[20:23], s57 offen lds\n"
    "ds_read_b64_tr_b16 v[134:135], v193 offset:8768\n"
    "s_waitcnt lgkmcnt(4)\n"
    "v_mfma_f32_32x32x16_bf16 v[64:79], v[144:147], a[40:43], v[64:79]\n"
    "s_add_u32 m0, s56, 0x18c00\n"
    "s_add_u32 s57, s57, 0x24000\n"
    "ds_read_b64_tr_b16 v[136:137], v193 offset:12672\n"
    "ds_read_b64_tr_b16 v[138:139], v193 offset:12928\n"
    "v_fmaak_f32 v184, v205, v184, 0xc0135761\n"
    "v_fmaak_f32 v185, v205, v185, 0xc0135761\n"
    "v_fmaak_f32 v186, v205, v186, 0xc0135761\n"
    "v_mfma_f32_32x32x16_bf16 v[80:95], v[144:147], a[44:47], v[80:95]\n"
    "buffer_load_dwordx4 v198, s[20:23], s57 offen lds\n"
    "v_mfma_f32_32x32x16_bf16 v[112:127], v[148:151], a[44:47], v[112:127]\n"
    "ds_read_b64_tr_b16 v[140:141], v193 offset:12736\n"
    "v_fmaak_f32 v187, v205, v187, 0xc0135761\n"
    "v_mul_f32 v184, v0, v184\n"
    "v_mul_f32 v185, v1, v185\n"
    "v_mul_f32 v186, v2, v186\n"
    "v_mul_f32 v187, v3, v187\n"
    "v_mfma_f32_32x32x16_bf16 v[96:111], v[148:151], a[40:43], v[96:111]\n"
    "ds_read_b64_tr_b16 v[142:143], v193 offset:12992\n"
    "v_exp_f32 v184, v184\n"
    "v_exp_f32 v185, v185\n"
    "s_waitcnt lgkmcnt(4)\n"
    "v_mfma_f32_32x32x16_bf16 v[64:79], v[128:131], a[48:51], v[64:79]\n"
    "s_add_u32 m0, s56, 0x17b80\n"
    "s_add_u32 s57, s57, 0x24000\n"
    "s_xor_b32 s1, s0, 7\n"
    "s_xor_b32 s100, s0, 6\n"
    "s_sub_u32 s1, s1, s100\n"
    "s_sub_u32 s1, s1, 1\n"
    "s_mul_i32 s1, s1, 589824\n"
    "s_add_u32 s57, s57, s1\n"
    "ds_read_b64_tr_b16 v[144:145], v193 offset:16896\n"
    "ds_read_b64_tr_b16 v[146:147], v193 offset:17152\n"
    "v_mfma_f32_32x32x16_bf16 v[80:95], v[128:131], a[52:55], v[80:95]\n"
    "buffer_load_dwordx4 v198, s[20:23], s57 offen lds\n"
    "v_mfma_f32_32x32x16_bf16 v[112:127], v[132:135], a[52:55], v[112:127]\n"
    "s_add_u32 m0, s56, 0x16b00\n"
    "s_add_u32 s57, s57, 0x24000\n"
    "ds_read_b64_tr_b16 v[148:149], v193 offset:16960\n"
    "v_exp_f32 v186, v186\n"
    "v_exp_f32 v187, v187\n"
    "v_mfma_f32_32x32x16_bf16 v[96:111], v[132:135], a[48:51], v[96:111]\n"
    "buffer_load_dwordx4 v198, s[20:23], s57 offen lds\n"
    "ds_read_b64_tr_b16 v[150:151], v193 offset:17216\n"
    "s_waitcnt lgkmcnt(4)\n"
    "v_mfma_f32_32x32x16_bf16 v[64:79], v[136:139], a[56:59], v[64:79]\n"
    "s_add_u32 m0, s56, 0x14a00\n"
    "s_add_u32 s57, s57, 0x24000\n"
    "ds_read_b64_tr_b16 v[128:129], v193 offset:21120\n"
    "ds_read_b64_tr_b16 v[130:131], v193 offset:21376\n"
    "v_mul_f32 v4, v0, v4\n"
    "v_mul_f32 v5, v1, v5\n"
    "v_mul_f32 v6, v2, v6\n"
    "v_mfma_f32_32x32x16_bf16 v[80:95], v[136:139], a[60:63], v[80:95]\n"
    "buffer_load_dwordx4 v198, s[20:23], s57 offen lds\n"
    "v_mfma_f32_32x32x16_bf16 v[112:127], v[140:143], a[60:63], v[112:127]\n"
    "ds_read_b64_tr_b16 v[132:133], v193 offset:21184\n"
    "v_mul_f32 v7, v3, v7\n"
    "v_add_f32 v184, 1.0, v184\n"
    "v_add_f32 v185, 1.0, v185\n"
    "v_add_f32 v186, 1.0, v186\n"
    "v_add_f32 v187, 1.0, v187\n"
    "v_mfma_f32_32x32x16_bf16 v[96:111], v[140:143], a[56:59], v[96:111]\n"
    "ds_read_b64_tr_b16 v[134:135], v193 offset:21440\n"
    "v_rcp_f32 v184, v184\n"
    "v_rcp_f32 v185, v185\n"
    "s_waitcnt lgkmcnt(4)\n"
    "v_mfma_f32_32x32x16_bf16 v[64:79], v[144:147], a[64:67], v[64:79]\n"
    "s_add_u32 m0, s56, 0x15a80\n"
    "s_add_u32 s57, s57, 0x24000\n"
    "ds_read_b64_tr_b16 v[136:137], v193 offset:25344\n"
    "ds_read_b64_tr_b16 v[138:139], v193 offset:25600\n"
    "v_rcp_f32 v186, v186\n"
    "v_mfma_f32_32x32x16_bf16 v[80:95], v[144:147], a[68:71], v[80:95]\n"
    "buffer_load_dwordx4 v198, s[20:23], s57 offen lds\n"
    "v_mfma_f32_32x32x16_bf16 v[112:127], v[148:151], a[68:71], v[112:127]\n"
    "ds_read_b64_tr_b16 v[140:141], v193 offset:25408\n"
    "v_rcp_f32 v187, v187\n"
    "v_mul_f32 v4, v4, v184\n"
    "v_mul_f32 v5, v5, v185\n"
    "v_mul_f32 v6, v6, v186\n"
    "v_mfma_f32_32x32x16_bf16 v[96:111], v[148:151], a[64:67], v[96:111]\n"
    "ds_read_b64_tr_b16 v[142:143], v193 offset:25664\n"
    "v_mul_f32 v7, v7, v187\n"
    "v_cvt_pk_bf16_f32 v0, v4, v5\n"
    "v_cvt_pk_bf16_f32 v1, v6, v7\n"
    "v_mul_f32 v188, v8, v8\n"
    "s_waitcnt lgkmcnt(4)\n"
    "v_mfma_f32_32x32x16_bf16 v[64:79], v[128:131], a[72:75], v[64:79]\n"
    "ds_read_b64_tr_b16 v[144:145], v193 offset:29568\n"
    "ds_read_b64_tr_b16 v[146:147], v193 offset:29824\n"
    "v_mul_f32 v189, v9, v9\n"
    "v_mul_f32 v190, v10, v10\n"
    "v_mul_f32 v191, v11, v11\n"
    "v_fmaak_f32 v188, v205, v188, 0xc0135761\n"
    "v_mfma_f32_32x32x16_bf16 v[80:95], v[128:131], a[76:79], v[80:95]\n"
    "v_fmaak_f32 v189, v205, v189, 0xc0135761\n"
    "v_fmaak_f32 v190, v205, v190, 0xc0135761\n"
    "v_fmaak_f32 v191, v205, v191, 0xc0135761\n"
    "v_mul_f32 v188, v8, v188\n"
    "v_mul_f32 v189, v9, v189\n"
    "v_mul_f32 v190, v10, v190\n"
    "v_mfma_f32_32x32x16_bf16 v[112:127], v[132:135], a[76:79], v[112:127]\n"
    "ds_read_b64_tr_b16 v[148:149], v193 offset:29632\n"
    "v_mul_f32 v191, v11, v191\n"
    "v_exp_f32 v188, v188\n"
    "v_exp_f32 v189, v189\n"
    "v_mfma_f32_32x32x16_bf16 v[96:111], v[132:135], a[72:75], v[96:111]\n"
    "ds_read_b64_tr_b16 v[150:151], v193 offset:29888\n"
    "s_waitcnt vmcnt(9)\n"
    "s_barrier\n"
    "s_waitcnt lgkmcnt(4)\n"
    "v_mfma_f32_32x32x16_bf16 v[64:79], v[136:139], a[80:83], v[64:79]\n"
    "s_add_u32 m0, s56, 0x0\n"
    "s_add_u32 s57, s57, 0x24000\n"
    "s_mov_b32 s1, 8\n"
    "s_xor_b32 s100, s0, 7\n"
    "s_sub_u32 s1, s1, s100\n"
    "s_sub_u32 s1, s1, 1\n"
    "s_mul_i32 s1, s1, 589824\n"
    "s_add_u32 s57, s57, s1\n"
    "ds_read_b64_tr_b16 v[128:129], v193 offset:33792\n"
    "ds_read_b64_tr_b16 v[130:131], v193 offset:34048\n"
    "v_mfma_f32_32x32x16_bf16 v[80:95], v[136:139], a[84:87], v[80:95]\n"
    "buffer_load_dwordx4 v198, s[20:23], s57 offen lds\n"
    "v_mfma_f32_32x32x16_bf16 v[112:127], v[140:143], a[84:87], v[112:127]\n"
    "s_add_u32 m0, s56, 0x1080\n"
    "s_add_u32 s57, s57, 0x24000\n"
    "ds_read_b64_tr_b16 v[132:133], v193 offset:33856\n"
    "v_exp_f32 v190, v190\n"
    "v_exp_f32 v191, v191\n"
    "v_mfma_f32_32x32x16_bf16 v[96:111], v[140:143], a[80:83], v[96:111]\n"
    "buffer_load_dwordx4 v198, s[20:23], s57 offen lds\n"
    "ds_read_b64_tr_b16 v[134:135], v193 offset:34112\n"
    "s_waitcnt lgkmcnt(4)\n"
    "v_mfma_f32_32x32x16_bf16 v[64:79], v[144:147], a[88:91], v[64:79]\n"
    "s_add_u32 m0, s56, 0x2100\n"
    "s_add_u32 s57, s57, 0x24000\n"
    "ds_read_b64_tr_b16 v[136:137], v193 offset:38016\n"
    "ds_read_b64_tr_b16 v[138:139], v193 offset:38272\n"
    "v_mul_f32 v12, v8, v12\n"
    "v_mul_f32 v13, v9, v13\n"
    "v_mul_f32 v14, v10, v14\n"
    "v_mfma_f32_32x32x16_bf16 v[80:95], v[144:147], a[92:95], v[80:95]\n"
    "buffer_load_dwordx4 v198, s[20:23], s57 offen lds\n"
    "v_mfma_f32_32x32x16_bf16 v[112:127], v[148:151], a[92:95], v[112:127]\n"
    "ds_read_b64_tr_b16 v[140:141], v193 offset:38080\n"
    "v_mul_f32 v15, v11, v15\n"
    "v_add_f32 v188, 1.0, v188\n"
    "v_add_f32 v189, 1.0, v189\n"
    "v_add_f32 v190, 1.0, v190\n"
    "v_add_f32 v191, 1.0, v191\n"
    "v_mfma_f32_32x32x16_bf16 v[96:111], v[148:151], a[88:91], v[96:111]\n"
    "ds_read_b64_tr_b16 v[142:143], v193 offset:38336\n"
    "v_rcp_f32 v188, v188\n"
    "v_rcp_f32 v189, v189\n"
    "s_waitcnt lgkmcnt(4)\n"
    "v_mfma_f32_32x32x16_bf16 v[64:79], v[128:131], a[96:99], v[64:79]\n"
    "s_add_u32 m0, s56, 0x3180\n"
    "s_add_u32 s57, s57, 0x24000\n"
    "ds_read_b64_tr_b16 v[144:145], v193 offset:42240\n"
    "ds_read_b64_tr_b16 v[146:147], v193 offset:42496\n"
    "v_rcp_f32 v190, v190\n"
    "v_mfma_f32_32x32x16_bf16 v[80:95], v[128:131], a[100:103], v[80:95]\n"
    "buffer_load_dwordx4 v198, s[20:23], s57 offen lds\n"
    "v_mfma_f32_32x32x16_bf16 v[112:127], v[132:135], a[100:103], v[112:127]\n"
    "ds_read_b64_tr_b16 v[148:149], v193 offset:42304\n"
    "v_rcp_f32 v191, v191\n"
    "v_mul_f32 v12, v12, v188\n"
    "v_mul_f32 v13, v13, v189\n"
    "v_mul_f32 v14, v14, v190\n"
    "v_mfma_f32_32x32x16_bf16 v[96:111], v[132:135], a[96:99], v[96:111]\n"
    "ds_read_b64_tr_b16 v[150:151], v193 offset:42560\n"
    "v_mul_f32 v15, v15, v191\n"
    "v_cvt_pk_bf16_f32 v2, v12, v13\n"
    "v_cvt_pk_bf16_f32 v3, v14, v15\n"
    "v_mul_f32 v184, v16, v16\n"
    "s_waitcnt lgkmcnt(4)\n"
    "v_mfma_f32_32x32x16_bf16 v[64:79], v[136:139], a[104:107], v[64:79]\n"
    "buffer_store_dwordx4 v[0:3], v210, s[24:27], s86 offen offset:0 sc1\n"
    "ds_read_b64_tr_b16 v[128:129], v193 offset:46464\n"
    "ds_read_b64_tr_b16 v[130:131], v193 offset:46720\n"
    "s_add_u32 m0, s56, 0x4200\n"
    "v_mfma_f32_32x32x16_bf16 v[80:95], v[136:139], a[108:111], v[80:95]\n"
    "global_load_lds_dwordx4 v[206:207], off\n"
    "v_lshl_add_u64 v[206:207], v[208:209], 0, v[206:207]\n"
    "v_mfma_f32_32x32x16_bf16 v[112:127], v[140:143], a[108:111], v[112:127]\n"
    "ds_read_b64_tr_b16 v[132:133], v193 offset:46528\n"
    "v_mul_f32 v185, v17, v17\n"
    "v_mul_f32 v186, v18, v18\n"
    "v_mul_f32 v187, v19, v19\n"
    "v_fmaak_f32 v184, v205, v184, 0xc0135761\n"
    "v_fmaak_f32 v185, v205, v185, 0xc0135761\n"
    "v_mfma_f32_32x32x16_bf16 v[96:111], v[140:143], a[104:107], v[96:111]\n"
    "ds_read_b64_tr_b16 v[134:135], v193 offset:46784\n"
    "v_fmaak_f32 v186, v205, v186, 0xc0135761\n"
    "v_fmaak_f32 v187, v205, v187, 0xc0135761\n"
    "v_mul_f32 v184, v16, v184\n"
    "v_mul_f32 v185, v17, v185\n"
    "s_waitcnt lgkmcnt(4)\n"
    "v_mfma_f32_32x32x16_bf16 v[64:79], v[144:147], a[112:115], v[64:79]\n"
    "s_add_u32 m0, s56, 0x5280\n"
    "ds_read_b64_tr_b16 v[136:137], v193 offset:50688\n"
    "ds_read_b64_tr_b16 v[138:139], v193 offset:50944\n"
    "v_mul_f32 v186, v18, v186\n"
    "v_mul_f32 v187, v19, v187\n"
    "v_exp_f32 v184, v184\n"
    "v_mfma_f32_32x32x16_bf16 v[80:95], v[144:147], a[116:119], v[80:95]\n"
    "global_load_lds_dwordx4 v[206:207], off\n"
    "v_lshl_add_u64 v[206:207], v[208:209], 0, v[206:207]\n"
    "v_mfma_f32_32x32x16_bf16 v[112:127], v[148:151], a[116:119], v[112:127]\n"
    "ds_read_b64_tr_b16 v[140:141], v193 offset:50752\n"
    "v_exp_f32 v185, v185\n"
    "v_exp_f32 v186, v186\n"
    "v_mfma_f32_32x32x16_bf16 v[96:111], v[148:151], a[112:115], v[96:111]\n"
    "ds_read_b64_tr_b16 v[142:143], v193 offset:51008\n"
    "v_exp_f32 v187, v187\n"
    "v_mul_f32 v20, v16, v20\n"
    "v_mul_f32 v21, v17, v21\n"
    "s_waitcnt lgkmcnt(4)\n"
    "v_mfma_f32_32x32x16_bf16 v[64:79], v[128:131], a[120:123], v[64:79]\n"
    "s_add_u32 m0, s56, 0x1ad00\n"
    "ds_read_b64_tr_b16 v[144:145], v193 offset:54912\n"
    "ds_read_b64_tr_b16 v[146:147], v193 offset:55168\n"
    "v_mul_f32 v22, v18, v22\n"
    "v_mul_f32 v23, v19, v23\n"
    "v_add_f32 v184, 1.0, v184\n"
    "v_add_f32 v185, 1.0, v185\n"
    "v_mfma_f32_32x32x16_bf16 v[80:95], v[128:131], a[124:127], v[80:95]\n"
    "global_load_lds_dwordx4 v[206:207], off\n"
    "v_lshl_add_u64 v[206:207], v[208:209], 0, v[206:207]\n"
    "v_mfma_f32_32x32x16_bf16 v[112:127], v[132:135], a[124:127], v[112:127]\n"
    "ds_read_b64_tr_b16 v[148:149], v193 offset:54976\n"
    "v_add_f32 v186, 1.0, v186\n"
    "v_add_f32 v187, 1.0, v187\n"
    "v_rcp_f32 v184, v184\n"
    "v_mfma_f32_32x32x16_bf16 v[96:111], v[132:135], a[120:123], v[96:111]\n"
    "ds_read_b64_tr_b16 v[150:151], v193 offset:55232\n"
    "v_rcp_f32 v185, v185\n"
    "v_rcp_f32 v186, v186\n"
    "s_waitcnt lgkmcnt(4)\n"
    "v_mfma_f32_32x32x16_bf16 v[64:79], v[136:139], a[128:131], v[64:79]\n"
    "s_add_u32 m0, s56, 0x1bd80\n"
    "ds_read_b64_tr_b16 v[128:129], v193 offset:59136\n"
    "ds_read_b64_tr_b16 v[130:131], v193 offset:59392\n"
    "v_rcp_f32 v187, v187\n"
    "v_mul_f32 v20, v20, v184\n"
    "v_mul_f32 v21, v21, v185\n"
    "v_mfma_f32_32x32x16_bf16 v[80:95], v[136:139], a[132:135], v[80:95]\n"
    "global_load_lds_dwordx4 v[206:207], off\n"
    "v_lshl_add_u64 v[206:207], v[208:209], 0, v[206:207]\n"
    "s_xor_b32 s1, s0, 1\n"
    "s_xor_b32 s100, s0, 0\n"
    "s_sub_u32 s1, s1, s100\n"
    "s_sub_u32 s1, s1, 1\n"
    "s_lshl_b32 s1, s1, 2\n"
    "v_mul_i32_i24 v214, s1, v208\n"
    "v_ashrrev_i32 v215, 31, v214\n"
    "v_lshl_add_u64 v[206:207], v[214:215], 0, v[206:207]\n"
    "v_mfma_f32_32x32x16_bf16 v[112:127], v[140:143], a[132:135], v[112:127]\n"
    "ds_read_b64_tr_b16 v[132:133], v193 offset:59200\n"
    "v_mul_f32 v22, v22, v186\n"
    "v_mul_f32 v23, v23, v187\n"
    "v_cvt_pk_bf16_f32 v16, v20, v21\n"
    "v_cvt_pk_bf16_f32 v17, v22, v23\n"
    "v_mul_f32 v188, v24, v24\n"
    "v_mfma_f32_32x32x16_bf16 v[96:111], v[140:143], a[128:131], v[96:111]\n"
    "ds_read_b64_tr_b16 v[134:135], v193 offset:59456\n"
    "v_mul_f32 v189, v25, v25\n"
    "v_mul_f32 v190, v26, v26\n"
    "v_mul_f32 v191, v27, v27\n"
    "v_fmaak_f32 v188, v205, v188, 0xc0135761\n"
    "s_waitcnt lgkmcnt(4)\n"
    "v_mfma_f32_32x32x16_bf16 v[64:79], v[144:147], a[136:139], v[64:79]\n"
    "s_add_u32 m0, s56, 0x1ce00\n"
    "ds_read_b64_tr_b16 v[136:137], v193 offset:63360\n"
    "ds_read_b64_tr_b16 v[138:139], v193 offset:63616\n"
    "v_fmaak_f32 v189, v205, v189, 0xc0135761\n"
    "v_fmaak_f32 v190, v205, v190, 0xc0135761\n"
    "v_fmaak_f32 v191, v205, v191, 0xc0135761\n"
    "v_mul_f32 v188, v24, v188\n"
    "v_mfma_f32_32x32x16_bf16 v[80:95], v[144:147], a[140:143], v[80:95]\n"
    "global_load_lds_dwordx4 v[206:207], off\n"
    "v_lshl_add_u64 v[206:207], v[208:209], 0, v[206:207]\n"
    "v_mfma_f32_32x32x16_bf16 v[112:127], v[148:151], a[140:143], v[112:127]\n"
    "ds_read_b64_tr_b16 v[140:141], v193 offset:63424\n"
    "v_mul_f32 v189, v25, v189\n"
    "v_mul_f32 v190, v26, v190\n"
    "v_mul_f32 v191, v27, v191\n"
    "v_exp_f32 v188, v188\n"
    "v_mfma_f32_32x32x16_bf16 v[96:111], v[148:151], a[136:139], v[96:111]\n"
    "ds_read_b64_tr_b16 v[142:143], v193 offset:63680\n"
    "v_exp_f32 v189, v189\n"
    "v_exp_f32 v190, v190\n"
    "s_waitcnt lgkmcnt(4)\n"
    "v_mfma_f32_32x32x16_bf16 v[64:79], v[128:131], a[144:147], v[64:79]\n"
    "s_add_u32 m0, s56, 0x1de80\n"
    "ds_read_b64_tr_b16 v[144:145], v194 offset:2048\n"
    "ds_read_b64_tr_b16 v[146:147], v194 offset:2304\n"
    "v_exp_f32 v191, v191\n"
    "v_mul_f32 v28, v24, v28\n"
    "v_mul_f32 v29, v25, v29\n"
    "v_mfma_f32_32x32x16_bf16 v[80:95], v[128:131], a[148:151], v[80:95]\n"
    "global_load_lds_dwordx4 v[206:207], off\n"
    "v_lshl_add_u64 v[206:207], v[208:209], 0, v[206:207]\n"
    "v_mfma_f32_32x32x16_bf16 v[112:127], v[132:135], a[148:151], v[112:127]\n"
    "ds_read_b64_tr_b16 v[148:149], v194 offset:2112\n"
    "v_mul_f32 v30, v26, v30\n"
    "v_mul_f32 v31, v27, v31\n"
    "v_add_f32 v188, 1.0, v188\n"
    "v_add_f32 v189, 1.0, v189\n"
    "v_add_f32 v190, 1.0, v190\n"
    "v_mfma_f32_32x32x16_bf16 v[96:111], v[132:135], a[144:147], v[96:111]\n"
    "ds_read_b64_tr_b16 v[150:151], v194 offset:2368\n"
    "v_add_f32 v191, 1.0, v191\n"
    "v_rcp_f32 v188, v188\n"
    "s_waitcnt lgkmcnt(4)\n"
    "v_mfma_f32_32x32x16_bf16 v[64:79], v[136:139], a[152:155], v[64:79]\n"
    "s_add_u32 m0, s56, 0x21000\n"
    "ds_read_b64_tr_b16 v[128:129], v194 offset:6272\n"
    "ds_read_b64_tr_b16 v[130:131], v194 offset:6528\n"
    "v_rcp_f32 v189, v189\n"
    "v_rcp_f32 v190, v190\n"
    "v_mfma_f32_32x32x16_bf16 v[80:95], v[136:139], a[156:159], v[80:95]\n"
    "global_load_lds_dwordx4 v[206:207], off\n"
    "v_lshl_add_u64 v[206:207], v[208:209], 0, v[206:207]\n"
    "v_mfma_f32_32x32x16_bf16 v[112:127], v[140:143], a[156:159], v[112:127]\n"
    "ds_read_b64_tr_b16 v[132:133], v194 offset:6336\n"
    "v_rcp_f32 v191, v191\n"
    "v_mul_f32 v28, v28, v188\n"
    "v_mul_f32 v29, v29, v189\n"
    "v_mul_f32 v30, v30, v190\n"
    "v_mfma_f32_32x32x16_bf16 v[96:111], v[140:143], a[152:155], v[96:111]\n"
    "ds_read_b64_tr_b16 v[134:135], v194 offset:6592\n"
    "v_mul_f32 v31, v31, v191\n"
    "v_cvt_pk_bf16_f32 v18, v28, v29\n"
    "v_cvt_pk_bf16_f32 v19, v30, v31\n"
    "v_mul_f32 v184, v32, v32\n"
    "s_waitcnt lgkmcnt(4)\n"
    "v_mfma_f32_32x32x16_bf16 v[64:79], v[144:147], a[160:163], v[64:79]\n"
    "buffer_store_dwordx4 v[16:19], v211, s[24:27], s86 offen offset:0 sc1\n"
    "ds_read_b64_tr_b16 v[136:137], v194 offset:10496\n"
    "ds_read_b64_tr_b16 v[138:139], v194 offset:10752\n"
    "s_add_u32 m0, s56, 0x22080\n"
    "v_mfma_f32_32x32x16_bf16 v[80:95], v[144:147], a[164:167], v[80:95]\n"
    "global_load_lds_dwordx4 v[206:207], off\n"
    "v_lshl_add_u64 v[206:207], v[208:209], 0, v[206:207]\n"
    "s_xor_b32 s1, s0, 2\n"
    "s_xor_b32 s100, s0, 1\n"
    "s_sub_u32 s1, s1, s100\n"
    "s_sub_u32 s1, s1, 1\n"
    "s_lshl_b32 s1, s1, 2\n"
    "v_mul_i32_i24 v214, s1, v208\n"
    "v_ashrrev_i32 v215, 31, v214\n"
    "v_lshl_add_u64 v[206:207], v[214:215], 0, v[206:207]\n"
    "v_mfma_f32_32x32x16_bf16 v[112:127], v[148:151], a[164:167], v[112:127]\n"
    "ds_read_b64_tr_b16 v[140:141], v194 offset:10560\n"
    "v_mul_f32 v185, v33, v33\n"
    "v_mul_f32 v186, v34, v34\n"
    "v_mul_f32 v187, v35, v35\n"
    "v_fmaak_f32 v184, v205, v184, 0xc0135761\n"
    "v_mfma_f32_32x32x16_bf16 v[96:111], v[148:151], a[160:163], v[96:111]\n"
    "ds_read_b64_tr_b16 v[142:143], v194 offset:10816\n"
    "v_fmaak_f32 v185, v205, v185, 0xc0135761\n"
    "v_fmaak_f32 v186, v205, v186, 0xc0135761\n"
    "v_fmaak_f32 v187, v205, v187, 0xc0135761\n"
    "v_mul_f32 v184, v32, v184\n"
    "s_waitcnt lgkmcnt(4)\n"
    "v_mfma_f32_32x32x16_bf16 v[64:79], v[128:131], a[168:171], v[64:79]\n"
    "s_add_u32 m0, s56, 0x23100\n"
    "ds_read_b64_tr_b16 v[144:145], v194 offset:14720\n"
    "ds_read_b64_tr_b16 v[146:147], v194 offset:14976\n"
    "v_mul_f32 v185, v33, v185\n"
    "v_mul_f32 v186, v34, v186\n"
    "v_mul_f32 v187, v35, v187\n"
    "v_mfma_f32_32x32x16_bf16 v[80:95], v[128:131], a[172:175], v[80:95]\n"
    "global_load_lds_dwordx4 v[206:207], off\n"
    "v_lshl_add_u64 v[206:207], v[208:209], 0, v[206:207]\n"
    "v_mfma_f32_32x32x16_bf16 v[112:127], v[132:135], a[172:175], v[112:127]\n"
    "ds_read_b64_tr_b16 v[148:149], v194 offset:14784\n"
    "v_exp_f32 v184, v184\n"
    "v_exp_f32 v185, v185\n"
    "v_mfma_f32_32x32x16_bf16 v[96:111], v[132:135], a[168:171], v[96:111]\n"
    "ds_read_b64_tr_b16 v[150:151], v194 offset:15040\n"
    "s_waitcnt vmcnt(11)\n"
    "s_barrier\n"
    "s_waitcnt lgkmcnt(4)\n"
    "v_mfma_f32_32x32x16_bf16 v[64:79], v[136:139], a[176:179], v[64:79]\n"
    "s_add_u32 m0, s56, 0x6300\n"
    "ds_read_b64_tr_b16 v[128:129], v194 offset:65408\n"
    "ds_read_b64_tr_b16 v[130:131], v195 offset:128\n"
    "v_exp_f32 v186, v186\n"
    "v_exp_f32 v187, v187\n"
    "v_mfma_f32_32x32x16_bf16 v[80:95], v[136:139], a[180:183], v[80:95]\n"
    "global_load_lds_dwordx4 v[206:207], off\n"
    "v_lshl_add_u64 v[206:207], v[208:209], 0, v[206:207]\n"
    "v_mfma_f32_32x32x16_bf16 v[112:127], v[140:143], a[180:183], v[112:127]\n"
    "ds_read_b64_tr_b16 v[132:133], v194 offset:65472\n"
    "v_mul_f32 v36, v32, v36\n"
    "v_mul_f32 v37, v33, v37\n"
    "v_mul_f32 v38, v34, v38\n"
    "v_mul_f32 v39, v35, v39\n"
    "v_add_f32 v184, 1.0, v184\n"
    "v_mfma_f32_32x32x16_bf16 v[96:111], v[140:143], a[176:179], v[96:111]\n"
    "ds_read_b64_tr_b16 v[134:135], v195 offset:192\n"
    "v_add_f32 v185, 1.0, v185\n"
    "v_add_f32 v186, 1.0, v186\n"
    "v_add_f32 v187, 1.0, v187\n"
    "s_waitcnt lgkmcnt(4)\n"
    "v_mfma_f32_32x32x16_bf16 v[64:79], v[144:147], a[184:187], v[64:79]\n"
    "s_add_u32 m0, s56, 0x7380\n"
    "ds_read_b64_tr_b16 v[136:137], v194 offset:61184\n"
    "ds_read_b64_tr_b16 v[138:139], v194 offset:61440\n"
    "v_rcp_f32 v184, v184\n"
    "v_rcp_f32 v185, v185\n"
    "v_mfma_f32_32x32x16_bf16 v[80:95], v[144:147], a[188:191], v[80:95]\n"
    "global_load_lds_dwordx4 v[206:207], off\n"
    "v_lshl_add_u64 v[206:207], v[208:209], 0, v[206:207]\n"
    "v_mfma_f32_32x32x16_bf16 v[112:127], v[148:151], a[188:191], v[112:127]\n"
    "ds_read_b64_tr_b16 v[140:141], v194 offset:61248\n"
    "v_rcp_f32 v186, v186\n"
    "v_rcp_f32 v187, v187\n"
    "v_mul_f32 v36, v36, v184\n"
    "v_mfma_f32_32x32x16_bf16 v[96:111], v[148:151], a[184:187], v[96:111]\n"
    "ds_read_b64_tr_b16 v[142:143], v194 offset:61504\n"
    "v_mul_f32 v37, v37, v185\n"
    "v_mul_f32 v38, v38, v186\n"
    "v_mul_f32 v39, v39, v187\n"
    "v_cvt_pk_bf16_f32 v32, v36, v37\n"
    "s_waitcnt lgkmcnt(4)\n"
    "v_mfma_f32_32x32x16_bf16 v[64:79], v[128:131], a[192:195], v[64:79]\n"
    "s_add_u32 m0, s56, 0x8400\n"
    "ds_read_b64_tr_b16 v[144:145], v194 offset:40064\n"
    "ds_read_b64_tr_b16 v[146:147], v194 offset:40320\n"
    "v_cvt_pk_bf16_f32 v33, v38, v39\n"
    "v_mul_f32 v188, v40, v40\n"
    "v_mul_f32 v189, v41, v41\n"
    "v_mul_f32 v190, v42, v42\n"
    "v_mfma_f32_32x32x16_bf16 v[80:95], v[128:131], a[196:199], v[80:95]\n"
    "global_load_lds_dwordx4 v[206:207], off\n"
    "v_lshl_add_u64 v[206:207], v[208:209], 0, v[206:207]\n"
    "s_xor_b32 s1, s0, 3\n"
    "s_xor_b32 s100, s0, 2\n"
    "s_sub_u32 s1, s1, s100\n"
    "s_sub_u32 s1, s1, 1\n"
    "s_lshl_b32 s1, s1, 2\n"
    "v_mul_i32_i24 v214, s1, v208\n"
    "v_ashrrev_i32 v215, 31, v214\n"
    "v_lshl_add_u64 v[206:207], v[214:215], 0, v[206:207]\n"
    "v_mfma_f32_32x32x16_bf16 v[112:127], v[132:135], a[196:199], v[112:127]\n"
    "ds_read_b64_tr_b16 v[148:149], v194 offset:40128\n"
    "v_mul_f32 v191, v43, v43\n"
    "v_fmaak_f32 v188, v205, v188, 0xc0135761\n"
    "v_fmaak_f32 v189, v205, v189, 0xc0135761\n"
    "v_fmaak_f32 v190, v205, v190, 0xc0135761\n"
    "v_fmaak_f32 v191, v205, v191, 0xc0135761\n"
    "v_mfma_f32_32x32x16_bf16 v[96:111], v[132:135], a[192:195], v[96:111]\n"
    "ds_read_b64_tr_b16 v[150:151], v194 offset:40384\n"
    "v_mul_f32 v188, v40, v188\n"
    "v_mul_f32 v189, v41, v189\n"
    "v_mul_f32 v190, v42, v190\n"
    "v_mul_f32 v191, v43, v191\n"
    "s_waitcnt lgkmcnt(4)\n"
    "v_mfma_f32_32x32x16_bf16 v[64:79], v[136:139], a[200:203], v[64:79]\n"
    "s_add_u32 m0, s56, 0x9480\n"
    "ds_read_b64_tr_b16 v[128:129], v194 offset:35840\n"
    "ds_read_b64_tr_b16 v[130:131], v194 offset:36096\n"
    "v_exp_f32 v188, v188\n"
    "v_exp_f32 v189, v189\n"
    "v_mfma_f32_32x32x16_bf16 v[80:95], v[136:139], a[204:207], v[80:95]\n"
    "global_load_lds_dwordx4 v[206:207], off\n"
    "v_lshl_add_u64 v[206:207], v[208:209], 0, v[206:207]\n"
    "v_mfma_f32_32x32x16_bf16 v[112:127], v[140:143], a[204:207], v[112:127]\n"
    "ds_read_b64_tr_b16 v[132:133], v194 offset:35904\n"
    "v_exp_f32 v190, v190\n"
    "v_exp_f32 v191, v191\n"
    "v_mul_f32 v44, v40, v44\n"
    "v_mfma_f32_32x32x16_bf16 v[96:111], v[140:143], a[200:203], v[96:111]\n"
    "ds_read_b64_tr_b16 v[134:135], v194 offset:36160\n"
    "v_mul_f32 v45, v41, v45\n"
    "v_mul_f32 v46, v42, v46\n"
    "v_mul_f32 v47, v43, v47\n"
    "v_add_f32 v188, 1.0, v188\n"
    "s_waitcnt lgkmcnt(4)\n"
    "v_mfma_f32_32x32x16_bf16 v[64:79], v[144:147], a[208:211], v[64:79]\n"
    "s_add_u32 m0, s56, 0xa500\n"
    "ds_read_b64_tr_b16 v[136:137], v194 offset:31616\n"
    "ds_read_b64_tr_b16 v[138:139], v194 offset:31872\n"
    "v_add_f32 v189, 1.0, v189\n"
    "v_add_f32 v190, 1.0, v190\n"
    "v_add_f32 v191, 1.0, v191\n"
    "v_mfma_f32_32x32x16_bf16 v[80:95], v[144:147], a[212:215], v[80:95]\n"
    "global_load_lds_dwordx4 v[206:207], off\n"
    "v_lshl_add_u64 v[206:207], v[208:209], 0, v[206:207]\n"
    "v_mfma_f32_32x32x16_bf16 v[112:127], v[148:151], a[212:215], v[112:127]\n"
    "ds_read_b64_tr_b16 v[140:141], v194 offset:31680\n"
    "v_rcp_f32 v188, v188\n"
    "v_rcp_f32 v189, v189\n"
    "v_mfma_f32_32x32x16_bf16 v[96:111], v[148:151], a[208:211], v[96:111]\n"
    "ds_read_b64_tr_b16 v[142:143], v194 offset:31936\n"
    "v_rcp_f32 v190, v190\n"
    "v_rcp_f32 v191, v191\n"
    "s_waitcnt lgkmcnt(4)\n"
    "v_mfma_f32_32x32x16_bf16 v[64:79], v[128:131], a[216:219], v[64:79]\n"
    "s_add_u32 m0, s56, 0xb580\n"
    "ds_read_b64_tr_b16 v[144:145], v194 offset:27392\n"
    "ds_read_b64_tr_b16 v[146:147], v194 offset:27648\n"
    "v_mul_f32 v44, v44, v188\n"
    "v_mul_f32 v45, v45, v189\n"
    "v_mul_f32 v46, v46, v190\n"
    "v_mul_f32 v47, v47, v191\n"
    "v_mfma_f32_32x32x16_bf16 v[80:95], v[128:131], a[220:223], v[80:95]\n"
    "global_load_lds_dwordx4 v[206:207], off\n"
    "v_lshl_add_u64 v[206:207], v[208:209], 0, v[206:207]\n"
    "v_mfma_f32_32x32x16_bf16 v[112:127], v[132:135], a[220:223], v[112:127]\n"
    "ds_read_b64_tr_b16 v[148:149], v194 offset:27456\n"
    "v_cvt_pk_bf16_f32 v34, v44, v45\n"
    "v_cvt_pk_bf16_f32 v35, v46, v47\n"
    "v_mul_f32 v184, v48, v48\n"
    "v_mul_f32 v185, v49, v49\n"
    "v_mul_f32 v186, v50, v50\n"
    "v_mfma_f32_32x32x16_bf16 v[96:111], v[132:135], a[216:219], v[96:111]\n"
    "buffer_store_dwordx4 v[32:35], v210, s[24:27], s86 offen offset:1024 sc1\n"
    "ds_read_b64_tr_b16 v[150:151], v194 offset:27712\n"
    "s_waitcnt lgkmcnt(4)\n"
    "v_mfma_f32_32x32x16_bf16 v[64:79], v[136:139], a[224:227], v[64:79]\n"
    "s_add_u32 m0, s56, 0xc600\n"
    "ds_read_b64_tr_b16 v[128:129], v194 offset:18944\n"
    "ds_read_b64_tr_b16 v[130:131], v194 offset:19200\n"
    "v_mul_f32 v187, v51, v51\n"
    "v_fmaak_f32 v184, v205, v184, 0xc0135761\n"
    "v_fmaak_f32 v185, v205, v185, 0xc0135761\n"
    "v_fmaak_f32 v186, v205, v186, 0xc0135761\n"
    "v_mfma_f32_32x32x16_bf16 v[80:95], v[136:139], a[228:231], v[80:95]\n"
    "global_load_lds_dwordx4 v[206:207], off\n"
    "v_lshl_add_u64 v[206:207], v[208:209], 0, v[206:207]\n"
    "s_xor_b32 s1, s0, 4\n"
    "s_xor_b32 s100, s0, 3\n"
    "s_sub_u32 s1, s1, s100\n"
    "s_sub_u32 s1, s1, 1\n"
    "s_lshl_b32 s1, s1, 2\n"
    "v_mul_i32_i24 v214, s1, v208\n"
    "v_ashrrev_i32 v215, 31, v214\n"
    "v_lshl_add_u64 v[206:207], v[214:215], 0, v[206:207]\n"
    "v_mfma_f32_32x32x16_bf16 v[112:127], v[140:143], a[228:231], v[112:127]\n"
    "ds_read_b64_tr_b16 v[132:133], v194 offset:19008\n"
    "v_fmaak_f32 v187, v205, v187, 0xc0135761\n"
    "v_mul_f32 v184, v48, v184\n"
    "v_mul_f32 v185, v49, v185\n"
    "v_mul_f32 v186, v50, v186\n"
    "v_mul_f32 v187, v51, v187\n"
    "v_mfma_f32_32x32x16_bf16 v[96:111], v[140:143], a[224:227], v[96:111]\n"
    "ds_read_b64_tr_b16 v[134:135], v194 offset:19264\n"
    "v_exp_f32 v184, v184\n"
    "v_exp_f32 v185, v185\n"
    "s_waitcnt lgkmcnt(4)\n"
    "v_mfma_f32_32x32x16_bf16 v[64:79], v[144:147], a[232:235], v[64:79]\n"
    "s_add_u32 m0, s56, 0xd680\n"
    "ds_read_b64_tr_b16 v[136:137], v194 offset:23168\n"
    "ds_read_b64_tr_b16 v[138:139], v194 offset:23424\n"
    "v_exp_f32 v186, v186\n"
    "v_exp_f32 v187, v187\n"
    "v_mfma_f32_32x32x16_bf16 v[80:95], v[144:147], a[236:239], v[80:95]\n"
    "global_load_lds_dwordx4 v[206:207], off\n"
    "v_lshl_add_u64 v[206:207], v[208:209], 0, v[206:207]\n"
    "v_mfma_f32_32x32x16_bf16 v[112:127], v[148:151], a[236:239], v[112:127]\n"
    "ds_read_b64_tr_b16 v[140:141], v194 offset:23232\n"
    "v_mul_f32 v52, v48, v52\n"
    "v_mul_f32 v53, v49, v53\n"
    "v_mul_f32 v54, v50, v54\n"
    "v_mul_f32 v55, v51, v55\n"
    "v_add_f32 v184, 1.0, v184\n"
    "v_mfma_f32_32x32x16_bf16 v[96:111], v[148:151], a[232:235], v[96:111]\n"
    "ds_read_b64_tr_b16 v[142:143], v194 offset:23488\n"
    "v_add_f32 v185, 1.0, v185\n"
    "v_add_f32 v186, 1.0, v186\n"
    "v_add_f32 v187, 1.0, v187\n"
    "s_waitcnt lgkmcnt(4)\n"
    "v_mfma_f32_32x32x16_bf16 v[64:79], v[128:131], a[240:243], v[64:79]\n"
    "s_add_u32 m0, s56, 0xe700\n"
    "ds_read_b64_tr_b16 v[144:145], v193 offset:0\n"
    "ds_read_b64_tr_b16 v[146:147], v193 offset:256\n"
    "v_rcp_f32 v184, v184\n"
    "v_rcp_f32 v185, v185\n"
    "v_mfma_f32_32x32x16_bf16 v[80:95], v[128:131], a[244:247], v[80:95]\n"
    "global_load_lds_dwordx4 v[206:207], off\n"
    "v_lshl_add_u64 v[206:207], v[208:209], 0, v[206:207]\n"
    "v_mfma_f32_32x32x16_bf16 v[112:127], v[132:135], a[244:247], v[112:127]\n"
    "ds_read_b64_tr_b16 v[148:149], v193 offset:64\n"
    "v_rcp_f32 v186, v186\n"
    "v_rcp_f32 v187, v187\n"
    "v_mul_f32 v52, v52, v184\n"
    "v_mfma_f32_32x32x16_bf16 v[96:111], v[132:135], a[240:243], v[96:111]\n"
    "ds_read_b64_tr_b16 v[150:151], v193 offset:320\n"
    "v_mul_f32 v53, v53, v185\n"
    "v_mul_f32 v54, v54, v186\n"
    "v_mul_f32 v55, v55, v187\n"
    "v_cvt_pk_bf16_f32 v48, v52, v53\n"
    "s_waitcnt lgkmcnt(4)\n"
    "v_mfma_f32_32x32x16_bf16 v[64:79], v[136:139], a[248:251], v[64:79]\n"
    "s_add_u32 m0, s56, 0x26280\n"
    "ds_read_b64_tr_b16 v[128:129], v193 offset:4224\n"
    "ds_read_b64_tr_b16 v[130:131], v193 offset:4480\n"
    "v_cvt_pk_bf16_f32 v49, v54, v55\n"
    "v_mul_f32 v188, v56, v56\n"
    "v_mul_f32 v189, v57, v57\n"
    "v_mul_f32 v190, v58, v58\n"
    "v_mfma_f32_32x32x16_bf16 v[80:95], v[136:139], a[252:255], v[80:95]\n"
    "global_load_lds_dwordx4 v[206:207], off\n"
    "v_lshl_add_u64 v[206:207], v[208:209], 0, v[206:207]\n"
    "v_mfma_f32_32x32x16_bf16 v[112:127], v[140:143], a[252:255], v[112:127]\n"
    "ds_read_b64_tr_b16 v[132:133], v193 offset:4288\n"
    "v_mul_f32 v191, v59, v59\n"
    "v_fmaak_f32 v188, v205, v188, 0xc0135761\n"
    "v_fmaak_f32 v189, v205, v189, 0xc0135761\n"
    "v_fmaak_f32 v190, v205, v190, 0xc0135761\n"
    "v_fmaak_f32 v191, v205, v191, 0xc0135761\n"
    "v_mfma_f32_32x32x16_bf16 v[96:111], v[140:143], a[248:251], v[96:111]\n"
    "ds_read_b64_tr_b16 v[134:135], v193 offset:4544\n"
    "v_mul_f32 v188, v56, v188\n"
    "v_mul_f32 v189, v57, v189\n"
    "v_mul_f32 v190, v58, v190\n"
    "v_mul_f32 v191, v59, v191\n"
    "s_waitcnt lgkmcnt(4)\n"
    "v_mfma_f32_32x32x16_bf16 v[64:79], v[144:147], v[152:155], v[64:79]\n"
    "s_add_u32 m0, s56, 0x25200\n"
    "ds_read_b64_tr_b16 v[136:137], v193 offset:8448\n"
    "ds_read_b64_tr_b16 v[138:139], v193 offset:8704\n"
    "v_exp_f32 v188, v188\n"
    "v_exp_f32 v189, v189\n"
    "v_mfma_f32_32x32x16_bf16 v[80:95], v[144:147], v[156:159], v[80:95]\n"
    "global_load_lds_dwordx4 v[206:207], off\n"
    "v_lshl_add_u64 v[206:207], v[208:209], 0, v[206:207]\n"
    "s_xor_b32 s1, s0, 5\n"
    "s_xor_b32 s100, s0, 4\n"
    "s_sub_u32 s1, s1, s100\n"
    "s_sub_u32 s1, s1, 1\n"
    "s_lshl_b32 s1, s1, 2\n"
    "v_mul_i32_i24 v214, s1, v208\n"
    "v_ashrrev_i32 v215, 31, v214\n"
    "v_lshl_add_u64 v[206:207], v[214:215], 0, v[206:207]\n"
    "v_mfma_f32_32x32x16_bf16 v[112:127], v[148:151], v[156:159], v[112:127]\n"
    "ds_read_b64_tr_b16 v[140:141], v193 offset:8512\n"
    "v_exp_f32 v190, v190\n"
    "v_exp_f32 v191, v191\n"
    "v_mul_f32 v60, v56, v60\n"
    "v_mfma_f32_32x32x16_bf16 v[96:111], v[148:151], v[152:155], v[96:111]\n"
    "ds_read_b64_tr_b16 v[142:143], v193 offset:8768\n"
    "v_mul_f32 v61, v57, v61\n"
    "v_mul_f32 v62, v58, v62\n"
    "v_mul_f32 v63, v59, v63\n"
    "v_add_f32 v188, 1.0, v188\n"
    "s_waitcnt lgkmcnt(4)\n"
    "v_mfma_f32_32x32x16_bf16 v[64:79], v[128:131], v[160:163], v[64:79]\n"
    "s_add_u32 m0, s56, 0x24180\n"
    "ds_read_b64_tr_b16 v[144:145], v193 offset:12672\n"
    "ds_read_b64_tr_b16 v[146:147], v193 offset:12928\n"
    "v_add_f32 v189, 1.0, v189\n"
    "v_add_f32 v190, 1.0, v190\n"
    "v_add_f32 v191, 1.0, v191\n"
    "v_mfma_f32_32x32x16_bf16 v[80:95], v[128:131], v[164:167], v[80:95]\n"
    "global_load_lds_dwordx4 v[206:207], off\n"
    "v_lshl_add_u64 v[206:207], v[208:209], 0, v[206:207]\n"
    "v_mfma_f32_32x32x16_bf16 v[112:127], v[132:135], v[164:167], v[112:127]\n"
    "ds_read_b64_tr_b16 v[148:149], v193 offset:12736\n"
    "v_rcp_f32 v188, v188\n"
    "v_rcp_f32 v189, v189\n"
    "v_mfma_f32_32x32x16_bf16 v[96:111], v[132:135], v[160:163], v[96:111]\n"
    "ds_read_b64_tr_b16 v[150:151], v193 offset:12992\n"
    "s_waitcnt vmcnt(10)\n"
    "s_barrier\n"
    "s_waitcnt lgkmcnt(4)\n"
    "v_mfma_f32_32x32x16_bf16 v[64:79], v[136:139], v[168:171], v[64:79]\n"
    "s_add_u32 m0, s56, 0x1ff80\n"
    "ds_read_b64_tr_b16 v[128:129], v250 offset:16896\n"
    "ds_read_b64_tr_b16 v[130:131], v250 offset:17152\n"
    "v_rcp_f32 v190, v190\n"
    "v_rcp_f32 v191, v191\n"
    "v_mfma_f32_32x32x16_bf16 v[80:95], v[136:139], v[172:175], v[80:95]\n"
    "global_load_lds_dwordx4 v[206:207], off\n"
    "v_lshl_add_u64 v[206:207], v[208:209], 0, v[206:207]\n"
    "v_mfma_f32_32x32x16_bf16 v[112:127], v[140:143], v[172:175], v[112:127]\n"
    "ds_read_b64_tr_b16 v[132:133], v196 offset:16896\n"
    "ds_read_b64_tr_b16 v[134:135], v196 offset:17152\n"
    "v_mul_f32 v60, v60, v188\n"
    "v_mul_f32 v61, v61, v189\n"
    "v_mul_f32 v62, v62, v190\n"
    "v_mul_f32 v63, v63, v191\n"
    "v_mfma_f32_32x32x16_bf16 v[96:111], v[140:143], v[168:171], v[96:111]\n"
    "s_mov_b64 exec, s[52:53]\n"
    "ds_read_b128 v[132:135], v197 offset:62208\n"
    "s_mov_b64 exec, -1\n"
    "v_cvt_pk_bf16_f32 v50, v60, v61\n"
    "v_cvt_pk_bf16_f32 v51, v62, v63\n"
    "s_waitcnt lgkmcnt(5)\n"
    "v_mfma_f32_32x32x16_bf16 v[64:79], v[144:147], v[176:179], v[64:79]\n"
    "buffer_store_dwordx4 v[48:51], v211, s[24:27], s86 offen offset:1024 sc1\n"
    "ds_read_b64_tr_b16 v[136:137], v250 offset:21120\n"
    "ds_read_b64_tr_b16 v[138:139], v250 offset:21376\n"
    "s_add_u32 m0, s56, 0x1ef00\n"
    "v_mfma_f32_32x32x16_bf16 v[80:95], v[144:147], v[180:183], v[80:95]\n"
    "global_load_lds_dwordx4 v[206:207], off\n"
    "v_lshl_add_u64 v[206:207], v[208:209], 0, v[206:207]\n"
    "v_mfma_f32_32x32x16_bf16 v[112:127], v[148:151], v[180:183], v[112:127]\n"
    "ds_read_b64_tr_b16 v[140:141], v196 offset:21120\n"
    "ds_read_b64_tr_b16 v[142:143], v196 offset:21376\n"
    "v_mfma_f32_32x32x16_bf16 v[96:111], v[148:151], v[176:179], v[96:111]\n"
    "s_mov_b64 exec, s[52:53]\n"
    "ds_read_b128 v[140:143], v197 offset:62240\n"
    "s_mov_b64 exec, -1\n"
    "s_waitcnt lgkmcnt(5)\n"
    "v_mfma_f32_32x32x16_bf16 v[0:15], v[128:131], a[0:3], 0\n"
    "s_add_u32 m0, s56, 0x19c80\n"
    "v_add_u32 v250, 0x18000, v250\n"
    "ds_read_b64_tr_b16 v[144:145], v250 offset:11520\n"
    "ds_read_b64_tr_b16 v[146:147], v250 offset:11776\n"
    "v_mfma_f32_32x32x16_bf16 v[16:31], v[128:131], a[4:7], 0\n"
    "global_load_lds_dwordx4 v[206:207], off\n"
    "v_lshl_add_u64 v[206:207], v[208:209], 0, v[206:207]\n"
    "s_xor_b32 s1, s0, 6\n"
    "s_xor_b32 s100, s0, 5\n"
    "s_sub_u32 s1, s1, s100\n"
    "s_sub_u32 s1, s1, 1\n"
    "s_lshl_b32 s1, s1, 2\n"
    "v_mul_i32_i24 v214, s1, v208\n"
    "v_ashrrev_i32 v215, 31, v214\n"
    "v_lshl_add_u64 v[206:207], v[214:215], 0, v[206:207]\n"
    "v_mfma_f32_32x32x16_bf16 v[48:63], v[132:135], a[4:7], 0\n"
    "v_add_u32 v196, 0x18000, v196\n"
    "ds_read_b64_tr_b16 v[148:149], v196 offset:11520\n"
    "ds_read_b64_tr_b16 v[150:151], v196 offset:11776\n"
    "v_mfma_f32_32x32x16_bf16 v[32:47], v[132:135], a[0:3], 0\n"
    "s_xor_b32 s86, s4, 1\n"
    "s_mul_i32 s86, s86, 8192\n"
    "s_add_u32 s86, s86, s6\n"
    "s_mov_b64 exec, s[52:53]\n"
    "ds_read_b128 v[148:151], v197 offset:62272\n"
    "s_mov_b64 exec, -1\n"
    "s_waitcnt lgkmcnt(5)\n"
    "v_mfma_f32_32x32x16_bf16 v[0:15], v[136:139], a[8:11], v[0:15]\n"
    "s_add_u32 m0, s56, 0x18c00\n"
    "ds_read_b64_tr_b16 v[128:129], v250 offset:15744\n"
    "ds_read_b64_tr_b16 v[130:131], v250 offset:16000\n"
    "v_mul_f32 v184, v64, v64\n"
    "v_mul_f32 v185, v65, v65\n"
    "v_mul_f32 v186, v66, v66\n"
    "v_mul_f32 v187, v67, v67\n"
    "v_fmaak_f32 v184, v205, v184, 0xc0135761\n"
    "v_fmaak_f32 v185, v205, v185, 0xc0135761\n"
    "v_mfma_f32_32x32x16_bf16 v[16:31], v[136:139], a[12:15], v[16:31]\n"
    "global_load_lds_dwordx4 v[206:207], off\n"
    "v_lshl_add_u64 v[206:207], v[208:209], 0, v[206:207]\n"
    "v_mfma_f32_32x32x16_bf16 v[48:63], v[140:143], a[12:15], v[48:63]\n"
    "ds_read_b64_tr_b16 v[132:133], v196 offset:15744\n"
    "ds_read_b64_tr_b16 v[134:135], v196 offset:16000\n"
    "v_fmaak_f32 v186, v205, v186, 0xc0135761\n"
    "v_fmaak_f32 v187, v205, v187, 0xc0135761\n"
    "v_mul_f32 v184, v64, v184\n"
    "v_mul_f32 v185, v65, v185\n"
    "v_mul_f32 v186, v66, v186\n"
    "v_mul_f32 v187, v67, v187\n"
    "v_mfma_f32_32x32x16_bf16 v[32:47], v[140:143], a[8:11], v[32:47]\n"
    "s_mov_b64 exec, s[52:53]\n"
    "ds_read_b128 v[132:135], v197 offset:62304\n"
    "s_mov_b64 exec, -1\n"
    "v_exp_f32 v184, v184\n"
    "s_waitcnt lgkmcnt(5)\n"
    "v_mfma_f32_32x32x16_bf16 v[0:15], v[144:147], a[16:19], v[0:15]\n"
    "s_add_u32 m0, s56, 0x17b80\n"
    "ds_read_b64_tr_b16 v[136:137], v250 offset:19968\n"
    "ds_read_b64_tr_b16 v[138:139], v250 offset:20224\n"
    "v_exp_f32 v185, v185\n"
    "v_exp_f32 v186, v186\n"
    "v_exp_f32 v187, v187\n"
    "v_mfma_f32_32x32x16_bf16 v[16:31], v[144:147], a[20:23], v[16:31]\n"
    "global_load_lds_dwordx4 v[206:207], off\n"
    "v_lshl_add_u64 v[206:207], v[208:209], 0, v[206:207]\n"
    "v_mfma_f32_32x32x16_bf16 v[48:63], v[148:151], a[20:23], v[48:63]\n"
    "ds_read_b64_tr_b16 v[140:141], v196 offset:19968\n"
    "ds_read_b64_tr_b16 v[142:143], v196 offset:20224\n"
    "v_mul_f32 v68, v64, v68\n"
    "v_mul_f32 v69, v65, v69\n"
    "v_mul_f32 v70, v66, v70\n"
    "v_mul_f32 v71, v67, v71\n"
    "v_add_f32 v184, 1.0, v184\n"
    "v_mfma_f32_32x32x16_bf16 v[32:47], v[148:151], a[16:19], v[32:47]\n"
    "s_mov_b64 exec, s[52:53]\n"
    "ds_read_b128 v[140:143], v197 offset:62336\n"
    "s_mov_b64 exec, -1\n"
    "v_add_f32 v185, 1.0, v185\n"
    "v_add_f32 v186, 1.0, v186\n"
    "v_add_f32 v187, 1.0, v187\n"
    "s_waitcnt lgkmcnt(5)\n"
    "v_mfma_f32_32x32x16_bf16 v[0:15], v[128:131], a[24:27], v[0:15]\n"
    "s_add_u32 m0, s56, 0x16b00\n"
    "ds_read_b64_tr_b16 v[144:145], v250 offset:24192\n"
    "ds_read_b64_tr_b16 v[146:147], v250 offset:24448\n"
    "v_rcp_f32 v184, v184\n"
    "v_rcp_f32 v185, v185\n"
    "v_mfma_f32_32x32x16_bf16 v[16:31], v[128:131], a[28:31], v[16:31]\n"
    "global_load_lds_dwordx4 v[206:207], off\n"
    "v_lshl_add_u64 v[206:207], v[208:209], 0, v[206:207]\n"
    "v_mfma_f32_32x32x16_bf16 v[48:63], v[132:135], a[28:31], v[48:63]\n"
    "ds_read_b64_tr_b16 v[148:149], v196 offset:24192\n"
    "ds_read_b64_tr_b16 v[150:151], v196 offset:24448\n"
    "v_rcp_f32 v186, v186\n"
    "v_rcp_f32 v187, v187\n"
    "v_mul_f32 v68, v68, v184\n"
    "v_mfma_f32_32x32x16_bf16 v[32:47], v[132:135], a[24:27], v[32:47]\n"
    "s_mov_b64 exec, s[52:53]\n"
    "ds_read_b128 v[148:151], v197 offset:62368\n"
    "s_mov_b64 exec, -1\n"
    "v_mul_f32 v69, v69, v185\n"
    "v_mul_f32 v70, v70, v186\n"
    "v_mul_f32 v71, v71, v187\n"
    "s_waitcnt lgkmcnt(5)\n"
    "v_mfma_f32_32x32x16_bf16 v[0:15], v[136:139], a[32:35], v[0:15]\n"
    "s_add_u32 m0, s56, 0x15a80\n"
    "ds_read_b64_tr_b16 v[128:129], v250 offset:36864\n"
    "ds_read_b64_tr_b16 v[130:131], v250 offset:37120\n"
    "v_cvt_pk_bf16_f32 v64, v68, v69\n"
    "v_cvt_pk_bf16_f32 v65, v70, v71\n"
    "v_mul_f32 v188, v72, v72\n"
    "v_mul_f32 v189, v73, v73\n"
    "v_mul_f32 v190, v74, v74\n"
    "v_mfma_f32_32x32x16_bf16 v[16:31], v[136:139], a[36:39], v[16:31]\n"
    "global_load_lds_dwordx4 v[206:207], off\n"
    "v_lshl_add_u64 v[206:207], v[208:209], 0, v[206:207]\n"
    "s_xor_b32 s1, s0, 7\n"
    "s_xor_b32 s100, s0, 6\n"
    "s_sub_u32 s1, s1, s100\n"
    "s_sub_u32 s1, s1, 1\n"
    "s_lshl_b32 s1, s1, 2\n"
    "v_mul_i32_i24 v214, s1, v208\n"
    "v_ashrrev_i32 v215, 31, v214\n"
    "v_lshl_add_u64 v[206:207], v[214:215], 0, v[206:207]\n"
    "v_mfma_f32_32x32x16_bf16 v[48:63], v[140:143], a[36:39], v[48:63]\n"
    "ds_read_b64_tr_b16 v[132:133], v196 offset:36864\n"
    "ds_read_b64_tr_b16 v[134:135], v196 offset:37120\n"
    "v_mul_f32 v191, v75, v75\n"
    "v_fmaak_f32 v188, v205, v188, 0xc0135761\n"
    "v_fmaak_f32 v189, v205, v189, 0xc0135761\n"
    "v_fmaak_f32 v190, v205, v190, 0xc0135761\n"
    "v_fmaak_f32 v191, v205, v191, 0xc0135761\n"
    "v_mfma_f32_32x32x16_bf16 v[32:47], v[140:143], a[32:35], v[32:47]\n"
    "s_mov_b64 exec, s[52:53]\n"
    "ds_read_b128 v[132:135], v197 offset:62400\n"
    "s_mov_b64 exec, -1\n"
    "v_mul_f32 v188, v72, v188\n"
    "v_mul_f32 v189, v73, v189\n"
    "s_waitcnt lgkmcnt(5)\n"
    "v_mfma_f32_32x32x16_bf16 v[0:15], v[144:147], a[40:43], v[0:15]\n"
    "s_add_u32 m0, s56, 0x14a00\n"
    "ds_read_b64_tr_b16 v[136:137], v250 offset:41088\n"
    "ds_read_b64_tr_b16 v[138:139], v250 offset:41344\n"
    "v_mul_f32 v190, v74, v190\n"
    "v_mul_f32 v191, v75, v191\n"
    "v_exp_f32 v188, v188\n"
    "v_mfma_f32_32x32x16_bf16 v[16:31], v[144:147], a[44:47], v[16:31]\n"
    "global_load_lds_dwordx4 v[206:207], off\n"
    "v_lshl_add_u64 v[206:207], v[208:209], 0, v[206:207]\n"
    "v_mfma_f32_32x32x16_bf16 v[48:63], v[148:151], a[44:47], v[48:63]\n"
    "ds_read_b64_tr_b16 v[140:141], v196 offset:41088\n"
    "ds_read_b64_tr_b16 v[142:143], v196 offset:41344\n"
    "v_exp_f32 v189, v189\n"
    "v_exp_f32 v190, v190\n"
    "v_exp_f32 v191, v191\n"
    "v_mfma_f32_32x32x16_bf16 v[32:47], v[148:151], a[40:43], v[32:47]\n"
    "s_mov_b64 exec, s[52:53]\n"
    "ds_read_b128 v[140:143], v197 offset:62432\n"
    "s_mov_b64 exec, -1\n"
    "v_mul_f32 v76, v72, v76\n"
    "v_mul_f32 v77, v73, v77\n"
    "s_waitcnt lgkmcnt(5)\n"
    "v_mfma_f32_32x32x16_bf16 v[0:15], v[128:131], a[48:51], v[0:15]\n"
    "s_add_u32 m0, s56, 0x13980\n"
    "ds_read_b64_tr_b16 v[144:145], v250 offset:45312\n"
    "ds_read_b64_tr_b16 v[146:147], v250 offset:45568\n"
    "v_mul_f32 v78, v74, v78\n"
    "v_mul_f32 v79, v75, v79\n"
    "v_add_f32 v188, 1.0, v188\n"
    "v_add_f32 v189, 1.0, v189\n"
    "v_add_f32 v190, 1.0, v190\n"
    "v_mfma_f32_32x32x16_bf16 v[16:31], v[128:131], a[52:55], v[16:31]\n"
    "global_load_lds_dwordx4 v[206:207], off\n"
    "v_lshl_add_u64 v[206:207], v[208:209], 0, v[206:207]\n"
    "v_mfma_f32_32x32x16_bf16 v[48:63], v[132:135], a[52:55], v[48:63]\n"
    "ds_read_b64_tr_b16 v[148:149], v196 offset:45312\n"
    "ds_read_b64_tr_b16 v[150:151], v196 offset:45568\n"
    "v_add_f32 v191, 1.0, v191\n"
    "v_rcp_f32 v188, v188\n"
    "v_rcp_f32 v189, v189\n"
    "v_mfma_f32_32x32x16_bf16 v[32:47], v[132:135], a[48:51], v[32:47]\n"
    "s_mov_b64 exec, s[52:53]\n"
    "ds_read_b128 v[148:151], v197 offset:62464\n"
    "s_mov_b64 exec, -1\n"
    "v_rcp_f32 v190, v190\n"
    "s_waitcnt lgkmcnt(5)\n"
    "v_mfma_f32_32x32x16_bf16 v[0:15], v[136:139], a[56:59], v[0:15]\n"
    "s_add_u32 m0, s56, 0x12900\n"
    "v_add_u32 v250, 0xfffec300, v250\n"
    "ds_read_b64_tr_b16 v[128:129], v250 offset:8192\n"
    "ds_read_b64_tr_b16 v[130:131], v250 offset:8448\n"
    "v_rcp_f32 v191, v191\n"
    "v_mul_f32 v76, v76, v188\n"
    "v_mul_f32 v77, v77, v189\n"
    "v_mfma_f32_32x32x16_bf16 v[16:31], v[136:139], a[60:63], v[16:31]\n"
    "global_load_lds_dwordx4 v[206:207], off\n"
    "v_lshl_add_u64 v[206:207], v[208:209], 0, v[206:207]\n"
    "v_mfma_f32_32x32x16_bf16 v[48:63], v[140:143], a[60:63], v[48:63]\n"
    "v_add_u32 v196, 0xfffec300, v196\n"
    "ds_read_b64_tr_b16 v[132:133], v196 offset:8192\n"
    "ds_read_b64_tr_b16 v[134:135], v196 offset:8448\n"
    "v_mul_f32 v78, v78, v190\n"
    "v_mul_f32 v79, v79, v191\n"
    "v_cvt_pk_bf16_f32 v66, v76, v77\n"
    "v_cvt_pk_bf16_f32 v67, v78, v79\n"
    "v_mfma_f32_32x32x16_bf16 v[32:47], v[140:143], a[56:59], v[32:47]\n"
    "buffer_store_dwordx4 v[64:67], v210, s[24:27], s86 offen offset:0 sc1\n"
    "s_mov_b64 exec, s[52:53]\n"
    "ds_read_b128 v[132:135], v197 offset:62496\n"
    "s_mov_b64 exec, -1\n"
    "s_waitcnt lgkmcnt(5)\n"
    "v_mfma_f32_32x32x16_bf16 v[0:15], v[144:147], a[64:67], v[0:15]\n"
    "s_add_u32 m0, s56, 0x11880\n"
    "ds_read_b64_tr_b16 v[136:137], v250 offset:12416\n"
    "ds_read_b64_tr_b16 v[138:139], v250 offset:12672\n"
    "v_mul_f32 v184, v80, v80\n"
    "v_mfma_f32_32x32x16_bf16 v[16:31], v[144:147], a[68:71], v[16:31]\n"
    "global_load_lds_dwordx4 v[206:207], off\n"
    "v_lshl_add_u64 v[206:207], v[208:209], 0, v[206:207]\n"
    "s_mov_b32 s1, 8\n"
    "s_xor_b32 s100, s0, 7\n"
    "s_sub_u32 s1, s1, s100\n"
    "s_sub_u32 s1, s1, 1\n"
    "s_lshl_b32 s1, s1, 2\n"
    "v_mul_i32_i24 v214, s1, v208\n"
    "v_ashrrev_i32 v215, 31, v214\n"
    "v_lshl_add_u64 v[206:207], v[214:215], 0, v[206:207]\n"
    "v_mfma_f32_32x32x16_bf16 v[48:63], v[148:151], a[68:71], v[48:63]\n"
    "ds_read_b64_tr_b16 v[140:141], v196 offset:12416\n"
    "ds_read_b64_tr_b16 v[142:143], v196 offset:12672\n"
    "v_mul_f32 v185, v81, v81\n"
    "v_mul_f32 v186, v82, v82\n"
    "v_mul_f32 v187, v83, v83\n"
    "v_fmaak_f32 v184, v205, v184, 0xc0135761\n"
    "v_fmaak_f32 v185, v205, v185, 0xc0135761\n"
    "v_mfma_f32_32x32x16_bf16 v[32:47], v[148:151], a[64:67], v[32:47]\n"
    "s_mov_b64 exec, s[52:53]\n"
    "ds_read_b128 v[140:143], v197 offset:62528\n"
    "s_mov_b64 exec, -1\n"
    "v_fmaak_f32 v186, v205, v186, 0xc0135761\n"
    "v_fmaak_f32 v187, v205, v187, 0xc0135761\n"
    "v_mul_f32 v184, v80, v184\n"
    "s_waitcnt lgkmcnt(5)\n"
    "v_mfma_f32_32x32x16_bf16 v[0:15], v[128:131], a[72:75], v[0:15]\n"
    "s_add_u32 m0, s56, 0x0\n"
    "ds_read_b64_tr_b16 v[144:145], v250 offset:16640\n"
    "ds_read_b64_tr_b16 v[146:147], v250 offset:16896\n"
    "v_mul_f32 v185, v81, v185\n"
    "v_mul_f32 v186, v82, v186\n"
    "v_mul_f32 v187, v83, v187\n"
    "v_exp_f32 v184, v184\n"
    "v_mfma_f32_32x32x16_bf16 v[16:31], v[128:131], a[76:79], v[16:31]\n"
    "global_load_lds_dwordx4 v[206:207], off\n"
    "v_lshl_add_u64 v[206:207], v[208:209], 0, v[206:207]\n"
    "v_mfma_f32_32x32x16_bf16 v[48:63], v[132:135], a[76:79], v[48:63]\n"
    "ds_read_b64_tr_b16 v[148:149], v196 offset:16640\n"
    "ds_read_b64_tr_b16 v[150:151], v196 offset:16896\n"
    "v_exp_f32 v185, v185\n"
    "v_exp_f32 v186, v186\n"
    "v_mfma_f32_32x32x16_bf16 v[32:47], v[132:135], a[72:75], v[32:47]\n"
    "s_mov_b64 exec, s[52:53]\n"
    "ds_read_b128 v[148:151], v197 offset:62560\n"
    "s_mov_b64 exec, -1\n"
    "s_waitcnt vmcnt(10)\n"
    "s_barrier\n"
    "s_waitcnt lgkmcnt(5)\n"
    "v_mfma_f32_32x32x16_bf16 v[0:15], v[136:139], a[80:83], v[0:15]\n"
    "s_add_u32 m0, s56, 0x1080\n"
    "ds_read_b64_tr_b16 v[128:129], v250 offset:20864\n"
    "ds_read_b64_tr_b16 v[130:131], v250 offset:21120\n"
    "v_exp_f32 v187, v187\n"
    "v_mul_f32 v84, v80, v84\n"
    "v_mul_f32 v85, v81, v85\n"
    "v_mul_f32 v86, v82, v86\n"
    "v_mul_f32 v87, v83, v87\n"
    "v_mfma_f32_32x32x16_bf16 v[16:31], v[136:139], a[84:87], v[16:31]\n"
    "global_load_lds_dwordx4 v[206:207], off\n"
    "v_lshl_add_u64 v[206:207], v[208:209], 0, v[206:207]\n"
    "v_mfma_f32_32x32x16_bf16 v[48:63], v[140:143], a[84:87], v[48:63]\n"
    "ds_read_b64_tr_b16 v[132:133], v196 offset:20864\n"
    "ds_read_b64_tr_b16 v[134:135], v196 offset:21120\n"
    "v_add_f32 v184, 1.0, v184\n"
    "v_add_f32 v185, 1.0, v185\n"
    "v_add_f32 v186, 1.0, v186\n"
    "v_add_f32 v187, 1.0, v187\n"
    "v_mfma_f32_32x32x16_bf16 v[32:47], v[140:143], a[80:83], v[32:47]\n"
    "s_mov_b64 exec, s[52:53]\n"
    "ds_read_b128 v[132:135], v197 offset:62592\n"
    "s_mov_b64 exec, -1\n"
    "v_rcp_f32 v184, v184\n"
    "s_waitcnt lgkmcnt(5)\n"
    "v_mfma_f32_32x32x16_bf16 v[0:15], v[144:147], a[88:91], v[0:15]\n"
    "s_add_u32 m0, s56, 0x2100\n"
    "ds_read_b64_tr_b16 v[136:137], v250 offset:25088\n"
    "ds_read_b64_tr_b16 v[138:139], v250 offset:25344\n"
    "v_rcp_f32 v185, v185\n"
    "v_rcp_f32 v186, v186\n"
    "v_rcp_f32 v187, v187\n"
    "v_mfma_f32_32x32x16_bf16 v[16:31], v[144:147], a[92:95], v[16:31]\n"
    "global_load_lds_dwordx4 v[206:207], off\n"
    "v_lshl_add_u64 v[206:207], v[208:209], 0, v[206:207]\n"
    "v_mfma_f32_32x32x16_bf16 v[48:63], v[148:151], a[92:95], v[48:63]\n"
    "ds_read_b64_tr_b16 v[140:141], v196 offset:25088\n"
    "ds_read_b64_tr_b16 v[142:143], v196 offset:25344\n"
    "v_mul_f32 v84, v84, v184\n"
    "v_mul_f32 v85, v85, v185\n"
    "v_mul_f32 v86, v86, v186\n"
    "v_mul_f32 v87, v87, v187\n"
    "v_cvt_pk_bf16_f32 v80, v84, v85\n"
    "v_mfma_f32_32x32x16_bf16 v[32:47], v[148:151], a[88:91], v[32:47]\n"
    "s_mov_b64 exec, s[52:53]\n"
    "ds_read_b128 v[140:143], v197 offset:62624\n"
    "s_mov_b64 exec, -1\n"
    "v_cvt_pk_bf16_f32 v81, v86, v87\n"
    "v_mul_f32 v188, v88, v88\n"
    "s_waitcnt lgkmcnt(5)\n"
    "v_mfma_f32_32x32x16_bf16 v[0:15], v[128:131], a[96:99], v[0:15]\n"
    "s_add_u32 m0, s56, 0x3180\n"
    "ds_read_b64_tr_b16 v[144:145], v250 offset:29312\n"
    "ds_read_b64_tr_b16 v[146:147], v250 offset:29568\n"
    "v_mul_f32 v189, v89, v89\n"
    "v_mul_f32 v190, v90, v90\n"
    "v_mul_f32 v191, v91, v91\n"
    "v_fmaak_f32 v188, v205, v188, 0xc0135761\n"
    "v_fmaak_f32 v189, v205, v189, 0xc0135761\n"
    "v_mfma_f32_32x32x16_bf16 v[16:31], v[128:131], a[100:103], v[16:31]\n"
    "global_load_lds_dwordx4 v[206:207], off\n"
    "v_lshl_add_u64 v[206:207], v[208:209], 0, v[206:207]\n"
    "v_mfma_f32_32x32x16_bf16 v[48:63], v[132:135], a[100:103], v[48:63]\n"
    "s_xor_b32 s57, s4, 2\n"
    "s_mul_i32 s57, s57, 512\n"
    "s_add_u32 s57, s57, s5\n"
    "s_add_u32 m0, s56, 0x4200\n"
    "ds_read_b64_tr_b16 v[148:149], v196 offset:29312\n"
    "ds_read_b64_tr_b16 v[150:151], v196 offset:29568\n"
    "v_fmaak_f32 v190, v205, v190, 0xc0135761\n"
    "v_mfma_f32_32x32x16_bf16 v[32:47], v[132:135], a[96:99], v[32:47]\n"
    "buffer_load_dwordx4 v198, s[20:23], s57 offen lds\n"
    "s_mov_b64 exec, s[52:53]\n"
    "ds_read_b128 v[148:151], v197 offset:62656\n"
    "s_mov_b64 exec, -1\n"
    "s_waitcnt lgkmcnt(5)\n"
    "v_mfma_f32_32x32x16_bf16 v[0:15], v[136:139], a[104:107], v[0:15]\n"
    "s_add_u32 m0, s56, 0x5280\n"
    "s_add_u32 s57, s57, 0x24000\n"
    "ds_read_b64_tr_b16 v[128:129], v250 offset:33536\n"
    "ds_read_b64_tr_b16 v[130:131], v250 offset:33792\n"
    "v_fmaak_f32 v191, v205, v191, 0xc0135761\n"
    "v_mul_f32 v188, v88, v188\n"
    "v_mul_f32 v189, v89, v189\n"
    "v_mul_f32 v190, v90, v190\n"
    "v_mfma_f32_32x32x16_bf16 v[16:31], v[136:139], a[108:111], v[16:31]\n"
    "buffer_load_dwordx4 v198, s[20:23], s57 offen lds\n"
    "v_mfma_f32_32x32x16_bf16 v[48:63], v[140:143], a[108:111], v[48:63]\n"
    "ds_read_b64_tr_b16 v[132:133], v196 offset:33536\n"
    "ds_read_b64_tr_b16 v[134:135], v196 offset:33792\n"
    "v_mul_f32 v191, v91, v191\n"
    "v_exp_f32 v188, v188\n"
    "v_exp_f32 v189, v189\n"
    "v_mfma_f32_32x32x16_bf16 v[32:47], v[140:143], a[104:107], v[32:47]\n"
    "s_mov_b64 exec, s[52:53]\n"
    "ds_read_b128 v[132:135], v197 offset:62688\n"
    "s_mov_b64 exec, -1\n"
    "v_exp_f32 v190, v190\n"
    "s_waitcnt lgkmcnt(5)\n"
    "v_mfma_f32_32x32x16_bf16 v[0:15], v[144:147], a[112:115], v[0:15]\n"
    "s_add_u32 m0, s56, 0x6300\n"
    "s_add_u32 s57, s57, 0x24000\n"
    "ds_read_b64_tr_b16 v[136:137], v250 offset:37760\n"
    "ds_read_b64_tr_b16 v[138:139], v250 offset:38016\n"
    "v_exp_f32 v191, v191\n"
    "v_mul_f32 v92, v88, v92\n"
    "v_mul_f32 v93, v89, v93\n"
    "v_mfma_f32_32x32x16_bf16 v[16:31], v[144:147], a[116:119], v[16:31]\n"
    "buffer_load_dwordx4 v198, s[20:23], s57 offen lds\n"
    "v_mfma_f32_32x32x16_bf16 v[48:63], v[148:151], a[116:119], v[48:63]\n"
    "s_add_u32 m0, s56, 0xf780\n"
    "s_add_u32 s57, s57, 0x24000\n"
    "ds_read_b64_tr_b16 v[140:141], v196 offset:37760\n"
    "ds_read_b64_tr_b16 v[142:143], v196 offset:38016\n"
    "v_mul_f32 v94, v90, v94\n"
    "v_mul_f32 v95, v91, v95\n"
    "v_add_f32 v188, 1.0, v188\n"
    "v_add_f32 v189, 1.0, v189\n"
    "v_mfma_f32_32x32x16_bf16 v[32:47], v[148:151], a[112:115], v[32:47]\n"
    "buffer_load_dwordx4 v198, s[20:23], s57 offen lds\n"
    "s_mov_b64 exec, s[52:53]\n"
    "ds_read_b128 v[140:143], v197 offset:62720\n"
    "s_mov_b64 exec, -1\n"
    "s_waitcnt lgkmcnt(5)\n"
    "v_mfma_f32_32x32x16_bf16 v[0:15], v[128:131], a[120:123], v[0:15]\n"
    "s_add_u32 m0, s56, 0x10800\n"
    "s_add_u32 s57, s57, 0x24000\n"
    "s_xor_b32 s1, s0, 1\n"
    "s_xor_b32 s100, s0, 0\n"
    "s_sub_u32 s1, s1, s100\n"
    "s_sub_u32 s1, s1, 1\n"
    "s_mul_i32 s1, s1, 589824\n"
    "s_add_u32 s57, s57, s1\n"
    "ds_read_b64_tr_b16 v[144:145], v250 offset:41984\n"
    "ds_read_b64_tr_b16 v[146:147], v250 offset:42240\n"
    "v_mfma_f32_32x32x16_bf16 v[16:31], v[128:131], a[124:127], v[16:31]\n"
    "buffer_load_dwordx4 v198, s[20:23], s57 offen lds\n"
    "v_mfma_f32_32x32x16_bf16 v[48:63], v[132:135], a[124:127], v[48:63]\n"
    "ds_read_b64_tr_b16 v[148:149], v196 offset:41984\n"
    "ds_read_b64_tr_b16 v[150:151], v196 offset:42240\n"
    "v_add_f32 v190, 1.0, v190\n"
    "v_add_f32 v191, 1.0, v191\n"
    "v_rcp_f32 v188, v188\n"
    "v_mfma_f32_32x32x16_bf16 v[32:47], v[132:135], a[120:123], v[32:47]\n"
    "s_mov_b64 exec, s[52:53]\n"
    "ds_read_b128 v[148:151], v197 offset:62752\n"
    "s_mov_b64 exec, -1\n"
    "v_rcp_f32 v189, v189\n"
    "s_waitcnt lgkmcnt(5)\n"
    "v_mfma_f32_32x32x16_bf16 v[0:15], v[136:139], a[128:131], v[0:15]\n"
    "s_add_u32 m0, s56, 0x1ad00\n"
    "s_add_u32 s57, s57, 0x24000\n"
    "v_add_u32 v250, 0x13d00, v250\n"
    "ds_read_b64_tr_b16 v[128:129], v250 offset:57984\n"
    "ds_read_b64_tr_b16 v[130:131], v250 offset:58240\n"
    "v_rcp_f32 v190, v190\n"
    "v_rcp_f32 v191, v191\n"
    "v_mfma_f32_32x32x16_bf16 v[16:31], v[136:139], a[132:135], v[16:31]\n"
    "buffer_load_dwordx4 v198, s[20:23], s57 offen lds\n"
    "v_mfma_f32_32x32x16_bf16 v[48:63], v[140:143], a[132:135], v[48:63]\n"
    "s_add_u32 m0, s56, 0x1bd80\n"
    "s_add_u32 s57, s57, 0x24000\n"
    "v_add_u32 v196, 0x13d00, v196\n"
    "ds_read_b64_tr_b16 v[132:133], v196 offset:57984\n"
    "ds_read_b64_tr_b16 v[134:135], v196 offset:58240\n"
    "v_mul_f32 v92, v92, v188\n"
    "v_mul_f32 v93, v93, v189\n"
    "v_mfma_f32_32x32x16_bf16 v[32:47], v[140:143], a[128:131], v[32:47]\n"
    "buffer_load_dwordx4 v198, s[20:23], s57 offen lds\n"
    "s_mov_b64 exec, s[52:53]\n"
    "ds_read_b128 v[132:135], v197 offset:62784\n"
    "s_mov_b64 exec, -1\n"
    "s_waitcnt lgkmcnt(5)\n"
    "v_mfma_f32_32x32x16_bf16 v[0:15], v[144:147], a[136:139], v[0:15]\n"
    "s_add_u32 m0, s56, 0x1ce00\n"
    "s_add_u32 s57, s57, 0x24000\n"
    "ds_read_b64_tr_b16 v[136:137], v250 offset:53760\n"
    "ds_read_b64_tr_b16 v[138:139], v250 offset:54016\n"
    "v_mul_f32 v94, v94, v190\n"
    "v_mul_f32 v95, v95, v191\n"
    "v_cvt_pk_bf16_f32 v82, v92, v93\n"
    "v_cvt_pk_bf16_f32 v83, v94, v95\n"
    "v_mfma_f32_32x32x16_bf16 v[16:31], v[144:147], a[140:143], v[16:31]\n"
    "buffer_load_dwordx4 v198, s[20:23], s57 offen lds\n"
    "v_mfma_f32_32x32x16_bf16 v[48:63], v[148:151], a[140:143], v[48:63]\n"
    "buffer_store_dwordx4 v[80:83], v211, s[24:27], s86 offen offset:0 sc1\n"
    "ds_read_b64_tr_b16 v[140:141], v196 offset:53760\n"
    "ds_read_b64_tr_b16 v[142:143], v196 offset:54016\n"
    "v_mfma_f32_32x32x16_bf16 v[32:47], v[148:151], a[136:139], v[32:47]\n"
    "s_mov_b64 exec, s[52:53]\n"
    "ds_read_b128 v[140:143], v197 offset:62816\n"
    "s_mov_b64 exec, -1\n"
    "v_mul_f32 v184, v96, v96\n"
    "s_waitcnt lgkmcnt(5)\n"
    "v_mfma_f32_32x32x16_bf16 v[0:15], v[128:131], a[144:147], v[0:15]\n"
    "s_add_u32 m0, s56, 0x1de80\n"
    "s_add_u32 s57, s57, 0x24000\n"
    "s_xor_b32 s1, s0, 2\n"
    "s_xor_b32 s100, s0, 1\n"
    "s_sub_u32 s1, s1, s100\n"
    "s_sub_u32 s1, s1, 1\n"
    "s_mul_i32 s1, s1, 589824\n"
    "s_add_u32 s57, s57, s1\n"
    "ds_read_b64_tr_b16 v[144:145], v250 offset:49536\n"
    "ds_read_b64_tr_b16 v[146:147], v250 offset:49792\n"
    "v_mfma_f32_32x32x16_bf16 v[16:31], v[128:131], a[148:151], v[16:31]\n"
    "buffer_load_dwordx4 v198, s[20:23], s57 offen lds\n"
    "v_mfma_f32_32x32x16_bf16 v[48:63], v[132:135], a[148:151], v[48:63]\n"
    "s_add_u32 m0, s56, 0x21000\n"
    "s_add_u32 s57, s57, 0x24000\n"
    "ds_read_b64_tr_b16 v[148:149], v196 offset:49536\n"
    "ds_read_b64_tr_b16 v[150:151], v196 offset:49792\n"
    "v_mul_f32 v185, v97, v97\n"
    "v_mul_f32 v186, v98, v98\n"
    "v_mul_f32 v187, v99, v99\n"
    "v_fmaak_f32 v184, v205, v184, 0xc0135761\n"
    "v_mfma_f32_32x32x16_bf16 v[32:47], v[132:135], a[144:147], v[32:47]\n"
    "buffer_load_dwordx4 v198, s[20:23], s57 offen lds\n"
    "s_mov_b64 exec, s[52:53]\n"
    "ds_read_b128 v[148:151], v197 offset:62848\n"
    "s_mov_b64 exec, -1\n"
    "s_waitcnt lgkmcnt(5)\n"
    "v_mfma_f32_32x32x16_bf16 v[0:15], v[136:139], a[152:155], v[0:15]\n"
    "s_add_u32 m0, s56, 0x22080\n"
    "s_add_u32 s57, s57, 0x24000\n"
    "ds_read_b64_tr_b16 v[128:129], v250 offset:32640\n"
    "ds_read_b64_tr_b16 v[130:131], v250 offset:32896\n"
    "v_fmaak_f32 v185, v205, v185, 0xc0135761\n"
    "v_fmaak_f32 v186, v205, v186, 0xc0135761\n"
    "v_fmaak_f32 v187, v205, v187, 0xc0135761\n"
    "v_mfma_f32_32x32x16_bf16 v[16:31], v[136:139], a[156:159], v[16:31]\n"
    "buffer_load_dwordx4 v198, s[20:23], s57 offen lds\n"
    "v_mfma_f32_32x32x16_bf16 v[48:63], v[140:143], a[156:159], v[48:63]\n"
    "ds_read_b64_tr_b16 v[132:133], v196 offset:32640\n"
    "ds_read_b64_tr_b16 v[134:135], v196 offset:32896\n"
    "v_mul_f32 v184, v96, v184\n"
    "v_mul_f32 v185, v97, v185\n"
    "v_mul_f32 v186, v98, v186\n"
    "v_mul_f32 v187, v99, v187\n"
    "v_mfma_f32_32x32x16_bf16 v[32:47], v[140:143], a[152:155], v[32:47]\n"
    "s_mov_b64 exec, s[52:53]\n"
    "ds_read_b128 v[132:135], v197 offset:62880\n"
    "s_mov_b64 exec, -1\n"
    "v_exp_f32 v184, v184\n"
    "v_exp_f32 v185, v185\n"
    "s_waitcnt lgkmcnt(5)\n"
    "v_mfma_f32_32x32x16_bf16 v[0:15], v[144:147], a[160:163], v[0:15]\n"
    "s_add_u32 m0, s56, 0x23100\n"
    "s_add_u32 s57, s57, 0x24000\n"
    "ds_read_b64_tr_b16 v[136:137], v250 offset:28416\n"
    "ds_read_b64_tr_b16 v[138:139], v250 offset:28672\n"
    "v_exp_f32 v186, v186\n"
    "v_exp_f32 v187, v187\n"
    "v_mfma_f32_32x32x16_bf16 v[16:31], v[144:147], a[164:167], v[16:31]\n"
    "buffer_load_dwordx4 v198, s[20:23], s57 offen lds\n"
    "v_mfma_f32_32x32x16_bf16 v[48:63], v[148:151], a[164:167], v[48:63]\n"
    "ds_read_b64_tr_b16 v[140:141], v196 offset:28416\n"
    "ds_read_b64_tr_b16 v[142:143], v196 offset:28672\n"
    "v_mul_f32 v100, v96, v100\n"
    "v_mul_f32 v101, v97, v101\n"
    "v_mul_f32 v102, v98, v102\n"
    "v_mul_f32 v103, v99, v103\n"
    "v_mfma_f32_32x32x16_bf16 v[32:47], v[148:151], a[160:163], v[32:47]\n"
    "s_mov_b64 exec, s[52:53]\n"
    "ds_read_b128 v[140:143], v197 offset:62912\n"
    "s_mov_b64 exec, -1\n"
    "v_add_f32 v184, 1.0, v184\n"
    "v_add_f32 v185, 1.0, v185\n"
    "v_add_f32 v186, 1.0, v186\n"
    "s_waitcnt lgkmcnt(5)\n"
    "v_mfma_f32_32x32x16_bf16 v[0:15], v[128:131], a[168:171], v[0:15]\n"
    "ds_read_b64_tr_b16 v[144:145], v250 offset:7296\n"
    "ds_read_b64_tr_b16 v[146:147], v250 offset:7552\n"
    "v_add_f32 v187, 1.0, v187\n"
    "v_rcp_f32 v184, v184\n"
    "v_rcp_f32 v185, v185\n"
    "v_mfma_f32_32x32x16_bf16 v[16:31], v[128:131], a[172:175], v[16:31]\n"
    "v_rcp_f32 v186, v186\n"
    "v_rcp_f32 v187, v187\n"
    "v_mul_f32 v100, v100, v184\n"
    "v_mul_f32 v101, v101, v185\n"
    "v_mul_f32 v102, v102, v186\n"
    "v_mfma_f32_32x32x16_bf16 v[48:63], v[132:135], a[172:175], v[48:63]\n"
    "ds_read_b64_tr_b16 v[148:149], v196 offset:7296\n"
    "ds_read_b64_tr_b16 v[150:151], v196 offset:7552\n"
    "v_mul_f32 v103, v103, v187\n"
    "v_cvt_pk_bf16_f32 v96, v100, v101\n"
    "v_cvt_pk_bf16_f32 v97, v102, v103\n"
    "v_mul_f32 v188, v104, v104\n"
    "v_mul_f32 v189, v105, v105\n"
    "v_mfma_f32_32x32x16_bf16 v[32:47], v[132:135], a[168:171], v[32:47]\n"
    "s_mov_b64 exec, s[52:53]\n"
    "ds_read_b128 v[148:151], v197 offset:62944\n"
    "s_mov_b64 exec, -1\n"
    "s_waitcnt vmcnt(13)\n"
    "s_barrier\n"
    "s_waitcnt lgkmcnt(5)\n"
    "v_mfma_f32_32x32x16_bf16 v[0:15], v[136:139], a[176:179], v[0:15]\n"
    "s_add_u32 m0, s56, 0x7380\n"
    "s_add_u32 s57, s57, 0x24000\n"
    "s_xor_b32 s1, s0, 3\n"
    "s_xor_b32 s100, s0, 2\n"
    "s_sub_u32 s1, s1, s100\n"
    "s_sub_u32 s1, s1, 1\n"
    "s_mul_i32 s1, s1, 589824\n"
    "s_add_u32 s57, s57, s1\n"
    "ds_read_b64_tr_b16 v[128:129], v250 offset:3072\n"
    "ds_read_b64_tr_b16 v[130:131], v250 offset:3328\n"
    "v_mfma_f32_32x32x16_bf16 v[16:31], v[136:139], a[180:183], v[16:31]\n"
    "buffer_load_dwordx4 v198, s[20:23], s57 offen lds\n"
    "v_mfma_f32_32x32x16_bf16 v[48:63], v[140:143], a[180:183], v[48:63]\n"
    "s_add_u32 m0, s56, 0x8400\n"
    "s_add_u32 s57, s57, 0x24000\n"
    "ds_read_b64_tr_b16 v[132:133], v196 offset:3072\n"
    "ds_read_b64_tr_b16 v[134:135], v196 offset:3328\n"
    "v_mul_f32 v190, v106, v106\n"
    "v_mul_f32 v191, v107, v107\n"
    "v_fmaak_f32 v188, v205, v188, 0xc0135761\n"
    "v_fmaak_f32 v189, v205, v189, 0xc0135761\n"
    "v_mfma_f32_32x32x16_bf16 v[32:47], v[140:143], a[176:179], v[32:47]\n"
    "buffer_load_dwordx4 v198, s[20:23], s57 offen lds\n"
    "s_mov_b64 exec, s[52:53]\n"
    "ds_read_b128 v[132:135], v197 offset:62976\n"
    "s_mov_b64 exec, -1\n"
    "s_waitcnt lgkmcnt(5)\n"
    "v_mfma_f32_32x32x16_bf16 v[0:15], v[144:147], a[184:187], v[0:15]\n"
    "s_add_u32 m0, s56, 0x9480\n"
    "s_add_u32 s57, s57, 0x24000\n"
    "v_add_u32 v250, 0xffffdb80, v250\n"
    "ds_read_b64_tr_b16 v[136:137], v250 offset:8192\n"
    "ds_read_b64_tr_b16 v[138:139], v250 offset:8448\n"
    "v_fmaak_f32 v190, v205, v190, 0xc0135761\n"
    "v_fmaak_f32 v191, v205, v191, 0xc0135761\n"
    "v_mfma_f32_32x32x16_bf16 v[16:31], v[144:147], a[188:191], v[16:31]\n"
    "buffer_load_dwordx4 v198, s[20:23], s57 offen lds\n"
    "v_mfma_f32_32x32x16_bf16 v[48:63], v[148:151], a[188:191], v[48:63]\n"
    "v_add_u32 v196, 0xffffdb80, v196\n"
    "ds_read_b64_tr_b16 v[140:141], v196 offset:8192\n"
    "ds_read_b64_tr_b16 v[142:143], v196 offset:8448\n"
    "v_mul_f32 v188, v104, v188\n"
    "v_mul_f32 v189, v105, v189\n"
    "v_mul_f32 v190, v106, v190\n"
    "v_mul_f32 v191, v107, v191\n"
    "v_mfma_f32_32x32x16_bf16 v[32:47], v[148:151], a[184:187], v[32:47]\n"
    "s_mov_b64 exec, s[52:53]\n"
    "ds_read_b128 v[140:143], v197 offset:63008\n"
    "s_mov_b64 exec, -1\n"
    "v_exp_f32 v188, v188\n"
    "s_waitcnt lgkmcnt(5)\n"
    "v_mfma_f32_32x32x16_bf16 v[0:15], v[128:131], a[192:195], v[0:15]\n"
    "s_add_u32 m0, s56, 0xa500\n"
    "s_add_u32 s57, s57, 0x24000\n"
    "ds_read_b64_tr_b16 v[144:145], v250 offset:3968\n"
    "ds_read_b64_tr_b16 v[146:147], v250 offset:4224\n"
    "v_exp_f32 v189, v189\n"
    "v_exp_f32 v190, v190\n"
    "v_mfma_f32_32x32x16_bf16 v[16:31], v[128:131], a[196:199], v[16:31]\n"
    "buffer_load_dwordx4 v198, s[20:23], s57 offen lds\n"
    "v_mfma_f32_32x32x16_bf16 v[48:63], v[132:135], a[196:199], v[48:63]\n"
    "s_add_u32 m0, s56, 0xb580\n"
    "s_add_u32 s57, s57, 0x24000\n"
    "s_xor_b32 s1, s0, 4\n"
    "s_xor_b32 s100, s0, 3\n"
    "s_sub_u32 s1, s1, s100\n"
    "s_sub_u32 s1, s1, 1\n"
    "s_mul_i32 s1, s1, 589824\n"
    "s_add_u32 s57, s57, s1\n"
    "ds_read_b64_tr_b16 v[148:149], v196 offset:3968\n"
    "ds_read_b64_tr_b16 v[150:151], v196 offset:4224\n"
    "v_mfma_f32_32x32x16_bf16 v[32:47], v[132:135], a[192:195], v[32:47]\n"
    "buffer_load_dwordx4 v198, s[20:23], s57 offen lds\n"
    "s_mov_b64 exec, s[52:53]\n"
    "ds_read_b128 v[148:151], v197 offset:63040\n"
    "s_mov_b64 exec, -1\n"
    "s_waitcnt lgkmcnt(5)\n"
    "v_mfma_f32_32x32x16_bf16 v[0:15], v[136:139], a[200:203], v[0:15]\n"
    "s_add_u32 m0, s56, 0xc600\n"
    "s_add_u32 s57, s57, 0x24000\n"
    "v_add_u32 v250, 0xffffdf00, v250\n"
    "ds_read_b64_tr_b16 v[128:129], v250 offset:8192\n"
    "ds_read_b64_tr_b16 v[130:131], v250 offset:8448\n"
    "v_exp_f32 v191, v191\n"
    "v_mul_f32 v108, v104, v108\n"
    "v_mfma_f32_32x32x16_bf16 v[16:31], v[136:139], a[204:207], v[16:31]\n"
    "buffer_load_dwordx4 v198, s[20:23], s57 offen lds\n"
    "v_mfma_f32_32x32x16_bf16 v[48:63], v[140:143], a[204:207], v[48:63]\n"
    "v_add_u32 v196, 0xffffdf00, v196\n"
    "ds_read_b64_tr_b16 v[132:133], v196 offset:8192\n"
    "ds_read_b64_tr_b16 v[134:135], v196 offset:8448\n"
    "v_mul_f32 v109, v105, v109\n"
    "v_mul_f32 v110, v106, v110\n"
    "v_mul_f32 v111, v107, v111\n"
    "v_mfma_f32_32x32x16_bf16 v[32:47], v[140:143], a[200:203], v[32:47]\n"
    "s_mov_b64 exec, s[52:53]\n"
    "ds_read_b128 v[132:135], v197 offset:63072\n"
    "s_mov_b64 exec, -1\n"
    "v_add_f32 v188, 1.0, v188\n"
    "v_add_f32 v189, 1.0, v189\n"
    "v_add_f32 v190, 1.0, v190\n"
    "s_waitcnt lgkmcnt(5)\n"
    "v_mfma_f32_32x32x16_bf16 v[0:15], v[144:147], a[208:211], v[0:15]\n"
    "s_add_u32 m0, s56, 0xd680\n"
    "s_add_u32 s57, s57, 0x24000\n"
    "ds_read_b64_tr_b16 v[136:137], v250 offset:3968\n"
    "ds_read_b64_tr_b16 v[138:139], v250 offset:4224\n"
    "v_add_f32 v191, 1.0, v191\n"
    "v_rcp_f32 v188, v188\n"
    "v_mfma_f32_32x32x16_bf16 v[16:31], v[144:147], a[212:215], v[16:31]\n"
    "buffer_load_dwordx4 v198, s[20:23], s57 offen lds\n"
    "v_mfma_f32_32x32x16_bf16 v[48:63], v[148:151], a[212:215], v[48:63]\n"
    "s_add_u32 m0, s56, 0xe700\n"
    "s_add_u32 s57, s57, 0x24000\n"
    "ds_read_b64_tr_b16 v[140:141], v196 offset:3968\n"
    "ds_read_b64_tr_b16 v[142:143], v196 offset:4224\n"
    "v_rcp_f32 v189, v189\n"
    "v_rcp_f32 v190, v190\n"
    "v_mfma_f32_32x32x16_bf16 v[32:47], v[148:151], a[208:211], v[32:47]\n"
    "buffer_load_dwordx4 v198, s[20:23], s57 offen lds\n"
    "s_mov_b64 exec, s[52:53]\n"
    "ds_read_b128 v[140:143], v197 offset:63104\n"
    "s_mov_b64 exec, -1\n"
    "s_waitcnt lgkmcnt(5)\n"
    "v_mfma_f32_32x32x16_bf16 v[0:15], v[128:131], a[216:219], v[0:15]\n"
    "s_add_u32 m0, s56, 0x26280\n"
    "s_add_u32 s57, s57, 0x24000\n"
    "s_xor_b32 s1, s0, 5\n"
    "s_xor_b32 s100, s0, 4\n"
    "s_sub_u32 s1, s1, s100\n"
    "s_sub_u32 s1, s1, 1\n"
    "s_mul_i32 s1, s1, 589824\n"
    "s_add_u32 s57, s57, s1\n"
    "v_add_u32 v250, 0xffffdf00, v250\n"
    "ds_read_b64_tr_b16 v[144:145], v250 offset:8192\n"
    "ds_read_b64_tr_b16 v[146:147], v250 offset:8448\n"
    "v_mfma_f32_32x32x16_bf16 v[16:31], v[128:131], a[220:223], v[16:31]\n"
    "buffer_load_dwordx4 v198, s[20:23], s57 offen lds\n"
    "v_mfma_f32_32x32x16_bf16 v[48:63], v[132:135], a[220:223], v[48:63]\n"
    "v_add_u32 v196, 0xffffdf00, v196\n"
    "ds_read_b64_tr_b16 v[148:149], v196 offset:8192\n"
    "ds_read_b64_tr_b16 v[150:151], v196 offset:8448\n"
    "v_rcp_f32 v191, v191\n"
    "v_mul_f32 v108, v108, v188\n"
    "v_mul_f32 v109, v109, v189\n"
    "v_mfma_f32_32x32x16_bf16 v[32:47], v[132:135], a[216:219], v[32:47]\n"
    "s_mov_b64 exec, s[52:53]\n"
    "ds_read_b128 v[148:151], v197 offset:63136\n"
    "s_mov_b64 exec, -1\n"
    "v_mul_f32 v110, v110, v190\n"
    "v_mul_f32 v111, v111, v191\n"
    "s_waitcnt lgkmcnt(5)\n"
    "v_mfma_f32_32x32x16_bf16 v[0:15], v[136:139], a[224:227], v[0:15]\n"
    "s_add_u32 m0, s56, 0x25200\n"
    "s_add_u32 s57, s57, 0x24000\n"
    "ds_read_b64_tr_b16 v[128:129], v250 offset:3968\n"
    "ds_read_b64_tr_b16 v[130:131], v250 offset:4224\n"
    "v_cvt_pk_bf16_f32 v98, v108, v109\n"
    "v_cvt_pk_bf16_f32 v99, v110, v111\n"
    "v_mul_f32 v184, v112, v112\n"
    "v_mul_f32 v185, v113, v113\n"
    "v_mfma_f32_32x32x16_bf16 v[16:31], v[136:139], a[228:231], v[16:31]\n"
    "buffer_load_dwordx4 v198, s[20:23], s57 offen lds\n"
    "v_mfma_f32_32x32x16_bf16 v[48:63], v[140:143], a[228:231], v[48:63]\n"
    "buffer_store_dwordx4 v[96:99], v210, s[24:27], s86 offen offset:1024 sc1\n"
    "ds_read_b64_tr_b16 v[132:133], v196 offset:3968\n"
    "ds_read_b64_tr_b16 v[134:135], v196 offset:4224\n"
    "s_add_u32 m0, s56, 0x24180\n"
    "s_add_u32 s57, s57, 0x24000\n"
    "v_mfma_f32_32x32x16_bf16 v[32:47], v[140:143], a[224:227], v[32:47]\n"
    "buffer_load_dwordx4 v198, s[20:23], s57 offen lds\n"
    "s_mov_b64 exec, s[52:53]\n"
    "ds_read_b128 v[132:135], v197 offset:63168\n"
    "s_mov_b64 exec, -1\n"
    "s_waitcnt lgkmcnt(5)\n"
    "v_mfma_f32_32x32x16_bf16 v[0:15], v[144:147], a[232:235], v[0:15]\n"
    "s_add_u32 m0, s56, 0x1ff80\n"
    "s_add_u32 s57, s57, 0x24000\n"
    "v_add_u32 v250, 0xffffdf00, v250\n"
    "ds_read_b64_tr_b16 v[136:137], v250 offset:8192\n"
    "ds_read_b64_tr_b16 v[138:139], v250 offset:8448\n"
    "v_mfma_f32_32x32x16_bf16 v[16:31], v[144:147], a[236:239], v[16:31]\n"
    "buffer_load_dwordx4 v198, s[20:23], s57 offen lds\n"
    "v_mfma_f32_32x32x16_bf16 v[48:63], v[148:151], a[236:239], v[48:63]\n"
    "v_add_u32 v196, 0xffffdf00, v196\n"
    "ds_read_b64_tr_b16 v[140:141], v196 offset:8192\n"
    "ds_read_b64_tr_b16 v[142:143], v196 offset:8448\n"
    "v_mul_f32 v186, v114, v114\n"
    "v_mul_f32 v187, v115, v115\n"
    "v_fmaak_f32 v184, v205, v184, 0xc0135761\n"
    "v_fmaak_f32 v185, v205, v185, 0xc0135761\n"
    "v_mfma_f32_32x32x16_bf16 v[32:47], v[148:151], a[232:235], v[32:47]\n"
    "s_mov_b64 exec, s[52:53]\n"
    "ds_read_b128 v[140:143], v197 offset:63200\n"
    "s_mov_b64 exec, -1\n"
    "v_fmaak_f32 v186, v205, v186, 0xc0135761\n"
    "v_fmaak_f32 v187, v205, v187, 0xc0135761\n"
    "s_waitcnt lgkmcnt(5)\n"
    "v_mfma_f32_32x32x16_bf16 v[0:15], v[128:131], a[240:243], v[0:15]\n"
    "v_add_u32 v250, 0xffff0780, v250\n"
    "ds_read_b64_tr_b16 v[144:145], v250 offset:0\n"
    "ds_read_b64_tr_b16 v[146:147], v250 offset:256\n"
    "v_mul_f32 v184, v112, v184\n"
    "v_mul_f32 v185, v113, v185\n"
    "v_mul_f32 v186, v114, v186\n"
    "v_mul_f32 v187, v115, v187\n"
    "v_mfma_f32_32x32x16_bf16 v[16:31], v[128:131], a[244:247], v[16:31]\n"
    "v_exp_f32 v184, v184\n"
    "v_exp_f32 v185, v185\n"
    "v_exp_f32 v186, v186\n"
    "v_mfma_f32_32x32x16_bf16 v[48:63], v[132:135], a[244:247], v[48:63]\n"
    "v_add_u32 v196, 0xffff0780, v196\n"
    "ds_read_b64_tr_b16 v[148:149], v196 offset:0\n"
    "ds_read_b64_tr_b16 v[150:151], v196 offset:256\n"
    "v_exp_f32 v187, v187\n"
    "v_mul_f32 v116, v112, v116\n"
    "v_mul_f32 v117, v113, v117\n"
    "v_mul_f32 v118, v114, v118\n"
    "v_mfma_f32_32x32x16_bf16 v[32:47], v[132:135], a[240:243], v[32:47]\n"
    "s_mov_b64 exec, s[52:53]\n"
    "ds_read_b128 v[148:151], v197 offset:63232\n"
    "s_mov_b64 exec, -1\n"
    "v_mul_f32 v119, v115, v119\n"
    "v_add_f32 v184, 1.0, v184\n"
    "s_waitcnt lgkmcnt(5)\n"
    "v_mfma_f32_32x32x16_bf16 v[0:15], v[136:139], a[248:251], v[0:15]\n"
    "ds_read_b64_tr_b16 v[128:129], v250 offset:4224\n"
    "ds_read_b64_tr_b16 v[130:131], v250 offset:4480\n"
    "v_add_f32 v185, 1.0, v185\n"
    "v_add_f32 v186, 1.0, v186\n"
    "v_add_f32 v187, 1.0, v187\n"
    "v_rcp_f32 v184, v184\n"
    "v_mfma_f32_32x32x16_bf16 v[16:31], v[136:139], a[252:255], v[16:31]\n"
    "v_rcp_f32 v185, v185\n"
    "v_rcp_f32 v186, v186\n"
    "v_rcp_f32 v187, v187\n"
    "v_mul_f32 v116, v116, v184\n"
    "v_mul_f32 v117, v117, v185\n"
    "v_mfma_f32_32x32x16_bf16 v[48:63], v[140:143], a[252:255], v[48:63]\n"
    "ds_read_b64_tr_b16 v[132:133], v196 offset:4224\n"
    "ds_read_b64_tr_b16 v[134:135], v196 offset:4480\n"
    "v_mul_f32 v118, v118, v186\n"
    "v_mul_f32 v119, v119, v187\n"
    "v_cvt_pk_bf16_f32 v112, v116, v117\n"
    "v_cvt_pk_bf16_f32 v113, v118, v119\n"
    "v_mul_f32 v188, v120, v120\n"
    "v_mfma_f32_32x32x16_bf16 v[32:47], v[140:143], a[248:251], v[32:47]\n"
    "s_mov_b64 exec, s[52:53]\n"
    "ds_read_b128 v[132:135], v197 offset:63264\n"
    "s_mov_b64 exec, -1\n"
    "v_mul_f32 v189, v121, v121\n"
    "v_mul_f32 v190, v122, v122\n"
    "s_waitcnt lgkmcnt(5)\n"
    "v_mfma_f32_32x32x16_bf16 v[0:15], v[144:147], v[152:155], v[0:15]\n"
    "ds_read_b64_tr_b16 v[136:137], v250 offset:8448\n"
    "ds_read_b64_tr_b16 v[138:139], v250 offset:8704\n"
    "v_mul_f32 v191, v123, v123\n"
    "v_fmaak_f32 v188, v205, v188, 0xc0135761\n"
    "v_fmaak_f32 v189, v205, v189, 0xc0135761\n"
    "v_fmaak_f32 v190, v205, v190, 0xc0135761\n"
    "v_fmaak_f32 v191, v205, v191, 0xc0135761\n"
    "v_mfma_f32_32x32x16_bf16 v[16:31], v[144:147], v[156:159], v[16:31]\n"
    "v_mul_f32 v188, v120, v188\n"
    "v_mul_f32 v189, v121, v189\n"
    "v_mul_f32 v190, v122, v190\n"
    "v_mul_f32 v191, v123, v191\n"
    "v_exp_f32 v188, v188\n"
    "v_mfma_f32_32x32x16_bf16 v[48:63], v[148:151], v[156:159], v[48:63]\n"
    "ds_read_b64_tr_b16 v[140:141], v196 offset:8448\n"
    "ds_read_b64_tr_b16 v[142:143], v196 offset:8704\n"
    "v_exp_f32 v189, v189\n"
    "v_exp_f32 v190, v190\n"
    "v_exp_f32 v191, v191\n"
    "v_mfma_f32_32x32x16_bf16 v[32:47], v[148:151], v[152:155], v[32:47]\n"
    "s_mov_b64 exec, s[52:53]\n"
    "ds_read_b128 v[140:143], v197 offset:63296\n"
    "s_mov_b64 exec, -1\n"
    "v_mul_f32 v124, v120, v124\n"
    "v_mul_f32 v125, v121, v125\n"
    "v_mul_f32 v126, v122, v126\n"
    "s_waitcnt lgkmcnt(5)\n"
    "v_mfma_f32_32x32x16_bf16 v[0:15], v[128:131], v[160:163], v[0:15]\n"
    "ds_read_b64_tr_b16 v[144:145], v250 offset:12672\n"
    "ds_read_b64_tr_b16 v[146:147], v250 offset:12928\n"
    "v_mul_f32 v127, v123, v127\n"
    "v_add_f32 v188, 1.0, v188\n"
    "v_add_f32 v189, 1.0, v189\n"
    "v_add_f32 v190, 1.0, v190\n"
    "v_add_f32 v191, 1.0, v191\n"
    "v_mfma_f32_32x32x16_bf16 v[16:31], v[128:131], v[164:167], v[16:31]\n"
    "v_rcp_f32 v188, v188\n"
    "v_rcp_f32 v189, v189\n"
    "v_rcp_f32 v190, v190\n"
    "v_mfma_f32_32x32x16_bf16 v[48:63], v[132:135], v[164:167], v[48:63]\n"
    "ds_read_b64_tr_b16 v[148:149], v196 offset:12672\n"
    "ds_read_b64_tr_b16 v[150:151], v196 offset:12928\n"
    "v_rcp_f32 v191, v191\n"
    "v_mul_f32 v124, v124, v188\n"
    "v_mul_f32 v125, v125, v189\n"
    "v_mul_f32 v126, v126, v190\n"
    "v_mul_f32 v127, v127, v191\n"
    "v_mfma_f32_32x32x16_bf16 v[32:47], v[132:135], v[160:163], v[32:47]\n"
    "s_mov_b64 exec, s[52:53]\n"
    "ds_read_b128 v[148:151], v197 offset:63328\n"
    "s_mov_b64 exec, -1\n"
    "s_waitcnt vmcnt(13)\n"
    "s_barrier\n"
    "s_waitcnt lgkmcnt(5)\n"
    "v_mfma_f32_32x32x16_bf16 v[0:15], v[136:139], v[168:171], v[0:15]\n"
    "s_add_u32 m0, s56, 0x1ef00\n"
    "s_add_u32 s57, s57, 0x24000\n"
    "s_xor_b32 s1, s0, 6\n"
    "s_xor_b32 s100, s0, 5\n"
    "s_sub_u32 s1, s1, s100\n"
    "s_sub_u32 s1, s1, 1\n"
    "s_mul_i32 s1, s1, 589824\n"
    "s_add_u32 s57, s57, s1\n"
    "ds_read_b64_tr_b16 v[128:129], v193 offset:16896\n"
    "ds_read_b64_tr_b16 v[130:131], v193 offset:17152\n"
    "v_mfma_f32_32x32x16_bf16 v[16:31], v[136:139], v[172:175], v[16:31]\n"
    "buffer_load_dwordx4 v198, s[20:23], s57 offen lds\n"
    "v_mfma_f32_32x32x16_bf16 v[48:63], v[140:143], v[172:175], v[48:63]\n"
    "s_add_u32 m0, s56, 0x19c80\n"
    "s_add_u32 s57, s57, 0x24000\n"
    "ds_read_b64_tr_b16 v[132:133], v193 offset:16960\n"
    "v_cvt_pk_bf16_f32 v114, v124, v125\n"
    "v_cvt_pk_bf16_f32 v115, v126, v127\n"
    "v_mfma_f32_32x32x16_bf16 v[32:47], v[140:143], v[168:171], v[32:47]\n"
    "buffer_load_dwordx4 v198, s[20:23], s57 offen lds\n"
    "ds_read_b64_tr_b16 v[134:135], v193 offset:17216\n"
    "s_waitcnt lgkmcnt(4)\n"
    "v_mfma_f32_32x32x16_bf16 v[0:15], v[144:147], v[176:179], v[0:15]\n"
    "buffer_store_dwordx4 v[112:115], v211, s[24:27], s86 offen offset:1024 sc1\n"
    "ds_read_b64_tr_b16 v[136:137], v193 offset:21120\n"
    "ds_read_b64_tr_b16 v[138:139], v193 offset:21376\n"
    "s_add_u32 m0, s56, 0x18c00\n"
    "s_add_u32 s57, s57, 0x24000\n"
    "v_mfma_f32_32x32x16_bf16 v[16:31], v[144:147], v[180:183], v[16:31]\n"
    "buffer_load_dwordx4 v198, s[20:23], s57 offen lds\n"
    "v_mfma_f32_32x32x16_bf16 v[48:63], v[148:151], v[180:183], v[48:63]\n"
    "ds_read_b64_tr_b16 v[140:141], v193 offset:21184\n"
    "v_mfma_f32_32x32x16_bf16 v[32:47], v[148:151], v[176:179], v[32:47]\n"
    "ds_read_b64_tr_b16 v[142:143], v193 offset:21440\n"
    "s_waitcnt lgkmcnt(4)\n"
    "v_mfma_f32_32x32x16_bf16 v[64:79], v[128:131], a[0:3], 0\n"
    "s_add_u32 m0, s56, 0x17b80\n"
    "s_add_u32 s57, s57, 0x24000\n"
    "ds_read_b64_tr_b16 v[144:145], v193 offset:25344\n"
    "ds_read_b64_tr_b16 v[146:147], v193 offset:25600\n"
    "v_mfma_f32_32x32x16_bf16 v[80:95], v[128:131], a[4:7], 0\n"
    "buffer_load_dwordx4 v198, s[20:23], s57 offen lds\n"
    "v_mfma_f32_32x32x16_bf16 v[112:127], v[132:135], a[4:7], 0\n"
    "s_add_u32 m0, s56, 0x16b00\n"
    "s_add_u32 s57, s57, 0x24000\n"
    "s_xor_b32 s1, s0, 7\n"
    "s_xor_b32 s100, s0, 6\n"
    "s_sub_u32 s1, s1, s100\n"
    "s_sub_u32 s1, s1, 1\n"
    "s_mul_i32 s1, s1, 589824\n"
    "s_add_u32 s57, s57, s1\n"
    "ds_read_b64_tr_b16 v[148:149], v193 offset:25408\n"
    "v_mfma_f32_32x32x16_bf16 v[96:111], v[132:135], a[0:3], 0\n"
    "buffer_load_dwordx4 v198, s[20:23], s57 offen lds\n"
    "ds_read_b64_tr_b16 v[150:151], v193 offset:25664\n"
    "s_lshl_b32 s86, s12, 10\n"
    "s_add_u32 s86, s86, 0x8000\n"
    "s_waitcnt lgkmcnt(4)\n"
    "v_mfma_f32_32x32x16_bf16 v[64:79], v[136:139], a[8:11], v[64:79]\n"
    "s_add_u32 m0, s56, 0x15a80\n"
    "s_add_u32 s57, s57, 0x24000\n"
    "ds_read_b64_tr_b16 v[128:129], v193 offset:63360\n"
    "ds_read_b64_tr_b16 v[130:131], v193 offset:63616\n"
    "v_cvt_pk_bf16_f32 v44, v44, v45\n"
    "v_cvt_pk_bf16_f32 v45, v46, v47\n"
    "v_cndmask_b32 v46, 0, v45, s[88:89]\n"
    "v_mfma_f32_32x32x16_bf16 v[80:95], v[136:139], a[12:15], v[80:95]\n"
    "buffer_load_dwordx4 v198, s[20:23], s57 offen lds\n"
    "v_mfma_f32_32x32x16_bf16 v[112:127], v[140:143], a[12:15], v[112:127]\n"
    "ds_read_b64_tr_b16 v[132:133], v193 offset:63424\n"
    "v_cndmask_b32 v44, v44, v46, s[90:91]\n"
    "v_cndmask_b32 v45, v45, v247, s[90:91]\n"
    "v_cvt_pk_bf16_f32 v60, v60, v61\n"
    "v_cvt_pk_bf16_f32 v61, v62, v63\n"
    "v_mfma_f32_32x32x16_bf16 v[96:111], v[140:143], a[8:11], v[96:111]\n"
    "buffer_store_dwordx2 v[44:45], v224, s[28:31], 0 offen sc1\n"
    "ds_read_b64_tr_b16 v[134:135], v193 offset:63680\n"
    "s_waitcnt lgkmcnt(4)\n"
    "v_mfma_f32_32x32x16_bf16 v[64:79], v[144:147], a[16:19], v[64:79]\n"
    "s_add_u32 m0, s56, 0x14a00\n"
    "s_add_u32 s57, s57, 0x24000\n"
    "ds_read_b64_tr_b16 v[136:137], v194 offset:2048\n"
    "ds_read_b64_tr_b16 v[138:139], v194 offset:2304\n"
    "v_mfma_f32_32x32x16_bf16 v[80:95], v[144:147], a[20:23], v[80:95]\n"
    "buffer_load_dwordx4 v198, s[20:23], s57 offen lds\n"
    "v_mfma_f32_32x32x16_bf16 v[112:127], v[148:151], a[20:23], v[112:127]\n"
    "s_add_u32 m0, s56, 0x13980\n"
    "s_add_u32 s57, s57, 0x24000\n"
    "ds_read_b64_tr_b16 v[140:141], v194 offset:2112\n"
    "v_cndmask_b32 v62, 0, v61, s[88:89]\n"
    "v_cndmask_b32 v60, v60, v62, s[90:91]\n"
    "v_mfma_f32_32x32x16_bf16 v[96:111], v[148:151], a[16:19], v[96:111]\n"
    "buffer_load_dwordx4 v198, s[20:23], s57 offen lds\n"
    "ds_read_b64_tr_b16 v[142:143], v194 offset:2368\n"
    "s_waitcnt lgkmcnt(4)\n"
    "v_mfma_f32_32x32x16_bf16 v[64:79], v[128:131], a[24:27], v[64:79]\n"
    "s_add_u32 m0, s56, 0x12900\n"
    "s_add_u32 s57, s57, 0x24000\n"
    "s_mov_b32 s1, 8\n"
    "s_xor_b32 s100, s0, 7\n"
    "s_sub_u32 s1, s1, s100\n"
    "s_sub_u32 s1, s1, 1\n"
    "s_mul_i32 s1, s1, 589824\n"
    "s_add_u32 s57, s57, s1\n"
    "ds_read_b64_tr_b16 v[144:145], v194 offset:44288\n"
    "ds_read_b64_tr_b16 v[146:147], v194 offset:44544\n"
    "v_mfma_f32_32x32x16_bf16 v[80:95], v[128:131], a[28:31], v[80:95]\n"
    "buffer_load_dwordx4 v198, s[20:23], s57 offen lds\n"
    "v_mfma_f32_32x32x16_bf16 v[112:127], v[132:135], a[28:31], v[112:127]\n"
    "ds_read_b64_tr_b16 v[148:149], v194 offset:44352\n"
    "v_cndmask_b32 v61, v61, v247, s[90:91]\n"
    "v_mfma_f32_32x32x16_bf16 v[96:111], v[132:135], a[24:27], v[96:111]\n"
    "buffer_store_dwordx2 v[60:61], v224, s[28:31], s87 offen sc1\n"
    "ds_read_b64_tr_b16 v[150:151], v194 offset:44608\n"
    "s_lshl_b32 s2, s9, 11\n"
    "s_waitcnt lgkmcnt(4)\n"
    "v_mfma_f32_32x32x16_bf16 v[64:79], v[136:139], a[32:35], v[64:79]\n"
    "s_add_u32 m0, s56, 0x11880\n"
    "s_add_u32 s57, s57, 0x24000\n"
    "ds_read_b64_tr_b16 v[128:129], v194 offset:48512\n"
    "ds_read_b64_tr_b16 v[130:131], v194 offset:48768\n"
    "v_add_u32 v208, s2, v246\n"
    "v_mov_b32 v200, 8192\n"
    "v_mov_b32 v201, 0x80000000\n"
    "v_mfma_f32_32x32x16_bf16 v[80:95], v[136:139], a[36:39], v[80:95]\n"
    "buffer_load_dwordx4 v198, s[20:23], s57 offen lds\n"
    "v_mfma_f32_32x32x16_bf16 v[112:127], v[140:143], a[36:39], v[112:127]\n"
    "s_add_u32 m0, s56, 0x0\n"
    "s_add_u32 s57, s57, 0x24000\n"
    "ds_read_b64_tr_b16 v[132:133], v194 offset:48576\n"
    "v_cmp_eq_u32 vcc, v208, v246\n"
    "v_lshlrev_b32 v207, 3, v246\n"
    "v_mfma_f32_32x32x16_bf16 v[96:111], v[140:143], a[32:35], v[96:111]\n"
    "buffer_load_dwordx4 v198, s[20:23], s57 offen lds\n"
    "ds_read_b64_tr_b16 v[134:135], v194 offset:48832\n"
    "s_lshr_b32 s65, s8, 3\n"
    "s_lshl_b32 s65, s65, 2\n"
    "s_add_u32 s65, s65, s9\n"
    "s_mul_i32 s75, s64, 36864\n"
    "s_waitcnt lgkmcnt(4)\n"
    "v_mfma_f32_32x32x16_bf16 v[64:79], v[144:147], a[40:43], v[64:79]\n"
    "s_add_u32 m0, s56, 0x1080\n"
    "s_add_u32 s57, s57, 0x24000\n"
    "ds_read_b64_tr_b16 v[136:137], v194 offset:52736\n"
    "ds_read_b64_tr_b16 v[138:139], v194 offset:52992\n"
    "v_cndmask_b32 v200, v201, v200, vcc\n"
    "v_add_u32 v209, v200, v246\n"
    "v_mov_b32 v200, -1\n"
    "v_mfma_f32_32x32x16_bf16 v[80:95], v[144:147], a[44:47], v[80:95]\n"
    "buffer_load_dwordx4 v198, s[20:23], s57 offen lds\n"
    "v_mfma_f32_32x32x16_bf16 v[112:127], v[148:151], a[44:47], v[112:127]\n"
    "ds_read_b64_tr_b16 v[140:141], v194 offset:52800\n"
    "v_mov_b32 v201, -1\n"
    "v_mov_b32 v202, -1\n"
    "v_mov_b32 v203, -1\n"
    "v_mfma_f32_32x32x16_bf16 v[96:111], v[148:151], a[40:43], v[96:111]\n"
    "s_xor_b32 s81, s11, 1\n"
    "s_mul_i32 s81, s81, 9216\n"
    "s_add_u32 s81, s81, s75\n"
    "s_xor_b32 s82, s11, 2\n"
    "s_mul_i32 s82, s82, 9216\n"
    "s_add_u32 s82, s82, s75\n"
    "s_xor_b32 s83, s11, 3\n"
    "s_mul_i32 s83, s83, 9216\n"
    "s_add_u32 s83, s83, s75\n"
    "ds_read_b64_tr_b16 v[142:143], v194 offset:53056\n"
    "s_waitcnt lgkmcnt(4)\n"
    "v_mfma_f32_32x32x16_bf16 v[64:79], v[128:131], a[48:51], v[64:79]\n"
    "ds_read_b64_tr_b16 v[144:145], v194 offset:56960\n"
    "ds_read_b64_tr_b16 v[146:147], v194 offset:57216\n"
    "v_mov_b32 v200, -1\n"
    "v_mul_f32 v32, 0x3eae1a93, v32\n"
    "v_mul_f32 v33, 0x3eae1a93, v33\n"
    "v_mul_f32 v34, 0x3eae1a93, v34\n"
    "v_mfma_f32_32x32x16_bf16 v[80:95], v[128:131], a[52:55], v[80:95]\n"
    "v_mul_f32 v35, 0x3eae1a93, v35\n"
    "v_mul_f32 v36, 0x3eae1a93, v36\n"
    "v_mul_f32 v37, 0x3eae1a93, v37\n"
    "v_mul_f32 v38, 0x3eae1a93, v38\n"
    "v_mul_f32 v39, 0x3eae1a93, v39\n"
    "v_mul_f32 v42, 0x3eae1a93, v42\n"
    "v_mfma_f32_32x32x16_bf16 v[112:127], v[132:135], a[52:55], v[112:127]\n"
    "ds_read_b64_tr_b16 v[148:149], v194 offset:57024\n"
    "v_mul_f32 v43, 0x3eae1a93, v43\n"
    "v_mul_f32 v40, 0xbfb8aa3b, v40\n"
    "v_exp_f32 v40, v40\n"
    "v_cvt_pk_bf16_f32 v212, v32, v33\n"
    "v_mfma_f32_32x32x16_bf16 v[96:111], v[132:135], a[48:51], v[96:111]\n"
    "ds_read_b64_tr_b16 v[150:151], v194 offset:57280\n"
    "v_cvt_pk_bf16_f32 v213, v34, v35\n"
    "v_cvt_pk_bf16_f32 v214, v36, v37\n"
    "v_cvt_pk_bf16_f32 v215, v38, v39\n"
    "v_add_f32 v40, 1.0, v40\n"
    "s_waitcnt lgkmcnt(4)\n"
    "v_mfma_f32_32x32x16_bf16 v[64:79], v[136:139], a[56:59], v[64:79]\n"
    "ds_read_b64_tr_b16 v[128:129], v195 offset:4096\n"
    "ds_read_b64_tr_b16 v[130:131], v195 offset:4352\n"
    "v_rcp_f32 v222, v40\n"
    "v_cvt_pk_bf16_f32 v42, v42, v43\n"
    "v_mfma_f32_32x32x16_bf16 v[80:95], v[136:139], a[60:63], v[80:95]\n"
    "buffer_store_dwordx4 v[200:203], v208, s[44:47], s81 offen offset:0 sc1\n"
    "v_mfma_f32_32x32x16_bf16 v[112:127], v[140:143], a[60:63], v[112:127]\n"
    "ds_read_b64_tr_b16 v[132:133], v195 offset:4160\n"
    "v_sub_f32 v41, 1.0, v222\n"
    "v_mul_f32 v48, 0x3eae1a93, v48\n"
    "v_mul_f32 v49, 0x3eae1a93, v49\n"
    "v_mfma_f32_32x32x16_bf16 v[96:111], v[140:143], a[56:59], v[96:111]\n"
    "ds_read_b64_tr_b16 v[134:135], v195 offset:4416\n"
    "v_mul_f32 v50, 0x3eae1a93, v50\n"
    "v_mul_f32 v51, 0x3eae1a93, v51\n"
    "v_mul_f32 v52, 0x3eae1a93, v52\n"
    "s_waitcnt lgkmcnt(4)\n"
    "v_mfma_f32_32x32x16_bf16 v[64:79], v[144:147], a[64:67], v[64:79]\n"
    "ds_read_b64_tr_b16 v[136:137], v195 offset:8320\n"
    "ds_read_b64_tr_b16 v[138:139], v195 offset:8576\n"
    "v_mul_f32 v53, 0x3eae1a93, v53\n"
    "v_mul_f32 v54, 0x3eae1a93, v54\n"
    "v_mul_f32 v55, 0x3eae1a93, v55\n"
    "v_mfma_f32_32x32x16_bf16 v[80:95], v[144:147], a[68:71], v[80:95]\n"
    "v_mul_f32 v58, 0x3eae1a93, v58\n"
    "v_mul_f32 v59, 0x3eae1a93, v59\n"
    "v_mul_f32 v56, 0xbfb8aa3b, v56\n"
    "v_mfma_f32_32x32x16_bf16 v[112:127], v[148:151], a[68:71], v[112:127]\n"
    "ds_read_b64_tr_b16 v[140:141], v195 offset:8384\n"
    "v_exp_f32 v56, v56\n"
    "v_cvt_pk_bf16_f32 v216, v48, v49\n"
    "v_cvt_pk_bf16_f32 v217, v50, v51\n"
    "v_cvt_pk_bf16_f32 v218, v52, v53\n"
    "v_mfma_f32_32x32x16_bf16 v[96:111], v[148:151], a[64:67], v[96:111]\n"
    "ds_read_b64_tr_b16 v[142:143], v195 offset:8640\n"
    "v_cvt_pk_bf16_f32 v219, v54, v55\n"
    "v_add_f32 v56, 1.0, v56\n"
    "s_waitcnt lgkmcnt(4)\n"
    "v_mfma_f32_32x32x16_bf16 v[64:79], v[128:131], a[72:75], v[64:79]\n"
    "buffer_store_dwordx4 v[200:203], v208, s[44:47], s81 offen offset:1024 sc1\n"
    "ds_read_b64_tr_b16 v[144:145], v195 offset:12544\n"
    "ds_read_b64_tr_b16 v[146:147], v195 offset:12800\n"
    "v_mfma_f32_32x32x16_bf16 v[80:95], v[128:131], a[76:79], v[80:95]\n"
    "v_rcp_f32 v223, v56\n"
    "v_cvt_pk_bf16_f32 v58, v58, v59\n"
    "v_mfma_f32_32x32x16_bf16 v[112:127], v[132:135], a[76:79], v[112:127]\n"
    "ds_read_b64_tr_b16 v[148:149], v195 offset:12608\n"
    "v_sub_f32 v57, 1.0, v223\n"
    "v_cndmask_b32 v220, v42, v58, s[88:89]\n"
    "v_and_b32 v248, 31, v192\n"
    "v_mfma_f32_32x32x16_bf16 v[96:111], v[132:135], a[72:75], v[96:111]\n"
    "s_lshl_b32 s96, s13, 8\n"
    "s_add_u32 s96, s96, 0x27b00\n"
    "ds_read_b64_tr_b16 v[150:151], v195 offset:12864\n"
    "s_waitcnt vmcnt(17)\n"
    "s_barrier\n"
    "s_waitcnt lgkmcnt(4)\n"
    "v_mfma_f32_32x32x16_bf16 v[64:79], v[136:139], a[80:83], v[64:79]\n"
    "s_xor_b32 s57, s4, 3\n"
    "s_mul_i32 s57, s57, 512\n"
    "s_add_u32 s57, s57, s5\n"
    "s_add_u32 m0, s56, 0x2100\n"
    "ds_read_b64_tr_b16 v[128:129], v193 offset:29568\n"
    "ds_read_b64_tr_b16 v[130:131], v193 offset:29824\n"
    "v_lshl_add_u32 v248, v248, 2, s96\n"
    "v_mfma_f32_32x32x16_bf16 v[80:95], v[136:139], a[84:87], v[80:95]\n"
    "buffer_load_dwordx4 v198, s[20:23], s57 offen lds\n"
    "ds_write_b32 v248, v41 offset:0\n"
    "ds_write_b32 v248, v57 offset:128\n"
    "v_mfma_f32_32x32x16_bf16 v[112:127], v[140:143], a[84:87], v[112:127]\n"
    "s_add_u32 m0, s56, 0x3180\n"
    "s_add_u32 s57, s57, 0x24000\n"
    "ds_read_b64_tr_b16 v[132:133], v193 offset:29632\n"
    "v_mul_f32 v184, v0, v0\n"
    "v_mul_f32 v185, v1, v1\n"
    "v_mul_f32 v186, v2, v2\n"
    "v_mul_f32 v187, v3, v3\n"
    "v_mfma_f32_32x32x16_bf16 v[96:111], v[140:143], a[80:83], v[96:111]\n"
    "buffer_load_dwordx4 v198, s[20:23], s57 offen lds\n"
    "ds_read_b64_tr_b16 v[134:135], v193 offset:29888\n"
    "s_waitcnt lgkmcnt(6)\n"
    "v_mfma_f32_32x32x16_bf16 v[64:79], v[144:147], a[88:91], v[64:79]\n"
    "s_add_u32 m0, s56, 0x4200\n"
    "s_add_u32 s57, s57, 0x24000\n"
    "ds_read_b64_tr_b16 v[136:137], v193 offset:33792\n"
    "ds_read_b64_tr_b16 v[138:139], v193 offset:34048\n"
    "v_fmaak_f32 v184, v205, v184, 0xc0135761\n"
    "v_fmaak_f32 v185, v205, v185, 0xc0135761\n"
    "v_fmaak_f32 v186, v205, v186, 0xc0135761\n"
    "v_mfma_f32_32x32x16_bf16 v[80:95], v[144:147], a[92:95], v[80:95]\n"
    "buffer_load_dwordx4 v198, s[20:23], s57 offen lds\n"
    "v_mfma_f32_32x32x16_bf16 v[112:127], v[148:151], a[92:95], v[112:127]\n"
    "buffer_store_dwordx4 v[200:203], v209, s[44:47], s81 offen offset:0 sc1\n"
    "ds_read_b64_tr_b16 v[140:141], v193 offset:33856\n"
    "v_mfma_f32_32x32x16_bf16 v[96:111], v[148:151], a[88:91], v[96:111]\n"
    "ds_read_b64_tr_b16 v[142:143], v193 offset:34112\n"
    "v_fmaak_f32 v187, v205, v187, 0xc0135761\n"
    "s_waitcnt lgkmcnt(4)\n"
    "v_mfma_f32_32x32x16_bf16 v[64:79], v[128:131], a[96:99], v[64:79]\n"
    "s_add_u32 m0, s56, 0x5280\n"
    "s_add_u32 s57, s57, 0x24000\n"
    "ds_read_b64_tr_b16 v[144:145], v193 offset:38016\n"
    "ds_read_b64_tr_b16 v[146:147], v193 offset:38272\n"
    "v_mul_f32 v184, v0, v184\n"
    "v_mul_f32 v185, v1, v185\n"
    "v_mfma_f32_32x32x16_bf16 v[80:95], v[128:131], a[100:103], v[80:95]\n"
    "buffer_load_dwordx4 v198, s[20:23], s57 offen lds\n"
    "v_mfma_f32_32x32x16_bf16 v[112:127], v[132:135], a[100:103], v[112:127]\n"
    "s_add_u32 m0, s56, 0x6300\n"
    "s_add_u32 s57, s57, 0x24000\n"
    "s_xor_b32 s1, s0, 1\n"
    "s_xor_b32 s100, s0, 0\n"
    "s_sub_u32 s1, s1, s100\n"
    "s_sub_u32 s1, s1, 1\n"
    "s_mul_i32 s1, s1, 589824\n"
    "s_add_u32 s57, s57, s1\n"
    "ds_read_b64_tr_b16 v[148:149], v193 offset:38080\n"
    "v_mfma_f32_32x32x16_bf16 v[96:111], v[132:135], a[96:99], v[96:111]\n"
    "buffer_load_dwordx4 v198, s[20:23], s57 offen lds\n"
    "ds_read_b64_tr_b16 v[150:151], v193 offset:38336\n"
    "s_waitcnt lgkmcnt(4)\n"
    "v_mfma_f32_32x32x16_bf16 v[64:79], v[136:139], a[104:107], v[64:79]\n"
    "s_add_u32 m0, s56, 0xf780\n"
    "s_add_u32 s57, s57, 0x24000\n"
    "ds_read_b64_tr_b16 v[128:129], v193 offset:42240\n"
    "ds_read_b64_tr_b16 v[130:131], v193 offset:42496\n"
    "v_mul_f32 v186, v2, v186\n"
    "v_mul_f32 v187, v3, v187\n"
    "v_mfma_f32_32x32x16_bf16 v[80:95], v[136:139], a[108:111], v[80:95]\n"
    "buffer_load_dwordx4 v198, s[20:23], s57 offen lds\n"
    "v_mfma_f32_32x32x16_bf16 v[112:127], v[140:143], a[108:111], v[112:127]\n"
    "ds_read_b64_tr_b16 v[132:133], v193 offset:42304\n"
    "v_exp_f32 v184, v184\n"
    "v_exp_f32 v185, v185\n"
    "v_mfma_f32_32x32x16_bf16 v[96:111], v[140:143], a[104:107], v[96:111]\n"
    "ds_read_b64_tr_b16 v[134:135], v193 offset:42560\n"
    "v_exp_f32 v186, v186\n"
    "v_exp_f32 v187, v187\n"
    "s_waitcnt lgkmcnt(4)\n"
    "v_mfma_f32_32x32x16_bf16 v[64:79], v[144:147], a[112:115], v[64:79]\n"
    "s_add_u32 m0, s56, 0x10800\n"
    "s_add_u32 s57, s57, 0x24000\n"
    "ds_read_b64_tr_b16 v[136:137], v193 offset:46464\n"
    "ds_read_b64_tr_b16 v[138:139], v193 offset:46720\n"
    "v_mul_f32 v4, v0, v4\n"
    "v_mul_f32 v5, v1, v5\n"
    "v_mfma_f32_32x32x16_bf16 v[80:95], v[144:147], a[116:119], v[80:95]\n"
    "buffer_load_dwordx4 v198, s[20:23], s57 offen lds\n"
    "v_mfma_f32_32x32x16_bf16 v[112:127], v[148:151], a[116:119], v[112:127]\n"
    "s_add_u32 m0, s56, 0x1ad00\n"
    "s_add_u32 s57, s57, 0x24000\n"
    "ds_read_b64_tr_b16 v[140:141], v193 offset:46528\n"
    "v_mul_f32 v6, v2, v6\n"
    "v_mul_f32 v7, v3, v7\n"
    "v_add_f32 v184, 1.0, v184\n"
    "v_mfma_f32_32x32x16_bf16 v[96:111], v[148:151], a[112:115], v[96:111]\n"
    "buffer_load_dwordx4 v198, s[20:23], s57 offen lds\n"
    "ds_read_b64_tr_b16 v[142:143], v193 offset:46784\n"
    "s_waitcnt lgkmcnt(4)\n"
    "v_mfma_f32_32x32x16_bf16 v[64:79], v[128:131], a[120:123], v[64:79]\n"
    "s_add_u32 m0, s56, 0x1bd80\n"
    "s_add_u32 s57, s57, 0x24000\n"
    "s_xor_b32 s1, s0, 2\n"
    "s_xor_b32 s100, s0, 1\n"
    "s_sub_u32 s1, s1, s100\n"
    "s_sub_u32 s1, s1, 1\n"
    "s_mul_i32 s1, s1, 589824\n"
    "s_add_u32 s57, s57, s1\n"
    "ds_read_b64_tr_b16 v[144:145], v193 offset:50688\n"
    "ds_read_b64_tr_b16 v[146:147], v193 offset:50944\n"
    "v_mfma_f32_32x32x16_bf16 v[80:95], v[128:131], a[124:127], v[80:95]\n"
    "buffer_load_dwordx4 v198, s[20:23], s57 offen lds\n"
    "v_mfma_f32_32x32x16_bf16 v[112:127], v[132:135], a[124:127], v[112:127]\n"
    "ds_read_b64_tr_b16 v[148:149], v193 offset:50752\n"
    "v_add_f32 v185, 1.0, v185\n"
    "v_add_f32 v186, 1.0, v186\n"
    "v_add_f32 v187, 1.0, v187\n"
    "v_mfma_f32_32x32x16_bf16 v[96:111], v[132:135], a[120:123], v[96:111]\n"
    "ds_read_b64_tr_b16 v[150:151], v193 offset:51008\n"
    "v_rcp_f32 v184, v184\n"
    "v_rcp_f32 v185, v185\n"
    "s_waitcnt lgkmcnt(4)\n"
    "v_mfma_f32_32x32x16_bf16 v[64:79], v[136:139], a[128:131], v[64:79]\n"
    "buffer_store_dwordx4 v[200:203], v208, s[44:47], s82 offen offset:0 sc1\n"
    "ds_read_b64_tr_b16 v[128:129], v193 offset:54912\n"
    "ds_read_b64_tr_b16 v[130:131], v193 offset:55168\n"
    "s_add_u32 m0, s56, 0x1ce00\n"
    "s_add_u32 s57, s57, 0x24000\n"
    "v_mfma_f32_32x32x16_bf16 v[80:95], v[136:139], a[132:135], v[80:95]\n"
    "buffer_load_dwordx4 v198, s[20:23], s57 offen lds\n"
    "v_mfma_f32_32x32x16_bf16 v[112:127], v[140:143], a[132:135], v[112:127]\n"
    "s_add_u32 m0, s56, 0x1de80\n"
    "s_add_u32 s57, s57, 0x24000\n"
    "ds_read_b64_tr_b16 v[132:133], v193 offset:54976\n"
    "v_mfma_f32_32x32x16_bf16 v[96:111], v[140:143], a[128:131], v[96:111]\n"
    "buffer_load_dwordx4 v198, s[20:23], s57 offen lds\n"
    "ds_read_b64_tr_b16 v[134:135], v193 offset:55232\n"
    "s_waitcnt lgkmcnt(4)\n"
    "v_mfma_f32_32x32x16_bf16 v[64:79], v[144:147], a[136:139], v[64:79]\n"
    "s_add_u32 m0, s56, 0x21000\n"
    "s_add_u32 s57, s57, 0x24000\n"
    "ds_read_b64_tr_b16 v[136:137], v193 offset:59136\n"
    "ds_read_b64_tr_b16 v[138:139], v193 offset:59392\n"
    "v_rcp_f32 v186, v186\n"
    "v_mfma_f32_32x32x16_bf16 v[80:95], v[144:147], a[140:143], v[80:95]\n"
    "buffer_load_dwordx4 v198, s[20:23], s57 offen lds\n"
    "v_mfma_f32_32x32x16_bf16 v[112:127], v[148:151], a[140:143], v[112:127]\n"
    "ds_read_b64_tr_b16 v[140:141], v193 offset:59200\n"
    "v_rcp_f32 v187, v187\n"
    "v_mul_f32 v4, v4, v184\n"
    "v_mul_f32 v5, v5, v185\n"
    "v_mfma_f32_32x32x16_bf16 v[96:111], v[148:151], a[136:139], v[96:111]\n"
    "ds_read_b64_tr_b16 v[142:143], v193 offset:59456\n"
    "v_mul_f32 v6, v6, v186\n"
    "v_mul_f32 v7, v7, v187\n"
    "v_cvt_pk_bf16_f32 v0, v4, v5\n"
    "s_waitcnt lgkmcnt(4)\n"
    "v_mfma_f32_32x32x16_bf16 v[64:79], v[128:131], a[144:147], v[64:79]\n"
    "ds_read_b64_tr_b16 v[144:145], v195 offset:25216\n"
    "ds_read_b64_tr_b16 v[146:147], v195 offset:25472\n"
    "v_cvt_pk_bf16_f32 v1, v6, v7\n"
    "v_mul_f32 v188, v8, v8\n"
    "v_mul_f32 v189, v9, v9\n"
    "v_mfma_f32_32x32x16_bf16 v[80:95], v[128:131], a[148:151], v[80:95]\n"
    "v_mul_f32 v190, v10, v10\n"
    "v_mul_f32 v191, v11, v11\n"
    "v_fmaak_f32 v188, v205, v188, 0xc0135761\n"
    "v_fmaak_f32 v189, v205, v189, 0xc0135761\n"
    "v_fmaak_f32 v190, v205, v190, 0xc0135761\n"
    "v_mfma_f32_32x32x16_bf16 v[112:127], v[132:135], a[148:151], v[112:127]\n"
    "ds_read_b64_tr_b16 v[148:149], v195 offset:25280\n"
    "v_fmaak_f32 v191, v205, v191, 0xc0135761\n"
    "v_mul_f32 v188, v8, v188\n"
    "v_mul_f32 v189, v9, v189\n"
    "v_mul_f32 v190, v10, v190\n"
    "v_mfma_f32_32x32x16_bf16 v[96:111], v[132:135], a[144:147], v[96:111]\n"
    "buffer_store_dwordx4 v[200:203], v208, s[44:47], s82 offen offset:1024 sc1\n"
    "ds_read_b64_tr_b16 v[150:151], v195 offset:25536\n"
    "s_waitcnt lgkmcnt(4)\n"
    "v_mfma_f32_32x32x16_bf16 v[64:79], v[136:139], a[152:155], v[64:79]\n"
    "ds_read_b64_tr_b16 v[128:129], v195 offset:20992\n"
    "ds_read_b64_tr_b16 v[130:131], v195 offset:21248\n"
    "v_mfma_f32_32x32x16_bf16 v[80:95], v[136:139], a[156:159], v[80:95]\n"
    "v_mul_f32 v191, v11, v191\n"
    "v_exp_f32 v188, v188\n"
    "v_exp_f32 v189, v189\n"
    "v_mfma_f32_32x32x16_bf16 v[112:127], v[140:143], a[156:159], v[112:127]\n"
    "ds_read_b64_tr_b16 v[132:133], v195 offset:21056\n"
    "v_exp_f32 v190, v190\n"
    "v_mfma_f32_32x32x16_bf16 v[96:111], v[140:143], a[152:155], v[96:111]\n"
    "ds_read_b64_tr_b16 v[134:135], v195 offset:21312\n"
    "v_exp_f32 v191, v191\n"
    "v_mul_f32 v12, v8, v12\n"
    "v_mul_f32 v13, v9, v13\n"
    "s_waitcnt lgkmcnt(4)\n"
    "v_mfma_f32_32x32x16_bf16 v[64:79], v[144:147], a[160:163], v[64:79]\n"
    "ds_read_b64_tr_b16 v[136:137], v195 offset:16768\n"
    "ds_read_b64_tr_b16 v[138:139], v195 offset:17024\n"
    "v_mul_f32 v14, v10, v14\n"
    "v_mul_f32 v15, v11, v15\n"
    "v_add_f32 v188, 1.0, v188\n"
    "v_add_f32 v189, 1.0, v189\n"
    "v_mfma_f32_32x32x16_bf16 v[80:95], v[144:147], a[164:167], v[80:95]\n"
    "v_add_f32 v190, 1.0, v190\n"
    "v_add_f32 v191, 1.0, v191\n"
    "v_rcp_f32 v188, v188\n"
    "v_mfma_f32_32x32x16_bf16 v[112:127], v[148:151], a[164:167], v[112:127]\n"
    "ds_read_b64_tr_b16 v[140:141], v195 offset:16832\n"
    "v_rcp_f32 v189, v189\n"
    "v_rcp_f32 v190, v190\n"
    "v_mfma_f32_32x32x16_bf16 v[96:111], v[148:151], a[160:163], v[96:111]\n"
    "buffer_store_dwordx4 v[200:203], v209, s[44:47], s82 offen offset:0 sc1\n"
    "ds_read_b64_tr_b16 v[142:143], v195 offset:17088\n"
    "s_waitcnt lgkmcnt(4)\n"
    "v_mfma_f32_32x32x16_bf16 v[64:79], v[128:131], a[168:171], v[64:79]\n"
    "ds_read_b64_tr_b16 v[144:145], v194 offset:65408\n"
    "ds_read_b64_tr_b16 v[146:147], v195 offset:128\n"
    "v_mfma_f32_32x32x16_bf16 v[80:95], v[128:131], a[172:175], v[80:95]\n"
    "v_rcp_f32 v191, v191\n"
    "v_mul_f32 v12, v12, v188\n"
    "v_mul_f32 v13, v13, v189\n"
    "v_mul_f32 v14, v14, v190\n"
    "v_mfma_f32_32x32x16_bf16 v[112:127], v[132:135], a[172:175], v[112:127]\n"
    "ds_read_b64_tr_b16 v[148:149], v194 offset:65472\n"
    "v_mul_f32 v15, v15, v191\n"
    "v_cvt_pk_bf16_f32 v2, v12, v13\n"
    "v_cvt_pk_bf16_f32 v3, v14, v15\n"
    "v_mul_f32 v184, v16, v16\n"
    "v_mfma_f32_32x32x16_bf16 v[96:111], v[132:135], a[168:171], v[96:111]\n"
    "buffer_store_dwordx4 v[0:3], v210, s[24:27], s86 offen offset:0 sc1\n"
    "ds_read_b64_tr_b16 v[150:151], v195 offset:192\n"
    "s_waitcnt vmcnt(19)\n"
    "s_barrier\n"
    "s_mul_i32 s15, s10, 3\n"
    "s_add_u32 s15, s15, s12\n"
    "s_lshl_b32 s15, s15, 3\n"
    "s_add_u32 s15, s15, 256\n"
    "s_lshl_b32 s54, s11, 2\n"
    "s_add_u32 s54, s54, s12\n"
    "s_lshl_b32 s55, s9, 4\n"
    "s_add_u32 s54, s54, s55\n"
    "s_cmp_ge_u32 s54, 3\n"
    "s_cselect_b32 s15, 0x8000, s15\n"
    "v_lshl_add_u32 v225, v192, 24, s15\n"
    "v_mov_b32 v226, s48\n"
    "v_mov_b32 v227, s49\n"
    "buffer_store_dwordx2 v[226:227], v225, s[68:71], 0 offen sc1\n"
    "s_waitcnt lgkmcnt(4)\n"
    "v_mfma_f32_32x32x16_bf16 v[64:79], v[136:139], a[176:179], v[64:79]\n"
    "s_add_u32 m0, s56, 0x7380\n"
    "s_add_u32 s57, s57, 0x24000\n"
    "s_xor_b32 s1, s0, 3\n"
    "s_xor_b32 s100, s0, 2\n"
    "s_sub_u32 s1, s1, s100\n"
    "s_sub_u32 s1, s1, 1\n"
    "s_mul_i32 s1, s1, 589824\n"
    "s_add_u32 s57, s57, s1\n"
    "ds_read_b64_tr_b16 v[128:129], v194 offset:61184\n"
    "ds_read_b64_tr_b16 v[130:131], v194 offset:61440\n"
    "v_mfma_f32_32x32x16_bf16 v[80:95], v[136:139], a[180:183], v[80:95]\n"
    "buffer_load_dwordx4 v198, s[20:23], s57 offen lds\n"
    "v_mfma_f32_32x32x16_bf16 v[112:127], v[140:143], a[180:183], v[112:127]\n"
    "s_add_u32 m0, s56, 0x8400\n"
    "s_add_u32 s57, s57, 0x24000\n"
    "ds_read_b64_tr_b16 v[132:133], v194 offset:61248\n"
    "v_mfma_f32_32x32x16_bf16 v[96:111], v[140:143], a[176:179], v[96:111]\n"
    "buffer_load_dwordx4 v198, s[20:23], s57 offen lds\n"
    "ds_read_b64_tr_b16 v[134:135], v194 offset:61504\n"
    "s_waitcnt lgkmcnt(4)\n"
    "v_mfma_f32_32x32x16_bf16 v[64:79], v[144:147], a[184:187], v[64:79]\n"
    "s_add_u32 m0, s56, 0x9480\n"
    "s_add_u32 s57, s57, 0x24000\n"
    "ds_read_b64_tr_b16 v[136:137], v194 offset:40064\n"
    "ds_read_b64_tr_b16 v[138:139], v194 offset:40320\n"
    "v_mfma_f32_32x32x16_bf16 v[80:95], v[144:147], a[188:191], v[80:95]\n"
    "buffer_load_dwordx4 v198, s[20:23], s57 offen lds\n"
    "v_mfma_f32_32x32x16_bf16 v[112:127], v[148:151], a[188:191], v[112:127]\n"
    "ds_read_b64_tr_b16 v[140:141], v194 offset:40128\n"
    "v_mul_f32 v185, v17, v17\n"
    "v_mul_f32 v186, v18, v18\n"
    "v_mul_f32 v187, v19, v19\n"
    "v_mfma_f32_32x32x16_bf16 v[96:111], v[148:151], a[184:187], v[96:111]\n"
    "ds_read_b64_tr_b16 v[142:143], v194 offset:40384\n"
    "v_fmaak_f32 v184, v205, v184, 0xc0135761\n"
    "v_fmaak_f32 v185, v205, v185, 0xc0135761\n"
    "v_fmaak_f32 v186, v205, v186, 0xc0135761\n"
    "s_waitcnt lgkmcnt(4)\n"
    "v_mfma_f32_32x32x16_bf16 v[64:79], v[128:131], a[192:195], v[64:79]\n"
    "s_add_u32 m0, s56, 0xa500\n"
    "s_add_u32 s57, s57, 0x24000\n"
    "ds_read_b64_tr_b16 v[144:145], v194 offset:35840\n"
    "ds_read_b64_tr_b16 v[146:147], v194 offset:36096\n"
    "v_fmaak_f32 v187, v205, v187, 0xc0135761\n"
    "v_mul_f32 v184, v16, v184\n"
    "v_mul_f32 v185, v17, v185\n"
    "v_mfma_f32_32x32x16_bf16 v[80:95], v[128:131], a[196:199], v[80:95]\n"
    "buffer_load_dwordx4 v198, s[20:23], s57 offen lds\n"
    "v_mfma_f32_32x32x16_bf16 v[112:127], v[132:135], a[196:199], v[112:127]\n"
    "buffer_store_dwordx4 v[200:203], v208, s[44:47], s83 offen offset:0 sc1\n"
    "ds_read_b64_tr_b16 v[148:149], v194 offset:35904\n"
    "s_add_u32 m0, s56, 0xb580\n"
    "s_add_u32 s57, s57, 0x24000\n"
    "s_xor_b32 s1, s0, 4\n"
    "s_xor_b32 s100, s0, 3\n"
    "s_sub_u32 s1, s1, s100\n"
    "s_sub_u32 s1, s1, 1\n"
    "s_mul_i32 s1, s1, 589824\n"
    "s_add_u32 s57, s57, s1\n"
    "v_mfma_f32_32x32x16_bf16 v[96:111], v[132:135], a[192:195], v[96:111]\n"
    "buffer_load_dwordx4 v198, s[20:23], s57 offen lds\n"
    "ds_read_b64_tr_b16 v[150:151], v194 offset:36160\n"
    "s_waitcnt lgkmcnt(4)\n"
    "v_mfma_f32_32x32x16_bf16 v[64:79], v[136:139], a[200:203], v[64:79]\n"
    "s_add_u32 m0, s56, 0xc600\n"
    "s_add_u32 s57, s57, 0x24000\n"
    "ds_read_b64_tr_b16 v[128:129], v194 offset:31616\n"
    "ds_read_b64_tr_b16 v[130:131], v194 offset:31872\n"
    "v_mfma_f32_32x32x16_bf16 v[80:95], v[136:139], a[204:207], v[80:95]\n"
    "buffer_load_dwordx4 v198, s[20:23], s57 offen lds\n"
    "v_mfma_f32_32x32x16_bf16 v[112:127], v[140:143], a[204:207], v[112:127]\n"
    "ds_read_b64_tr_b16 v[132:133], v194 offset:31680\n"
    "v_mfma_f32_32x32x16_bf16 v[96:111], v[140:143], a[200:203], v[96:111]\n"
    "ds_read_b64_tr_b16 v[134:135], v194 offset:31936\n"
    "v_mul_f32 v186, v18, v186\n"
    "v_mul_f32 v187, v19, v187\n"
    "s_waitcnt lgkmcnt(4)\n"
    "v_mfma_f32_32x32x16_bf16 v[64:79], v[144:147], a[208:211], v[64:79]\n"
    "s_add_u32 m0, s56, 0xd680\n"
    "s_add_u32 s57, s57, 0x24000\n"
    "ds_read_b64_tr_b16 v[136:137], v194 offset:27392\n"
    "ds_read_b64_tr_b16 v[138:139], v194 offset:27648\n"
    "v_exp_f32 v184, v184\n"
    "v_mfma_f32_32x32x16_bf16 v[80:95], v[144:147], a[212:215], v[80:95]\n"
    "buffer_load_dwordx4 v198, s[20:23], s57 offen lds\n"
    "v_mfma_f32_32x32x16_bf16 v[112:127], v[148:151], a[212:215], v[112:127]\n"
    "s_add_u32 m0, s56, 0xe700\n"
    "s_add_u32 s57, s57, 0x24000\n"
    "ds_read_b64_tr_b16 v[140:141], v194 offset:27456\n"
    "v_exp_f32 v185, v185\n"
    "v_exp_f32 v186, v186\n"
    "v_mfma_f32_32x32x16_bf16 v[96:111], v[148:151], a[208:211], v[96:111]\n"
    "buffer_load_dwordx4 v198, s[20:23], s57 offen lds\n"
    "ds_read_b64_tr_b16 v[142:143], v194 offset:27712\n"
    "s_waitcnt lgkmcnt(4)\n"
    "v_mfma_f32_32x32x16_bf16 v[64:79], v[128:131], a[216:219], v[64:79]\n"
    "s_add_u32 m0, s56, 0x22080\n"
    "s_add_u32 s57, s57, 0x24000\n"
    "s_xor_b32 s1, s0, 5\n"
    "s_xor_b32 s100, s0, 4\n"
    "s_sub_u32 s1, s1, s100\n"
    "s_sub_u32 s1, s1, 1\n"
    "s_mul_i32 s1, s1, 589824\n"
    "s_add_u32 s57, s57, s1\n"
    "ds_read_b64_tr_b16 v[144:145], v194 offset:23168\n"
    "ds_read_b64_tr_b16 v[146:147], v194 offset:23424\n"
    "v_mfma_f32_32x32x16_bf16 v[80:95], v[128:131], a[220:223], v[80:95]\n"
    "buffer_load_dwordx4 v198, s[20:23], s57 offen lds\n"
    "v_mfma_f32_32x32x16_bf16 v[112:127], v[132:135], a[220:223], v[112:127]\n"
    "ds_read_b64_tr_b16 v[148:149], v194 offset:23232\n"
    "v_exp_f32 v187, v187\n"
    "v_mul_f32 v20, v16, v20\n"
    "v_mul_f32 v21, v17, v21\n"
    "v_mfma_f32_32x32x16_bf16 v[96:111], v[132:135], a[216:219], v[96:111]\n"
    "ds_read_b64_tr_b16 v[150:151], v194 offset:23488\n"
    "v_mul_f32 v22, v18, v22\n"
    "v_mul_f32 v23, v19, v23\n"
    "v_add_f32 v184, 1.0, v184\n"
    "v_add_f32 v185, 1.0, v185\n"
    "s_waitcnt lgkmcnt(4)\n"
    "v_mfma_f32_32x32x16_bf16 v[64:79], v[136:139], a[224:227], v[64:79]\n"
    "s_add_u32 m0, s56, 0x23100\n"
    "s_add_u32 s57, s57, 0x24000\n"
    "ds_read_b64_tr_b16 v[128:129], v194 offset:18944\n"
    "ds_read_b64_tr_b16 v[130:131], v194 offset:19200\n"
    "v_add_f32 v186, 1.0, v186\n"
    "v_add_f32 v187, 1.0, v187\n"
    "v_mfma_f32_32x32x16_bf16 v[80:95], v[136:139], a[228:231], v[80:95]\n"
    "buffer_load_dwordx4 v198, s[20:23], s57 offen lds\n"
    "v_mfma_f32_32x32x16_bf16 v[112:127], v[140:143], a[228:231], v[112:127]\n"
    "s_add_u32 m0, s56, 0x26280\n"
    "s_add_u32 s57, s57, 0x24000\n"
    "ds_read_b64_tr_b16 v[132:133], v194 offset:19008\n"
    "v_rcp_f32 v184, v184\n"
    "v_mfma_f32_32x32x16_bf16 v[96:111], v[140:143], a[224:227], v[96:111]\n"
    "buffer_load_dwordx4 v198, s[20:23], s57 offen lds\n"
    "ds_read_b64_tr_b16 v[134:135], v194 offset:19264\n"
    "s_waitcnt lgkmcnt(4)\n"
    "v_mfma_f32_32x32x16_bf16 v[64:79], v[144:147], a[232:235], v[64:79]\n"
    "s_add_u32 m0, s56, 0x25200\n"
    "s_add_u32 s57, s57, 0x24000\n"
    "ds_read_b64_tr_b16 v[136:137], v194 offset:14720\n"
    "ds_read_b64_tr_b16 v[138:139], v194 offset:14976\n"
    "v_rcp_f32 v185, v185\n"
    "v_mfma_f32_32x32x16_bf16 v[80:95], v[144:147], a[236:239], v[80:95]\n"
    "buffer_load_dwordx4 v198, s[20:23], s57 offen lds\n"
    "v_mfma_f32_32x32x16_bf16 v[112:127], v[148:151], a[236:239], v[112:127]\n"
    "ds_read_b64_tr_b16 v[140:141], v194 offset:14784\n"
    "v_rcp_f32 v186, v186\n"
    "v_rcp_f32 v187, v187\n"
    "v_mul_f32 v20, v20, v184\n"
    "v_mfma_f32_32x32x16_bf16 v[96:111], v[148:151], a[232:235], v[96:111]\n"
    "buffer_store_dwordx4 v[200:203], v208, s[44:47], s83 offen offset:1024 sc1\n"
    "ds_read_b64_tr_b16 v[142:143], v194 offset:15040\n"
    "s_waitcnt lgkmcnt(4)\n"
    "v_mfma_f32_32x32x16_bf16 v[64:79], v[128:131], a[240:243], v[64:79]\n"
    "ds_read_b64_tr_b16 v[144:145], v194 offset:10496\n"
    "ds_read_b64_tr_b16 v[146:147], v194 offset:10752\n"
    "v_mfma_f32_32x32x16_bf16 v[80:95], v[128:131], a[244:247], v[80:95]\n"
    "v_mul_f32 v21, v21, v185\n"
    "v_mul_f32 v22, v22, v186\n"
    "v_mul_f32 v23, v23, v187\n"
    "v_cvt_pk_bf16_f32 v16, v20, v21\n"
    "v_cvt_pk_bf16_f32 v17, v22, v23\n"
    "v_mfma_f32_32x32x16_bf16 v[112:127], v[132:135], a[244:247], v[112:127]\n"
    "ds_read_b64_tr_b16 v[148:149], v194 offset:10560\n"
    "v_mul_f32 v188, v24, v24\n"
    "v_mul_f32 v189, v25, v25\n"
    "v_mul_f32 v190, v26, v26\n"
    "v_mul_f32 v191, v27, v27\n"
    "v_mfma_f32_32x32x16_bf16 v[96:111], v[132:135], a[240:243], v[96:111]\n"
    "ds_read_b64_tr_b16 v[150:151], v194 offset:10816\n"
    "v_fmaak_f32 v188, v205, v188, 0xc0135761\n"
    "v_fmaak_f32 v189, v205, v189, 0xc0135761\n"
    "v_fmaak_f32 v190, v205, v190, 0xc0135761\n"
    "s_waitcnt lgkmcnt(4)\n"
    "v_mfma_f32_32x32x16_bf16 v[64:79], v[136:139], a[248:251], v[64:79]\n"
    "ds_read_b64_tr_b16 v[128:129], v194 offset:6272\n"
    "ds_read_b64_tr_b16 v[130:131], v194 offset:6528\n"
    "v_fmaak_f32 v191, v205, v191, 0xc0135761\n"
    "v_mul_f32 v188, v24, v188\n"
    "v_mul_f32 v189, v25, v189\n"
    "v_mfma_f32_32x32x16_bf16 v[80:95], v[136:139], a[252:255], v[80:95]\n"
    "v_mul_f32 v190, v26, v190\n"
    "v_mul_f32 v191, v27, v191\n"
    "v_exp_f32 v188, v188\n"
    "v_mfma_f32_32x32x16_bf16 v[112:127], v[140:143], a[252:255], v[112:127]\n"
    "buffer_store_dwordx4 v[200:203], v209, s[44:47], s83 offen offset:0 sc1\n"
    "ds_read_b64_tr_b16 v[132:133], v194 offset:6336\n"
    "v_mfma_f32_32x32x16_bf16 v[96:111], v[140:143], a[248:251], v[96:111]\n"
    "ds_read_b64_tr_b16 v[134:135], v194 offset:6592\n"
    "v_exp_f32 v189, v189\n"
    "s_waitcnt lgkmcnt(4)\n"
    "v_mfma_f32_32x32x16_bf16 v[64:79], v[144:147], v[152:155], v[64:79]\n"
    "ds_read_b64_tr_b16 v[136:137], v193 offset:0\n"
    "ds_read_b64_tr_b16 v[138:139], v193 offset:256\n"
    "v_exp_f32 v190, v190\n"
    "v_mfma_f32_32x32x16_bf16 v[80:95], v[144:147], v[156:159], v[80:95]\n"
    "v_exp_f32 v191, v191\n"
    "v_mul_f32 v28, v24, v28\n"
    "v_mul_f32 v29, v25, v29\n"
    "v_mul_f32 v30, v26, v30\n"
    "v_mul_f32 v31, v27, v31\n"
    "v_mfma_f32_32x32x16_bf16 v[112:127], v[148:151], v[156:159], v[112:127]\n"
    "ds_read_b64_tr_b16 v[140:141], v193 offset:64\n"
    "v_add_f32 v188, 1.0, v188\n"
    "v_add_f32 v189, 1.0, v189\n"
    "v_add_f32 v190, 1.0, v190\n"
    "v_add_f32 v191, 1.0, v191\n"
    "v_mfma_f32_32x32x16_bf16 v[96:111], v[148:151], v[152:155], v[96:111]\n"
    "ds_read_b64_tr_b16 v[142:143], v193 offset:320\n"
    "v_rcp_f32 v188, v188\n"
    "s_waitcnt lgkmcnt(4)\n"
    "v_mfma_f32_32x32x16_bf16 v[64:79], v[128:131], v[160:163], v[64:79]\n"
    "ds_read_b64_tr_b16 v[144:145], v193 offset:4224\n"
    "ds_read_b64_tr_b16 v[146:147], v193 offset:4480\n"
    "v_rcp_f32 v189, v189\n"
    "v_rcp_f32 v190, v190\n"
    "v_mfma_f32_32x32x16_bf16 v[80:95], v[128:131], v[164:167], v[80:95]\n"
    "v_rcp_f32 v191, v191\n"
    "v_mul_f32 v28, v28, v188\n"
    "v_mul_f32 v29, v29, v189\n"
    "v_mfma_f32_32x32x16_bf16 v[112:127], v[132:135], v[164:167], v[112:127]\n"
    "ds_read_b64_tr_b16 v[148:149], v193 offset:4288\n"
    "v_mul_f32 v30, v30, v190\n"
    "v_mul_f32 v31, v31, v191\n"
    "v_cvt_pk_bf16_f32 v18, v28, v29\n"
    "v_cvt_pk_bf16_f32 v19, v30, v31\n"
    "v_mfma_f32_32x32x16_bf16 v[96:111], v[132:135], v[160:163], v[96:111]\n"
    "buffer_store_dwordx4 v[16:19], v211, s[24:27], s86 offen offset:0 sc1\n"
    "ds_read_b64_tr_b16 v[150:151], v193 offset:4544\n"
    "s_waitcnt vmcnt(20)\n"
    "s_barrier\n"
    "s_waitcnt lgkmcnt(4)\n"
    "v_mfma_f32_32x32x16_bf16 v[64:79], v[136:139], v[168:171], v[64:79]\n"
    "s_add_u32 m0, s56, 0x24180\n"
    "s_add_u32 s57, s57, 0x24000\n"
    "s_xor_b32 s1, s0, 6\n"
    "s_xor_b32 s100, s0, 5\n"
    "s_sub_u32 s1, s1, s100\n"
    "s_sub_u32 s1, s1, 1\n"
    "s_mul_i32 s1, s1, 589824\n"
    "s_add_u32 s57, s57, s1\n"
    "ds_read_b64_tr_b16 v[128:129], v193 offset:8448\n"
    "ds_read_b64_tr_b16 v[130:131], v193 offset:8704\n"
    "v_mfma_f32_32x32x16_bf16 v[80:95], v[136:139], v[172:175], v[80:95]\n"
    "buffer_load_dwordx4 v198, s[20:23], s57 offen lds\n"
    "v_mfma_f32_32x32x16_bf16 v[112:127], v[140:143], v[172:175], v[112:127]\n"
    "s_add_u32 m0, s56, 0x1ff80\n"
    "s_add_u32 s57, s57, 0x24000\n"
    "ds_read_b64_tr_b16 v[132:133], v193 offset:8512\n"
    "v_mfma_f32_32x32x16_bf16 v[96:111], v[140:143], v[168:171], v[96:111]\n"
    "buffer_load_dwordx4 v198, s[20:23], s57 offen lds\n"
    "ds_read_b64_tr_b16 v[134:135], v193 offset:8768\n"
    "s_waitcnt lgkmcnt(4)\n"
    "v_mfma_f32_32x32x16_bf16 v[64:79], v[144:147], v[176:179], v[64:79]\n"
    "s_add_u32 m0, s56, 0x1ef00\n"
    "s_add_u32 s57, s57, 0x24000\n"
    "ds_read_b64_tr_b16 v[136:137], v193 offset:12672\n"
    "ds_read_b64_tr_b16 v[138:139], v193 offset:12928\n"
    "v_mfma_f32_32x32x16_bf16 v[80:95], v[144:147], v[180:183], v[80:95]\n"
    "buffer_load_dwordx4 v198, s[20:23], s57 offen lds\n"
    "v_mfma_f32_32x32x16_bf16 v[112:127], v[148:151], v[180:183], v[112:127]\n"
    "ds_read_b64_tr_b16 v[140:141], v193 offset:12736\n"
    "v_mfma_f32_32x32x16_bf16 v[96:111], v[148:151], v[176:179], v[96:111]\n"
    "ds_read_b64_tr_b16 v[142:143], v193 offset:12992\n"
    "s_waitcnt lgkmcnt(4)\n"
    "v_mfma_f32_32x32x16_bf16 v[0:15], v[128:131], a[0:3], 0\n"
    "s_add_u32 m0, s56, 0x19c80\n"
    "s_add_u32 s57, s57, 0x24000\n"
    "ds_read_b64_tr_b16 v[144:145], v193 offset:16896\n"
    "ds_read_b64_tr_b16 v[146:147], v193 offset:17152\n"
    "v_mfma_f32_32x32x16_bf16 v[16:31], v[128:131], a[4:7], 0\n"
    "buffer_load_dwordx4 v198, s[20:23], s57 offen lds\n"
    "v_mfma_f32_32x32x16_bf16 v[48:63], v[132:135], a[4:7], 0\n"
    "s_add_u32 m0, s56, 0x18c00\n"
    "s_add_u32 s57, s57, 0x24000\n"
    "s_xor_b32 s1, s0, 7\n"
    "s_xor_b32 s100, s0, 6\n"
    "s_sub_u32 s1, s1, s100\n"
    "s_sub_u32 s1, s1, 1\n"
    "s_mul_i32 s1, s1, 589824\n"
    "s_add_u32 s57, s57, s1\n"
    "ds_read_b64_tr_b16 v[148:149], v193 offset:16960\n"
    "v_mfma_f32_32x32x16_bf16 v[32:47], v[132:135], a[0:3], 0\n"
    "buffer_load_dwordx4 v198, s[20:23], s57 offen lds\n"
    "ds_read_b64_tr_b16 v[150:151], v193 offset:17216\n"
    "s_xor_b32 s86, s4, 2\n"
    "s_mul_i32 s86, s86, 8192\n"
    "s_add_u32 s86, s86, s6\n"
    "s_waitcnt lgkmcnt(4)\n"
    "v_mfma_f32_32x32x16_bf16 v[0:15], v[136:139], a[8:11], v[0:15]\n"
    "s_add_u32 m0, s56, 0x17b80\n"
    "s_add_u32 s57, s57, 0x24000\n"
    "ds_read_b64_tr_b16 v[128:129], v193 offset:21120\n"
    "ds_read_b64_tr_b16 v[130:131], v193 offset:21376\n"
    "v_mul_f32 v184, v64, v64\n"
    "v_mul_f32 v185, v65, v65\n"
    "v_mul_f32 v186, v66, v66\n"
    "v_mfma_f32_32x32x16_bf16 v[16:31], v[136:139], a[12:15], v[16:31]\n"
    "buffer_load_dwordx4 v198, s[20:23], s57 offen lds\n"
    "v_mfma_f32_32x32x16_bf16 v[48:63], v[140:143], a[12:15], v[48:63]\n"
    "ds_read_b64_tr_b16 v[132:133], v193 offset:21184\n"
    "v_mul_f32 v187, v67, v67\n"
    "v_fmaak_f32 v184, v205, v184, 0xc0135761\n"
    "v_fmaak_f32 v185, v205, v185, 0xc0135761\n"
    "v_mfma_f32_32x32x16_bf16 v[32:47], v[140:143], a[8:11], v[32:47]\n"
    "ds_read_b64_tr_b16 v[134:135], v193 offset:21440\n"
    "v_fmaak_f32 v186, v205, v186, 0xc0135761\n"
    "v_fmaak_f32 v187, v205, v187, 0xc0135761\n"
    "v_mul_f32 v184, v64, v184\n"
    "s_waitcnt lgkmcnt(4)\n"
    "v_mfma_f32_32x32x16_bf16 v[0:15], v[144:147], a[16:19], v[0:15]\n"
    "s_add_u32 m0, s56, 0x16b00\n"
    "s_add_u32 s57, s57, 0x24000\n"
    "ds_read_b64_tr_b16 v[136:137], v193 offset:25344\n"
    "ds_read_b64_tr_b16 v[138:139], v193 offset:25600\n"
    "v_mul_f32 v185, v65, v185\n"
    "v_mul_f32 v186, v66, v186\n"
    "v_mfma_f32_32x32x16_bf16 v[16:31], v[144:147], a[20:23], v[16:31]\n"
    "buffer_load_dwordx4 v198, s[20:23], s57 offen lds\n"
    "v_mfma_f32_32x32x16_bf16 v[48:63], v[148:151], a[20:23], v[48:63]\n"
    "s_add_u32 m0, s56, 0x15a80\n"
    "s_add_u32 s57, s57, 0x24000\n"
    "ds_read_b64_tr_b16 v[140:141], v193 offset:25408\n"
    "v_mul_f32 v187, v67, v187\n"
    "v_exp_f32 v184, v184\n"
    "v_mfma_f32_32x32x16_bf16 v[32:47], v[148:151], a[16:19], v[32:47]\n"
    "buffer_load_dwordx4 v198, s[20:23], s57 offen lds\n"
    "ds_read_b64_tr_b16 v[142:143], v193 offset:25664\n"
    "s_waitcnt lgkmcnt(4)\n"
    "v_mfma_f32_32x32x16_bf16 v[0:15], v[128:131], a[24:27], v[0:15]\n"
    "s_add_u32 m0, s56, 0x14a00\n"
    "s_add_u32 s57, s57, 0x24000\n"
    "s_mov_b32 s1, 8\n"
    "s_xor_b32 s100, s0, 7\n"
    "s_sub_u32 s1, s1, s100\n"
    "s_sub_u32 s1, s1, 1\n"
    "s_mul_i32 s1, s1, 589824\n"
    "s_add_u32 s57, s57, s1\n"
    "ds_read_b64_tr_b16 v[144:145], v193 offset:63360\n"
    "ds_read_b64_tr_b16 v[146:147], v193 offset:63616\n"
    "v_mfma_f32_32x32x16_bf16 v[16:31], v[128:131], a[28:31], v[16:31]\n"
    "buffer_load_dwordx4 v198, s[20:23], s57 offen lds\n"
    "v_mfma_f32_32x32x16_bf16 v[48:63], v[132:135], a[28:31], v[48:63]\n"
    "ds_read_b64_tr_b16 v[148:149], v193 offset:63424\n"
    "v_exp_f32 v185, v185\n"
    "v_mfma_f32_32x32x16_bf16 v[32:47], v[132:135], a[24:27], v[32:47]\n"
    "ds_read_b64_tr_b16 v[150:151], v193 offset:63680\n"
    "v_exp_f32 v186, v186\n"
    "v_exp_f32 v187, v187\n"
    "s_waitcnt lgkmcnt(4)\n"
    "v_mfma_f32_32x32x16_bf16 v[0:15], v[136:139], a[32:35], v[0:15]\n"
    "s_add_u32 m0, s56, 0x13980\n"
    "s_add_u32 s57, s57, 0x24000\n"
    "ds_read_b64_tr_b16 v[128:129], v194 offset:2048\n"
    "ds_read_b64_tr_b16 v[130:131], v194 offset:2304\n"
    "v_mul_f32 v68, v64, v68\n"
    "v_mul_f32 v69, v65, v69\n"
    "v_mfma_f32_32x32x16_bf16 v[16:31], v[136:139], a[36:39], v[16:31]\n"
    "buffer_load_dwordx4 v198, s[20:23], s57 offen lds\n"
    "v_mfma_f32_32x32x16_bf16 v[48:63], v[140:143], a[36:39], v[48:63]\n"
    "s_add_u32 m0, s56, 0x12900\n"
    "s_add_u32 s57, s57, 0x24000\n"
    "ds_read_b64_tr_b16 v[132:133], v194 offset:2112\n"
    "v_mul_f32 v70, v66, v70\n"
    "v_mul_f32 v71, v67, v71\n"
    "v_add_f32 v184, 1.0, v184\n"
    "v_mfma_f32_32x32x16_bf16 v[32:47], v[140:143], a[32:35], v[32:47]\n"
    "buffer_load_dwordx4 v198, s[20:23], s57 offen lds\n"
    "ds_read_b64_tr_b16 v[134:135], v194 offset:2368\n"
    "s_waitcnt lgkmcnt(4)\n"
    "v_mfma_f32_32x32x16_bf16 v[0:15], v[144:147], a[40:43], v[0:15]\n"
    "s_add_u32 m0, s56, 0x11880\n"
    "s_add_u32 s57, s57, 0x24000\n"
    "ds_read_b64_tr_b16 v[136:137], v194 offset:44288\n"
    "ds_read_b64_tr_b16 v[138:139], v194 offset:44544\n"
    "v_add_f32 v185, 1.0, v185\n"
    "v_add_f32 v186, 1.0, v186\n"
    "v_mfma_f32_32x32x16_bf16 v[16:31], v[144:147], a[44:47], v[16:31]\n"
    "buffer_load_dwordx4 v198, s[20:23], s57 offen lds\n"
    "v_mfma_f32_32x32x16_bf16 v[48:63], v[148:151], a[44:47], v[48:63]\n"
    "ds_read_b64_tr_b16 v[140:141], v194 offset:44352\n"
    "v_add_f32 v187, 1.0, v187\n"
    "v_rcp_f32 v184, v184\n"
    "v_mfma_f32_32x32x16_bf16 v[32:47], v[148:151], a[40:43], v[32:47]\n"
    "ds_read_b64_tr_b16 v[142:143], v194 offset:44608\n"
    "v_rcp_f32 v185, v185\n"
    "s_waitcnt lgkmcnt(4)\n"
    "v_mfma_f32_32x32x16_bf16 v[0:15], v[128:131], a[48:51], v[0:15]\n"
    "ds_read_b64_tr_b16 v[144:145], v194 offset:48512\n"
    "ds_read_b64_tr_b16 v[146:147], v194 offset:48768\n"
    "v_rcp_f32 v186, v186\n"
    "v_rcp_f32 v187, v187\n"
    "v_mfma_f32_32x32x16_bf16 v[16:31], v[128:131], a[52:55], v[16:31]\n"
    "v_mul_f32 v68, v68, v184\n"
    "v_mul_f32 v69, v69, v185\n"
    "v_mul_f32 v70, v70, v186\n"
    "v_mul_f32 v71, v71, v187\n"
    "v_mfma_f32_32x32x16_bf16 v[48:63], v[132:135], a[52:55], v[48:63]\n"
    "ds_read_b64_tr_b16 v[148:149], v194 offset:48576\n"
    "v_cvt_pk_bf16_f32 v64, v68, v69\n"
    "v_cvt_pk_bf16_f32 v65, v70, v71\n"
    "v_mul_f32 v188, v72, v72\n"
    "v_mul_f32 v189, v73, v73\n"
    "v_mfma_f32_32x32x16_bf16 v[32:47], v[132:135], a[48:51], v[32:47]\n"
    "ds_read_b64_tr_b16 v[150:151], v194 offset:48832\n"
    "v_mul_f32 v190, v74, v74\n"
    "v_mul_f32 v191, v75, v75\n"
    "s_waitcnt lgkmcnt(4)\n"
    "v_mfma_f32_32x32x16_bf16 v[0:15], v[136:139], a[56:59], v[0:15]\n"
    "ds_read_b64_tr_b16 v[128:129], v194 offset:52736\n"
    "ds_read_b64_tr_b16 v[130:131], v194 offset:52992\n"
    "v_fmaak_f32 v188, v205, v188, 0xc0135761\n"
    "v_fmaak_f32 v189, v205, v189, 0xc0135761\n"
    "v_fmaak_f32 v190, v205, v190, 0xc0135761\n"
    "v_mfma_f32_32x32x16_bf16 v[16:31], v[136:139], a[60:63], v[16:31]\n"
    "v_fmaak_f32 v191, v205, v191, 0xc0135761\n"
    "v_mul_f32 v188, v72, v188\n"
    "v_mul_f32 v189, v73, v189\n"
    "v_mul_f32 v190, v74, v190\n"
    "v_mfma_f32_32x32x16_bf16 v[48:63], v[140:143], a[60:63], v[48:63]\n"
    "ds_read_b64_tr_b16 v[132:133], v194 offset:52800\n"
    "v_mul_f32 v191, v75, v191\n"
    "v_exp_f32 v188, v188\n"
    "v_mfma_f32_32x32x16_bf16 v[32:47], v[140:143], a[56:59], v[32:47]\n"
    "ds_read_b64_tr_b16 v[134:135], v194 offset:53056\n"
    "v_exp_f32 v189, v189\n"
    "v_exp_f32 v190, v190\n"
    "s_waitcnt lgkmcnt(4)\n"
    "v_mfma_f32_32x32x16_bf16 v[0:15], v[144:147], a[64:67], v[0:15]\n"
    "ds_read_b64_tr_b16 v[136:137], v194 offset:56960\n"
    "ds_read_b64_tr_b16 v[138:139], v194 offset:57216\n"
    "v_exp_f32 v191, v191\n"
    "v_mfma_f32_32x32x16_bf16 v[16:31], v[144:147], a[68:71], v[16:31]\n"
    "v_mul_f32 v76, v72, v76\n"
    "v_mul_f32 v77, v73, v77\n"
    "v_mul_f32 v78, v74, v78\n"
    "v_mul_f32 v79, v75, v79\n"
    "v_add_f32 v188, 1.0, v188\n"
    "v_mfma_f32_32x32x16_bf16 v[48:63], v[148:151], a[68:71], v[48:63]\n"
    "ds_read_b64_tr_b16 v[140:141], v194 offset:57024\n"
    "v_min_u32 v225, 47, v192\n"
    "v_lshlrev_b32 v225, 3, v225\n"
    "v_add_u32 v225, 256, v225\n"
    "v_add_f32 v189, 1.0, v189\n"
    "v_mfma_f32_32x32x16_bf16 v[32:47], v[148:151], a[64:67], v[32:47]\n"
    "global_load_dwordx2 v[226:227], v225, s[50:51] sc1\n"
    "ds_read_b64_tr_b16 v[142:143], v194 offset:57280\n"
    "s_waitcnt lgkmcnt(4)\n"
    "v_mfma_f32_32x32x16_bf16 v[0:15], v[128:131], a[72:75], v[0:15]\n"
    "ds_read_b64_tr_b16 v[144:145], v195 offset:4096\n"
    "ds_read_b64_tr_b16 v[146:147], v195 offset:4352\n"
    "v_add_f32 v190, 1.0, v190\n"
    "v_add_f32 v191, 1.0, v191\n"
    "v_mfma_f32_32x32x16_bf16 v[16:31], v[128:131], a[76:79], v[16:31]\n"
    "v_rcp_f32 v188, v188\n"
    "v_rcp_f32 v189, v189\n"
    "v_mfma_f32_32x32x16_bf16 v[48:63], v[132:135], a[76:79], v[48:63]\n"
    "ds_read_b64_tr_b16 v[148:149], v195 offset:4160\n"
    "v_rcp_f32 v190, v190\n"
    "v_rcp_f32 v191, v191\n"
    "v_mfma_f32_32x32x16_bf16 v[32:47], v[132:135], a[72:75], v[32:47]\n"
    "ds_read_b64_tr_b16 v[150:151], v195 offset:4416\n"
    "v_mul_f32 v76, v76, v188\n"
    "s_waitcnt vmcnt(16)\n"
    "s_barrier\n"
    "s_waitcnt lgkmcnt(4)\n"
    "v_mfma_f32_32x32x16_bf16 v[0:15], v[136:139], a[80:83], v[0:15]\n"
    "ds_read_b64_tr_b16 v[128:129], v193 offset:29568\n"
    "ds_read_b64_tr_b16 v[130:131], v193 offset:29824\n"
    "v_mul_f32 v77, v77, v189\n"
    "v_mul_f32 v78, v78, v190\n"
    "v_mul_f32 v79, v79, v191\n"
    "v_mfma_f32_32x32x16_bf16 v[16:31], v[136:139], a[84:87], v[16:31]\n"
    "v_cvt_pk_bf16_f32 v66, v76, v77\n"
    "v_cvt_pk_bf16_f32 v67, v78, v79\n"
    "v_mul_f32 v184, v80, v80\n"
    "v_mul_f32 v185, v81, v81\n"
    "v_mfma_f32_32x32x16_bf16 v[48:63], v[140:143], a[84:87], v[48:63]\n"
    "buffer_store_dwordx4 v[64:67], v210, s[24:27], s86 offen offset:0 sc1\n"
    "ds_read_b64_tr_b16 v[132:133], v193 offset:29632\n"
    "v_mfma_f32_32x32x16_bf16 v[32:47], v[140:143], a[80:83], v[32:47]\n"
    "ds_read_b64_tr_b16 v[134:135], v193 offset:29888\n"
    "s_waitcnt lgkmcnt(4)\n"
    "v_mfma_f32_32x32x16_bf16 v[0:15], v[144:147], a[88:91], v[0:15]\n"
    "ds_read_b64_tr_b16 v[136:137], v193 offset:33792\n"
    "ds_read_b64_tr_b16 v[138:139], v193 offset:34048\n"
    "v_mul_f32 v186, v82, v82\n"
    "v_mul_f32 v187, v83, v83\n"
    "v_fmaak_f32 v184, v205, v184, 0xc0135761\n"
    "v_mfma_f32_32x32x16_bf16 v[16:31], v[144:147], a[92:95], v[16:31]\n"
    "v_fmaak_f32 v185, v205, v185, 0xc0135761\n"
    "v_fmaak_f32 v186, v205, v186, 0xc0135761\n"
    "v_fmaak_f32 v187, v205, v187, 0xc0135761\n"
    "v_mul_f32 v184, v80, v184\n"
    "v_mfma_f32_32x32x16_bf16 v[48:63], v[148:151], a[92:95], v[48:63]\n"
    "ds_read_b64_tr_b16 v[140:141], v193 offset:33856\n"
    "v_mul_f32 v185, v81, v185\n"
    "v_mul_f32 v186, v82, v186\n"
    "v_mul_f32 v187, v83, v187\n"
    "v_mfma_f32_32x32x16_bf16 v[32:47], v[148:151], a[88:91], v[32:47]\n"
    "ds_read_b64_tr_b16 v[142:143], v193 offset:34112\n"
    "v_exp_f32 v184, v184\n"
    "v_exp_f32 v185, v185\n"
    "s_waitcnt lgkmcnt(4)\n"
    "v_mfma_f32_32x32x16_bf16 v[0:15], v[128:131], a[96:99], v[0:15]\n"
    "ds_read_b64_tr_b16 v[144:145], v193 offset:38016\n"
    "ds_read_b64_tr_b16 v[146:147], v193 offset:38272\n"
    "v_exp_f32 v186, v186\n"
    "v_mfma_f32_32x32x16_bf16 v[16:31], v[128:131], a[100:103], v[16:31]\n"
    "v_exp_f32 v187, v187\n"
    "v_mul_f32 v84, v80, v84\n"
    "v_mul_f32 v85, v81, v85\n"
    "v_mul_f32 v86, v82, v86\n"
    "v_mfma_f32_32x32x16_bf16 v[48:63], v[132:135], a[100:103], v[48:63]\n"
    "ds_read_b64_tr_b16 v[148:149], v193 offset:38080\n"
    "v_mul_f32 v87, v83, v87\n"
    "v_add_f32 v184, 1.0, v184\n"
    "v_add_f32 v185, 1.0, v185\n"
    "v_mfma_f32_32x32x16_bf16 v[32:47], v[132:135], a[96:99], v[32:47]\n"
    "ds_read_b64_tr_b16 v[150:151], v193 offset:38336\n"
    "v_add_f32 v186, 1.0, v186\n"
    "v_add_f32 v187, 1.0, v187\n"
    "s_waitcnt lgkmcnt(4)\n"
    "v_mfma_f32_32x32x16_bf16 v[0:15], v[136:139], a[104:107], v[0:15]\n"
    "ds_read_b64_tr_b16 v[128:129], v193 offset:42240\n"
    "ds_read_b64_tr_b16 v[130:131], v193 offset:42496\n"
    "v_rcp_f32 v184, v184\n"
    "v_rcp_f32 v185, v185\n"
    "v_mfma_f32_32x32x16_bf16 v[16:31], v[136:139], a[108:111], v[16:31]\n"
    "v_rcp_f32 v186, v186\n"
    "v_rcp_f32 v187, v187\n"
    "v_mfma_f32_32x32x16_bf16 v[48:63], v[140:143], a[108:111], v[48:63]\n"
    "ds_read_b64_tr_b16 v[132:133], v193 offset:42304\n"
    "v_mul_f32 v84, v84, v184\n"
    "v_mul_f32 v85, v85, v185\n"
    "v_mul_f32 v86, v86, v186\n"
    "v_mfma_f32_32x32x16_bf16 v[32:47], v[140:143], a[104:107], v[32:47]\n"
    "ds_read_b64_tr_b16 v[134:135], v193 offset:42560\n"
    "v_mul_f32 v87, v87, v187\n"
    "v_cvt_pk_bf16_f32 v80, v84, v85\n"
    "v_cvt_pk_bf16_f32 v81, v86, v87\n"
    "s_waitcnt lgkmcnt(4)\n"
    "v_mfma_f32_32x32x16_bf16 v[0:15], v[144:147], a[112:115], v[0:15]\n"
    "ds_read_b64_tr_b16 v[136:137], v193 offset:46464\n"
    "ds_read_b64_tr_b16 v[138:139], v193 offset:46720\n"
    "v_mul_f32 v188, v88, v88\n"
    "v_mul_f32 v189, v89, v89\n"
    "v_mul_f32 v190, v90, v90\n"
    "v_mfma_f32_32x32x16_bf16 v[16:31], v[144:147], a[116:119], v[16:31]\n"
    "v_mul_f32 v191, v91, v91\n"
    "v_fmaak_f32 v188, v205, v188, 0xc0135761\n"
    "v_fmaak_f32 v189, v205, v189, 0xc0135761\n"
    "v_fmaak_f32 v190, v205, v190, 0xc0135761\n"
    "v_mfma_f32_32x32x16_bf16 v[48:63], v[148:151], a[116:119], v[48:63]\n"
    "ds_read_b64_tr_b16 v[140:141], v193 offset:46528\n"
    "v_fmaak_f32 v191, v205, v191, 0xc0135761\n"
    "v_mul_f32 v188, v88, v188\n"
    "v_mul_f32 v189, v89, v189\n"
    "v_mul_f32 v190, v90, v190\n"
    "v_mfma_f32_32x32x16_bf16 v[32:47], v[148:151], a[112:115], v[32:47]\n"
    "ds_read_b64_tr_b16 v[142:143], v193 offset:46784\n"
    "v_mul_f32 v191, v91, v191\n"
    "s_waitcnt lgkmcnt(4)\n"
    "v_mfma_f32_32x32x16_bf16 v[0:15], v[128:131], a[120:123], v[0:15]\n"
    "ds_read_b64_tr_b16 v[144:145], v193 offset:50688\n"
    "ds_read_b64_tr_b16 v[146:147], v193 offset:50944\n"
    "v_exp_f32 v188, v188\n"
    "v_exp_f32 v189, v189\n"
    "v_mfma_f32_32x32x16_bf16 v[16:31], v[128:131], a[124:127], v[16:31]\n"
    "v_exp_f32 v190, v190\n"
    "v_exp_f32 v191, v191\n"
    "v_mfma_f32_32x32x16_bf16 v[48:63], v[132:135], a[124:127], v[48:63]\n"
    "ds_read_b64_tr_b16 v[148:149], v193 offset:50752\n"
    "v_mul_f32 v92, v88, v92\n"
    "v_mul_f32 v93, v89, v93\n"
    "v_mul_f32 v94, v90, v94\n"
    "v_mul_f32 v95, v91, v95\n"
    "v_mfma_f32_32x32x16_bf16 v[32:47], v[132:135], a[120:123], v[32:47]\n"
    "ds_read_b64_tr_b16 v[150:151], v193 offset:51008\n"
    "v_add_f32 v188, 1.0, v188\n"
    "v_add_f32 v189, 1.0, v189\n"
    "v_add_f32 v190, 1.0, v190\n"
    "s_waitcnt lgkmcnt(4)\n"
    "v_mfma_f32_32x32x16_bf16 v[0:15], v[136:139], a[128:131], v[0:15]\n"
    "ds_read_b64_tr_b16 v[128:129], v193 offset:54912\n"
    "ds_read_b64_tr_b16 v[130:131], v193 offset:55168\n"
    "v_add_f32 v191, 1.0, v191\n"
    "v_mfma_f32_32x32x16_bf16 v[16:31], v[136:139], a[132:135], v[16:31]\n"
    "v_rcp_f32 v188, v188\n"
    "v_rcp_f32 v189, v189\n"
    "v_rcp_f32 v190, v190\n"
    "v_mfma_f32_32x32x16_bf16 v[48:63], v[140:143], a[132:135], v[48:63]\n"
    "ds_read_b64_tr_b16 v[132:133], v193 offset:54976\n"
    "v_rcp_f32 v191, v191\n"
    "v_mul_f32 v92, v92, v188\n"
    "v_mfma_f32_32x32x16_bf16 v[32:47], v[140:143], a[128:131], v[32:47]\n"
    "ds_read_b64_tr_b16 v[134:135], v193 offset:55232\n"
    "v_mul_f32 v93, v93, v189\n"
    "v_mul_f32 v94, v94, v190\n"
    "v_mul_f32 v95, v95, v191\n"
    "s_waitcnt lgkmcnt(4)\n"
    "v_mfma_f32_32x32x16_bf16 v[0:15], v[144:147], a[136:139], v[0:15]\n"
    "ds_read_b64_tr_b16 v[136:137], v193 offset:59136\n"
    "ds_read_b64_tr_b16 v[138:139], v193 offset:59392\n"
    "v_cvt_pk_bf16_f32 v82, v92, v93\n"
    "v_cvt_pk_bf16_f32 v83, v94, v95\n"
    "v_mul_f32 v184, v96, v96\n"
    "v_mfma_f32_32x32x16_bf16 v[16:31], v[144:147], a[140:143], v[16:31]\n"
    "buffer_store_dwordx4 v[80:83], v211, s[24:27], s86 offen offset:0 sc1\n"
    "v_mfma_f32_32x32x16_bf16 v[48:63], v[148:151], a[140:143], v[48:63]\n"
    "ds_read_b64_tr_b16 v[140:141], v193 offset:59200\n"
    "v_mul_f32 v185, v97, v97\n"
    "v_mfma_f32_32x32x16_bf16 v[32:47], v[148:151], a[136:139], v[32:47]\n"
    "ds_read_b64_tr_b16 v[142:143], v193 offset:59456\n"
    "v_mul_f32 v186, v98, v98\n"
    "v_mul_f32 v187, v99, v99\n"
    "v_fmaak_f32 v184, v205, v184, 0xc0135761\n"
    "s_waitcnt lgkmcnt(4)\n"
    "v_mfma_f32_32x32x16_bf16 v[0:15], v[128:131], a[144:147], v[0:15]\n"
    "ds_read_b64_tr_b16 v[144:145], v195 offset:8320\n"
    "ds_read_b64_tr_b16 v[146:147], v195 offset:8576\n"
    "v_fmaak_f32 v185, v205, v185, 0xc0135761\n"
    "v_fmaak_f32 v186, v205, v186, 0xc0135761\n"
    "v_fmaak_f32 v187, v205, v187, 0xc0135761\n"
    "v_mfma_f32_32x32x16_bf16 v[16:31], v[128:131], a[148:151], v[16:31]\n"
    "v_mul_f32 v184, v96, v184\n"
    "v_mul_f32 v185, v97, v185\n"
    "v_mul_f32 v186, v98, v186\n"
    "v_mul_f32 v187, v99, v187\n"
    "v_mfma_f32_32x32x16_bf16 v[48:63], v[132:135], a[148:151], v[48:63]\n"
    "ds_read_b64_tr_b16 v[148:149], v195 offset:8384\n"
    "v_exp_f32 v184, v184\n"
    "v_mfma_f32_32x32x16_bf16 v[32:47], v[132:135], a[144:147], v[32:47]\n"
    "ds_read_b64_tr_b16 v[150:151], v195 offset:8640\n"
    "v_exp_f32 v185, v185\n"
    "v_exp_f32 v186, v186\n"
    "s_waitcnt lgkmcnt(4)\n"
    "v_mfma_f32_32x32x16_bf16 v[0:15], v[136:139], a[152:155], v[0:15]\n"
    "ds_read_b64_tr_b16 v[128:129], v195 offset:12544\n"
    "ds_read_b64_tr_b16 v[130:131], v195 offset:12800\n"
    "v_exp_f32 v187, v187\n"
    "v_mul_f32 v100, v96, v100\n"
    "v_mfma_f32_32x32x16_bf16 v[16:31], v[136:139], a[156:159], v[16:31]\n"
    "v_mul_f32 v101, v97, v101\n"
    "v_mul_f32 v102, v98, v102\n"
    "v_mul_f32 v103, v99, v103\n"
    "v_add_f32 v184, 1.0, v184\n"
    "v_mfma_f32_32x32x16_bf16 v[48:63], v[140:143], a[156:159], v[48:63]\n"
    "ds_read_b64_tr_b16 v[132:133], v195 offset:12608\n"
    "v_add_f32 v185, 1.0, v185\n"
    "v_add_f32 v186, 1.0, v186\n"
    "v_add_f32 v187, 1.0, v187\n"
    "v_mfma_f32_32x32x16_bf16 v[32:47], v[140:143], a[152:155], v[32:47]\n"
    "ds_read_b64_tr_b16 v[134:135], v195 offset:12864\n"
    "v_rcp_f32 v184, v184\n"
    "s_waitcnt lgkmcnt(4)\n"
    "v_mfma_f32_32x32x16_bf16 v[0:15], v[144:147], a[160:163], v[0:15]\n"
    "ds_read_b64_tr_b16 v[136:137], v195 offset:25216\n"
    "ds_read_b64_tr_b16 v[138:139], v195 offset:25472\n"
    "v_rcp_f32 v185, v185\n"
    "v_rcp_f32 v186, v186\n"
    "v_mfma_f32_32x32x16_bf16 v[16:31], v[144:147], a[164:167], v[16:31]\n"
    "v_rcp_f32 v187, v187\n"
    "v_mul_f32 v100, v100, v184\n"
    "v_mul_f32 v101, v101, v185\n"
    "v_mfma_f32_32x32x16_bf16 v[48:63], v[148:151], a[164:167], v[48:63]\n"
    "ds_read_b64_tr_b16 v[140:141], v195 offset:25280\n"
    "v_mul_f32 v102, v102, v186\n"
    "v_mul_f32 v103, v103, v187\n"
    "v_cvt_pk_bf16_f32 v96, v100, v101\n"
    "v_cvt_pk_bf16_f32 v97, v102, v103\n"
    "v_mfma_f32_32x32x16_bf16 v[32:47], v[148:151], a[160:163], v[32:47]\n"
    "ds_read_b64_tr_b16 v[142:143], v195 offset:25536\n"
    "s_waitcnt vmcnt(2)\n"
    "v_cmp_ne_u64 s[54:55], s[48:49], v[226:227]\n"
    "s_nop 1\n"
    "s_cmp_eq_u64 s[54:55], 0\n"
    "s_cbranch_scc0 .Lpoll0\n"
    ".Lpollok0:\n"
    "s_waitcnt lgkmcnt(4)\n"
    "v_mfma_f32_32x32x16_bf16 v[0:15], v[128:131], a[168:171], v[0:15]\n"
    "s_add_u32 m0, s60, 0x0\n"
    "ds_read_b64_tr_b16 v[144:145], v195 offset:20992\n"
    "ds_read_b64_tr_b16 v[146:147], v195 offset:21248\n"
    "v_mul_f32 v188, v104, v104\n"
    "v_mul_f32 v189, v105, v105\n"
    "v_mul_f32 v190, v106, v106\n"
    "v_mfma_f32_32x32x16_bf16 v[16:31], v[128:131], a[172:175], v[16:31]\n"
    "buffer_load_dwordx4 v246, s[28:31], s61 offen offset:0 sc1 lds\n"
    "v_mfma_f32_32x32x16_bf16 v[48:63], v[132:135], a[172:175], v[48:63]\n"
    "ds_read_b64_tr_b16 v[148:149], v195 offset:21056\n"
    "v_mul_f32 v191, v107, v107\n"
    "v_fmaak_f32 v188, v205, v188, 0xc0135761\n"
    "v_fmaak_f32 v189, v205, v189, 0xc0135761\n"
    "v_mfma_f32_32x32x16_bf16 v[32:47], v[132:135], a[168:171], v[32:47]\n"
    "buffer_load_dwordx4 v246, s[28:31], s61 offen offset:1024 sc1 lds\n"
    "ds_read_b64_tr_b16 v[150:151], v195 offset:21312\n"
    "s_barrier\n"
    "s_waitcnt lgkmcnt(4)\n"
    "v_mfma_f32_32x32x16_bf16 v[0:15], v[136:139], a[176:179], v[0:15]\n"
    "ds_read_b64_tr_b16 v[128:129], v195 offset:16768\n"
    "ds_read_b64_tr_b16 v[130:131], v195 offset:17024\n"
    "v_fmaak_f32 v190, v205, v190, 0xc0135761\n"
    "v_fmaak_f32 v191, v205, v191, 0xc0135761\n"
    "v_mul_f32 v188, v104, v188\n"
    "v_mfma_f32_32x32x16_bf16 v[16:31], v[136:139], a[180:183], v[16:31]\n"
    "buffer_load_dwordx4 v246, s[28:31], s61 offen offset:2048 sc1 lds\n"
    "v_mfma_f32_32x32x16_bf16 v[48:63], v[140:143], a[180:183], v[48:63]\n"
    "s_add_u32 m0, s60, 0x3180\n"
    "s_add_u32 s61, s61, 0x3000\n"
    "ds_read_b64_tr_b16 v[132:133], v195 offset:16832\n"
    "v_mul_f32 v189, v105, v189\n"
    "v_mul_f32 v190, v106, v190\n"
    "v_mul_f32 v191, v107, v191\n"
    "v_mfma_f32_32x32x16_bf16 v[32:47], v[140:143], a[176:179], v[32:47]\n"
    "buffer_load_dwordx4 v246, s[28:31], s61 offen offset:0 sc1 lds\n"
    "ds_read_b64_tr_b16 v[134:135], v195 offset:17088\n"
    "s_waitcnt lgkmcnt(4)\n"
    "v_mfma_f32_32x32x16_bf16 v[0:15], v[144:147], a[184:187], v[0:15]\n"
    "ds_read_b64_tr_b16 v[136:137], v194 offset:65408\n"
    "ds_read_b64_tr_b16 v[138:139], v195 offset:128\n"
    "v_exp_f32 v188, v188\n"
    "v_mfma_f32_32x32x16_bf16 v[16:31], v[144:147], a[188:191], v[16:31]\n"
    "buffer_load_dwordx4 v246, s[28:31], s61 offen offset:1024 sc1 lds\n"
    "v_mfma_f32_32x32x16_bf16 v[48:63], v[148:151], a[188:191], v[48:63]\n"
    "ds_read_b64_tr_b16 v[140:141], v194 offset:65472\n"
    "v_exp_f32 v189, v189\n"
    "v_exp_f32 v190, v190\n"
    "v_mfma_f32_32x32x16_bf16 v[32:47], v[148:151], a[184:187], v[32:47]\n"
    "buffer_load_dwordx4 v246, s[28:31], s61 offen offset:2048 sc1 lds\n"
    "ds_read_b64_tr_b16 v[142:143], v195 offset:192\n"
    "s_waitcnt lgkmcnt(4)\n"
    "v_mfma_f32_32x32x16_bf16 v[0:15], v[128:131], a[192:195], v[0:15]\n"
    "s_add_u32 m0, s60, 0x6300\n"
    "s_add_u32 s61, s61, 0x3000\n"
    "ds_read_b64_tr_b16 v[144:145], v194 offset:61184\n"
    "ds_read_b64_tr_b16 v[146:147], v194 offset:61440\n"
    "v_exp_f32 v191, v191\n"
    "v_mfma_f32_32x32x16_bf16 v[16:31], v[128:131], a[196:199], v[16:31]\n"
    "buffer_load_dwordx4 v246, s[28:31], s61 offen offset:0 sc1 lds\n"
    "v_mfma_f32_32x32x16_bf16 v[48:63], v[132:135], a[196:199], v[48:63]\n"
    "ds_read_b64_tr_b16 v[148:149], v194 offset:61248\n"
    "v_mul_f32 v108, v104, v108\n"
    "v_mul_f32 v109, v105, v109\n"
    "v_mul_f32 v110, v106, v110\n"
    "v_mul_f32 v111, v107, v111\n"
    "v_mfma_f32_32x32x16_bf16 v[32:47], v[132:135], a[192:195], v[32:47]\n"
    "buffer_load_dwordx4 v246, s[28:31], s61 offen offset:1024 sc1 lds\n"
    "ds_read_b64_tr_b16 v[150:151], v194 offset:61504\n"
    "s_waitcnt lgkmcnt(4)\n"
    "v_mfma_f32_32x32x16_bf16 v[0:15], v[136:139], a[200:203], v[0:15]\n"
    "ds_read_b64_tr_b16 v[128:129], v194 offset:40064\n"
    "ds_read_b64_tr_b16 v[130:131], v194 offset:40320\n"
    "v_add_f32 v188, 1.0, v188\n"
    "v_add_f32 v189, 1.0, v189\n"
    "v_mfma_f32_32x32x16_bf16 v[16:31], v[136:139], a[204:207], v[16:31]\n"
    "buffer_load_dwordx4 v246, s[28:31], s61 offen offset:2048 sc1 lds\n"
    "v_mfma_f32_32x32x16_bf16 v[48:63], v[140:143], a[204:207], v[48:63]\n"
    "s_add_u32 m0, s60, 0x9480\n"
    "s_add_u32 s61, s61, 0x3000\n"
    "ds_read_b64_tr_b16 v[132:133], v194 offset:40128\n"
    "v_add_f32 v190, 1.0, v190\n"
    "v_add_f32 v191, 1.0, v191\n"
    "v_mfma_f32_32x32x16_bf16 v[32:47], v[140:143], a[200:203], v[32:47]\n"
    "buffer_load_dwordx4 v246, s[28:31], s61 offen offset:0 sc1 lds\n"
    "ds_read_b64_tr_b16 v[134:135], v194 offset:40384\n"
    "s_waitcnt lgkmcnt(4)\n"
    "v_mfma_f32_32x32x16_bf16 v[0:15], v[144:147], a[208:211], v[0:15]\n"
    "ds_read_b64_tr_b16 v[136:137], v194 offset:35840\n"
    "ds_read_b64_tr_b16 v[138:139], v194 offset:36096\n"
    "v_rcp_f32 v188, v188\n"
    "v_rcp_f32 v189, v189\n"
    "v_mfma_f32_32x32x16_bf16 v[16:31], v[144:147], a[212:215], v[16:31]\n"
    "buffer_load_dwordx4 v246, s[28:31], s61 offen offset:1024 sc1 lds\n"
    "v_mfma_f32_32x32x16_bf16 v[48:63], v[148:151], a[212:215], v[48:63]\n"
    "ds_read_b64_tr_b16 v[140:141], v194 offset:35904\n"
    "v_rcp_f32 v190, v190\n"
    "v_mfma_f32_32x32x16_bf16 v[32:47], v[148:151], a[208:211], v[32:47]\n"
    "buffer_load_dwordx4 v246, s[28:31], s61 offen offset:2048 sc1 lds\n"
    "ds_read_b64_tr_b16 v[142:143], v194 offset:36160\n"
    "s_waitcnt lgkmcnt(4)\n"
    "v_mfma_f32_32x32x16_bf16 v[0:15], v[128:131], a[216:219], v[0:15]\n"
    "s_add_u32 m0, s60, 0xc600\n"
    "s_add_u32 s61, s61, 0x3000\n"
    "ds_read_b64_tr_b16 v[144:145], v194 offset:31616\n"
    "ds_read_b64_tr_b16 v[146:147], v194 offset:31872\n"
    "v_rcp_f32 v191, v191\n"
    "v_mul_f32 v108, v108, v188\n"
    "v_mfma_f32_32x32x16_bf16 v[16:31], v[128:131], a[220:223], v[16:31]\n"
    "buffer_load_dwordx4 v246, s[28:31], s61 offen offset:0 sc1 lds\n"
    "v_mfma_f32_32x32x16_bf16 v[48:63], v[132:135], a[220:223], v[48:63]\n"
    "ds_read_b64_tr_b16 v[148:149], v194 offset:31680\n"
    "v_mul_f32 v109, v109, v189\n"
    "v_mul_f32 v110, v110, v190\n"
    "v_mul_f32 v111, v111, v191\n"
    "v_cvt_pk_bf16_f32 v98, v108, v109\n"
    "v_mfma_f32_32x32x16_bf16 v[32:47], v[132:135], a[216:219], v[32:47]\n"
    "buffer_load_dwordx4 v246, s[28:31], s61 offen offset:1024 sc1 lds\n"
    "ds_read_b64_tr_b16 v[150:151], v194 offset:31936\n"
    "s_waitcnt lgkmcnt(4)\n"
    "v_mfma_f32_32x32x16_bf16 v[0:15], v[136:139], a[224:227], v[0:15]\n"
    "ds_read_b64_tr_b16 v[128:129], v194 offset:27392\n"
    "ds_read_b64_tr_b16 v[130:131], v194 offset:27648\n"
    "v_cvt_pk_bf16_f32 v99, v110, v111\n"
    "v_mul_f32 v184, v112, v112\n"
    "v_mul_f32 v185, v113, v113\n"
    "v_mfma_f32_32x32x16_bf16 v[16:31], v[136:139], a[228:231], v[16:31]\n"
    "buffer_load_dwordx4 v246, s[28:31], s61 offen offset:2048 sc1 lds\n"
    "v_mfma_f32_32x32x16_bf16 v[48:63], v[140:143], a[228:231], v[48:63]\n"
    "buffer_store_dwordx4 v[96:99], v210, s[24:27], s86 offen offset:1024 sc1\n"
    "ds_read_b64_tr_b16 v[132:133], v194 offset:27456\n"
    "v_mfma_f32_32x32x16_bf16 v[32:47], v[140:143], a[224:227], v[32:47]\n"
    "ds_read_b64_tr_b16 v[134:135], v194 offset:27712\n"
    "s_waitcnt lgkmcnt(4)\n"
    "v_mfma_f32_32x32x16_bf16 v[0:15], v[144:147], a[232:235], v[0:15]\n"
    "ds_read_b64_tr_b16 v[136:137], v194 offset:23168\n"
    "ds_read_b64_tr_b16 v[138:139], v194 offset:23424\n"
    "v_mul_f32 v186, v114, v114\n"
    "v_mul_f32 v187, v115, v115\n"
    "v_fmaak_f32 v184, v205, v184, 0xc0135761\n"
    "v_mfma_f32_32x32x16_bf16 v[16:31], v[144:147], a[236:239], v[16:31]\n"
    "v_fmaak_f32 v185, v205, v185, 0xc0135761\n"
    "v_fmaak_f32 v186, v205, v186, 0xc0135761\n"
    "v_fmaak_f32 v187, v205, v187, 0xc0135761\n"
    "v_mul_f32 v184, v112, v184\n"
    "v_mfma_f32_32x32x16_bf16 v[48:63], v[148:151], a[236:239], v[48:63]\n"
    "ds_read_b64_tr_b16 v[140:141], v194 offset:23232\n"
    "v_mul_f32 v185, v113, v185\n"
    "v_mul_f32 v186, v114, v186\n"
    "v_mul_f32 v187, v115, v187\n"
    "v_mfma_f32_32x32x16_bf16 v[32:47], v[148:151], a[232:235], v[32:47]\n"
    "ds_read_b64_tr_b16 v[142:143], v194 offset:23488\n"
    "v_exp_f32 v184, v184\n"
    "s_waitcnt lgkmcnt(4)\n"
    "v_mfma_f32_32x32x16_bf16 v[0:15], v[128:131], a[240:243], v[0:15]\n"
    "ds_read_b64_tr_b16 v[144:145], v194 offset:18944\n"
    "ds_read_b64_tr_b16 v[146:147], v194 offset:19200\n"
    "v_exp_f32 v185, v185\n"
    "v_exp_f32 v186, v186\n"
    "v_mfma_f32_32x32x16_bf16 v[16:31], v[128:131], a[244:247], v[16:31]\n"
    "v_exp_f32 v187, v187\n"
    "v_mul_f32 v116, v112, v116\n"
    "v_mul_f32 v117, v113, v117\n"
    "v_mfma_f32_32x32x16_bf16 v[48:63], v[132:135], a[244:247], v[48:63]\n"
    "ds_read_b64_tr_b16 v[148:149], v194 offset:19008\n"
    "v_mul_f32 v118, v114, v118\n"
    "v_mul_f32 v119, v115, v119\n"
    "v_add_f32 v184, 1.0, v184\n"
    "v_add_f32 v185, 1.0, v185\n"
    "v_mfma_f32_32x32x16_bf16 v[32:47], v[132:135], a[240:243], v[32:47]\n"
    "ds_read_b64_tr_b16 v[150:151], v194 offset:19264\n"
    "v_add_f32 v186, 1.0, v186\n"
    "v_add_f32 v187, 1.0, v187\n"
    "s_waitcnt lgkmcnt(4)\n"
    "v_mfma_f32_32x32x16_bf16 v[0:15], v[136:139], a[248:251], v[0:15]\n"
    "ds_read_b64_tr_b16 v[128:129], v194 offset:14720\n"
    "ds_read_b64_tr_b16 v[130:131], v194 offset:14976\n"
    "v_rcp_f32 v184, v184\n"
    "v_mfma_f32_32x32x16_bf16 v[16:31], v[136:139], a[252:255], v[16:31]\n"
    "v_rcp_f32 v185, v185\n"
    "v_rcp_f32 v186, v186\n"
    "v_mfma_f32_32x32x16_bf16 v[48:63], v[140:143], a[252:255], v[48:63]\n"
    "ds_read_b64_tr_b16 v[132:133], v194 offset:14784\n"
    "v_rcp_f32 v187, v187\n"
    "v_mul_f32 v116, v116, v184\n"
    "v_mul_f32 v117, v117, v185\n"
    "v_mul_f32 v118, v118, v186\n"
    "v_mfma_f32_32x32x16_bf16 v[32:47], v[140:143], a[248:251], v[32:47]\n"
    "ds_read_b64_tr_b16 v[134:135], v194 offset:15040\n"
    "v_mul_f32 v119, v119, v187\n"
    "v_cvt_pk_bf16_f32 v112, v116, v117\n"
    "v_cvt_pk_bf16_f32 v113, v118, v119\n"
    "s_waitcnt lgkmcnt(4)\n"
    "v_mfma_f32_32x32x16_bf16 v[0:15], v[144:147], v[152:155], v[0:15]\n"
    "ds_read_b64_tr_b16 v[136:137], v194 offset:10496\n"
    "ds_read_b64_tr_b16 v[138:139], v194 offset:10752\n"
    "v_mul_f32 v188, v120, v120\n"
    "v_mul_f32 v189, v121, v121\n"
    "v_mul_f32 v190, v122, v122\n"
    "v_mfma_f32_32x32x16_bf16 v[16:31], v[144:147], v[156:159], v[16:31]\n"
    "v_mul_f32 v191, v123, v123\n"
    "v_fmaak_f32 v188, v205, v188, 0xc0135761\n"
    "v_fmaak_f32 v189, v205, v189, 0xc0135761\n"
    "v_fmaak_f32 v190, v205, v190, 0xc0135761\n"
    "v_mfma_f32_32x32x16_bf16 v[48:63], v[148:151], v[156:159], v[48:63]\n"
    "ds_read_b64_tr_b16 v[140:141], v194 offset:10560\n"
    "v_fmaak_f32 v191, v205, v191, 0xc0135761\n"
    "v_mul_f32 v188, v120, v188\n"
    "v_mul_f32 v189, v121, v189\n"
    "v_mfma_f32_32x32x16_bf16 v[32:47], v[148:151], v[152:155], v[32:47]\n"
    "ds_read_b64_tr_b16 v[142:143], v194 offset:10816\n"
    "v_mul_f32 v190, v122, v190\n"
    "v_mul_f32 v191, v123, v191\n"
    "s_waitcnt lgkmcnt(4)\n"
    "v_mfma_f32_32x32x16_bf16 v[0:15], v[128:131], v[160:163], v[0:15]\n"
    "ds_read_b64_tr_b16 v[144:145], v194 offset:6272\n"
    "ds_read_b64_tr_b16 v[146:147], v194 offset:6528\n"
    "v_exp_f32 v188, v188\n"
    "v_exp_f32 v189, v189\n"
    "v_mfma_f32_32x32x16_bf16 v[16:31], v[128:131], v[164:167], v[16:31]\n"
    "v_exp_f32 v190, v190\n"
    "v_exp_f32 v191, v191\n"
    "v_mfma_f32_32x32x16_bf16 v[48:63], v[132:135], v[164:167], v[48:63]\n"
    "ds_read_b64_tr_b16 v[148:149], v194 offset:6336\n"
    "v_mul_f32 v124, v120, v124\n"
    "v_mul_f32 v125, v121, v125\n"
    "v_mul_f32 v126, v122, v126\n"
    "v_mfma_f32_32x32x16_bf16 v[32:47], v[132:135], v[160:163], v[32:47]\n"
    "ds_read_b64_tr_b16 v[150:151], v194 offset:6592\n"
    "v_mul_f32 v127, v123, v127\n"
    "s_waitcnt vmcnt(13)\n"
    "s_barrier\n"
    "s_waitcnt lgkmcnt(4)\n"
    "v_mfma_f32_32x32x16_bf16 v[0:15], v[136:139], v[168:171], v[0:15]\n"
    "v_add_f32 v188, 1.0, v188\n"
    "v_add_f32 v189, 1.0, v189\n"
    "v_add_f32 v190, 1.0, v190\n"
    "v_add_f32 v191, 1.0, v191\n"
    "v_mfma_f32_32x32x16_bf16 v[16:31], v[136:139], v[172:175], v[16:31]\n"
    "v_rcp_f32 v188, v188\n"
    "v_rcp_f32 v189, v189\n"
    "v_mfma_f32_32x32x16_bf16 v[48:63], v[140:143], v[172:175], v[48:63]\n"
    "v_rcp_f32 v190, v190\n"
    "v_rcp_f32 v191, v191\n"
    "v_mul_f32 v124, v124, v188\n"
    "v_mfma_f32_32x32x16_bf16 v[32:47], v[140:143], v[168:171], v[32:47]\n"
    "v_mul_f32 v125, v125, v189\n"
    "v_mul_f32 v126, v126, v190\n"
    "v_mul_f32 v127, v127, v191\n"
    "s_waitcnt lgkmcnt(0)\n"
    "v_mfma_f32_32x32x16_bf16 v[0:15], v[144:147], v[176:179], v[0:15]\n"
    "v_cvt_pk_bf16_f32 v114, v124, v125\n"
    "v_cvt_pk_bf16_f32 v115, v126, v127\n"
    "v_mfma_f32_32x32x16_bf16 v[16:31], v[144:147], v[180:183], v[16:31]\n"
    "buffer_store_dwordx4 v[112:115], v211, s[24:27], s86 offen offset:1024 sc1\n"
    "v_mfma_f32_32x32x16_bf16 v[48:63], v[148:151], v[180:183], v[48:63]\n"
    "v_mfma_f32_32x32x16_bf16 v[32:47], v[148:151], v[176:179], v[32:47]\n"
    "ds_read_b64 v[136:137], v229 offset:10240\n"
    "ds_read_b128 v[128:131], v228 offset:0\n"
    "ds_read_b128 v[132:135], v228 offset:1024\n"
    "ds_read_b64_tr_b16 v[140:141], v230 offset:4096\n"
    "ds_read_b64_tr_b16 v[142:143], v230 offset:4480\n"
    "ds_read_b64_tr_b16 v[144:145], v230 offset:4864\n"
    "ds_read_b64_tr_b16 v[146:147], v230 offset:5248\n"
    "s_xor_b32 s86, s4, 3\n"
    "s_mul_i32 s86, s86, 8192\n"
    "s_add_u32 s86, s86, s6\n"
    "v_mul_f32 v184, v0, v0\n"
    "v_mul_f32 v185, v1, v1\n"
    "v_mul_f32 v186, v2, v2\n"
    "v_mul_f32 v187, v3, v3\n"
    "v_fmaak_f32 v184, v205, v184, 0xc0135761\n"
    "v_fmaak_f32 v185, v205, v185, 0xc0135761\n"
    "v_fmaak_f32 v186, v205, v186, 0xc0135761\n"
    "v_fmaak_f32 v187, v205, v187, 0xc0135761\n"
    "v_mul_f32 v184, v0, v184\n"
    "v_mul_f32 v185, v1, v185\n"
    "v_mul_f32 v186, v2, v186\n"
    "v_mul_f32 v187, v3, v187\n"
    "v_exp_f32 v184, v184\n"
    "v_exp_f32 v185, v185\n"
    "v_exp_f32 v186, v186\n"
    "v_exp_f32 v187, v187\n"
    "v_mul_f32 v4, v0, v4\n"
    "v_mul_f32 v5, v1, v5\n"
    "v_mul_f32 v6, v2, v6\n"
    "v_mul_f32 v7, v3, v7\n"
    "v_add_f32 v184, 1.0, v184\n"
    "v_add_f32 v185, 1.0, v185\n"
    "v_add_f32 v186, 1.0, v186\n"
    "v_add_f32 v187, 1.0, v187\n"
    "v_rcp_f32 v184, v184\n"
    "v_rcp_f32 v185, v185\n"
    "v_rcp_f32 v186, v186\n"
    "v_rcp_f32 v187, v187\n"
    "v_mul_f32 v4, v4, v184\n"
    "v_mul_f32 v5, v5, v185\n"
    "v_mul_f32 v6, v6, v186\n"
    "v_mul_f32 v7, v7, v187\n"
    "v_cvt_pk_bf16_f32 v0, v4, v5\n"
    "v_cvt_pk_bf16_f32 v1, v6, v7\n"
    "v_mul_f32 v188, v8, v8\n"
    "v_mul_f32 v189, v9, v9\n"
    "v_mul_f32 v190, v10, v10\n"
    "v_mul_f32 v191, v11, v11\n"
    "v_fmaak_f32 v188, v205, v188, 0xc0135761\n"
    "v_fmaak_f32 v189, v205, v189, 0xc0135761\n"
    "v_fmaak_f32 v190, v205, v190, 0xc0135761\n"
    "v_fmaak_f32 v191, v205, v191, 0xc0135761\n"
    "v_mul_f32 v188, v8, v188\n"
    "v_mul_f32 v189, v9, v189\n"
    "v_mul_f32 v190, v10, v190\n"
    "v_mul_f32 v191, v11, v191\n"
    "v_exp_f32 v188, v188\n"
    "v_exp_f32 v189, v189\n"
    "v_exp_f32 v190, v190\n"
    "v_exp_f32 v191, v191\n"
    "v_mul_f32 v12, v8, v12\n"
    "v_mul_f32 v13, v9, v13\n"
    "v_mul_f32 v14, v10, v14\n"
    "v_mul_f32 v15, v11, v15\n"
    "v_add_f32 v188, 1.0, v188\n"
    "v_add_f32 v189, 1.0, v189\n"
    "v_add_f32 v190, 1.0, v190\n"
    "v_add_f32 v191, 1.0, v191\n"
    "v_rcp_f32 v188, v188\n"
    "v_rcp_f32 v189, v189\n"
    "v_rcp_f32 v190, v190\n"
    "v_rcp_f32 v191, v191\n"
    "v_mul_f32 v12, v12, v188\n"
    "v_mul_f32 v13, v13, v189\n"
    "v_mul_f32 v14, v14, v190\n"
    "v_mul_f32 v15, v15, v191\n"
    "v_cvt_pk_bf16_f32 v2, v12, v13\n"
    "v_cvt_pk_bf16_f32 v3, v14, v15\n"
    "buffer_store_dwordx4 v[0:3], v210, s[24:27], s86 offen offset:0 sc1\n"
    "v_mul_f32 v184, v16, v16\n"
    "v_mul_f32 v185, v17, v17\n"
    "v_mul_f32 v186, v18, v18\n"
    "v_mul_f32 v187, v19, v19\n"
    "v_fmaak_f32 v184, v205, v184, 0xc0135761\n"
    "v_fmaak_f32 v185, v205, v185, 0xc0135761\n"
    "v_fmaak_f32 v186, v205, v186, 0xc0135761\n"
    "v_fmaak_f32 v187, v205, v187, 0xc0135761\n"
    "v_mul_f32 v184, v16, v184\n"
    "v_mul_f32 v185, v17, v185\n"
    "v_mul_f32 v186, v18, v186\n"
    "v_mul_f32 v187, v19, v187\n"
    "v_exp_f32 v184, v184\n"
    "v_exp_f32 v185, v185\n"
    "v_exp_f32 v186, v186\n"
    "v_exp_f32 v187, v187\n"
    "v_mul_f32 v20, v16, v20\n"
    "v_mul_f32 v21, v17, v21\n"
    "v_mul_f32 v22, v18, v22\n"
    "v_mul_f32 v23, v19, v23\n"
    "v_add_f32 v184, 1.0, v184\n"
    "v_add_f32 v185, 1.0, v185\n"
    "v_add_f32 v186, 1.0, v186\n"
    "v_add_f32 v187, 1.0, v187\n"
    "v_rcp_f32 v184, v184\n"
    "v_rcp_f32 v185, v185\n"
    "v_rcp_f32 v186, v186\n"
    "v_rcp_f32 v187, v187\n"
    "v_mul_f32 v20, v20, v184\n"
    "v_mul_f32 v21, v21, v185\n"
    "v_mul_f32 v22, v22, v186\n"
    "v_mul_f32 v23, v23, v187\n"
    "v_cvt_pk_bf16_f32 v16, v20, v21\n"
    "v_cvt_pk_bf16_f32 v17, v22, v23\n"
    "v_mul_f32 v188, v24, v24\n"
    "v_mul_f32 v189, v25, v25\n"
    "v_mul_f32 v190, v26, v26\n"
    "v_mul_f32 v191, v27, v27\n"
    "v_fmaak_f32 v188, v205, v188, 0xc0135761\n"
    "v_fmaak_f32 v189, v205, v189, 0xc0135761\n"
    "v_fmaak_f32 v190, v205, v190, 0xc0135761\n"
    "v_fmaak_f32 v191, v205, v191, 0xc0135761\n"
    "v_mul_f32 v188, v24, v188\n"
    "v_mul_f32 v189, v25, v189\n"
    "v_mul_f32 v190, v26, v190\n"
    "v_mul_f32 v191, v27, v191\n"
    "v_exp_f32 v188, v188\n"
    "v_exp_f32 v189, v189\n"
    "v_exp_f32 v190, v190\n"
    "v_exp_f32 v191, v191\n"
    "v_mul_f32 v28, v24, v28\n"
    "v_mul_f32 v29, v25, v29\n"
    "v_mul_f32 v30, v26, v30\n"
    "v_mul_f32 v31, v27, v31\n"
    "v_add_f32 v188, 1.0, v188\n"
    "v_add_f32 v189, 1.0, v189\n"
    "v_add_f32 v190, 1.0, v190\n"
    "v_add_f32 v191, 1.0, v191\n"
    "v_rcp_f32 v188, v188\n"
    "v_rcp_f32 v189, v189\n"
    "v_rcp_f32 v190, v190\n"
    "v_rcp_f32 v191, v191\n"
    "v_mul_f32 v28, v28, v188\n"
    "v_mul_f32 v29, v29, v189\n"
    "v_mul_f32 v30, v30, v190\n"
    "v_mul_f32 v31, v31, v191\n"
    "v_cvt_pk_bf16_f32 v18, v28, v29\n"
    "s_waitcnt lgkmcnt(5)\n"
    "v_mfma_f32_32x32x4_2b_bf16 v[64:95], v[136:137], v[220:221], 0 cbsz:1 abid:0\n"
    "v_cvt_pk_bf16_f32 v19, v30, v31\n"
    "buffer_store_dwordx4 v[16:19], v211, s[24:27], s86 offen offset:0 sc1\n"
    "v_mul_f32 v184, v32, v32\n"
    "v_mul_f32 v185, v33, v33\n"
    "v_mul_f32 v186, v34, v34\n"
    "v_mul_f32 v187, v35, v35\n"
    "v_fmaak_f32 v184, v205, v184, 0xc0135761\n"
    "v_fmaak_f32 v185, v205, v185, 0xc0135761\n"
    "v_fmaak_f32 v186, v205, v186, 0xc0135761\n"
    "v_fmaak_f32 v187, v205, v187, 0xc0135761\n"
    "v_mul_f32 v184, v32, v184\n"
    "v_mul_f32 v185, v33, v185\n"
    "v_mul_f32 v186, v34, v186\n"
    "v_mul_f32 v187, v35, v187\n"
    "v_exp_f32 v184, v184\n"
    "v_exp_f32 v185, v185\n"
    "v_exp_f32 v186, v186\n"
    "v_exp_f32 v187, v187\n"
    "v_mul_f32 v36, v32, v36\n"
    "v_mul_f32 v37, v33, v37\n"
    "v_mul_f32 v38, v34, v38\n"
    "v_mul_f32 v39, v35, v39\n"
    "v_add_f32 v184, 1.0, v184\n"
    "v_add_f32 v185, 1.0, v185\n"
    "v_add_f32 v186, 1.0, v186\n"
    "v_add_f32 v187, 1.0, v187\n"
    "v_rcp_f32 v184, v184\n"
    "v_rcp_f32 v185, v185\n"
    "v_rcp_f32 v186, v186\n"
    "v_rcp_f32 v187, v187\n"
    "v_mul_f32 v36, v36, v184\n"
    "v_mul_f32 v37, v37, v185\n"
    "v_mul_f32 v38, v38, v186\n"
    "v_mul_f32 v39, v39, v187\n"
    "v_cvt_pk_bf16_f32 v32, v36, v37\n"
    "v_cvt_pk_bf16_f32 v33, v38, v39\n"
    "v_mul_f32 v188, v40, v40\n"
    "v_mul_f32 v189, v41, v41\n"
    "v_mul_f32 v190, v42, v42\n"
    "v_mul_f32 v191, v43, v43\n"
    "v_fmaak_f32 v188, v205, v188, 0xc0135761\n"
    "v_fmaak_f32 v189, v205, v189, 0xc0135761\n"
    "v_fmaak_f32 v190, v205, v190, 0xc0135761\n"
    "v_fmaak_f32 v191, v205, v191, 0xc0135761\n"
    "v_mul_f32 v188, v40, v188\n"
    "v_mul_f32 v189, v41, v189\n"
    "v_mul_f32 v190, v42, v190\n"
    "v_mul_f32 v191, v43, v191\n"
    "v_exp_f32 v188, v188\n"
    "v_exp_f32 v189, v189\n"
    "v_exp_f32 v190, v190\n"
    "v_exp_f32 v191, v191\n"
    "v_mul_f32 v44, v40, v44\n"
    "v_mul_f32 v45, v41, v45\n"
    "v_mul_f32 v46, v42, v46\n"
    "v_mul_f32 v47, v43, v47\n"
    "v_add_f32 v188, 1.0, v188\n"
    "v_add_f32 v189, 1.0, v189\n"
    "v_add_f32 v190, 1.0, v190\n"
    "v_add_f32 v191, 1.0, v191\n"
    "v_rcp_f32 v188, v188\n"
    "v_rcp_f32 v189, v189\n"
    "v_rcp_f32 v190, v190\n"
    "v_rcp_f32 v191, v191\n"
    "v_mul_f32 v44, v44, v188\n"
    "v_mul_f32 v45, v45, v189\n"
    "v_mul_f32 v46, v46, v190\n"
    "v_mul_f32 v47, v47, v191\n"
    "v_cvt_pk_bf16_f32 v34, v44, v45\n"
    "v_cvt_pk_bf16_f32 v35, v46, v47\n"
    "v_mfma_f32_32x32x16_bf16 v[64:79], v[128:131], v[212:215], v[64:79]\n"
    "buffer_store_dwordx4 v[32:35], v210, s[24:27], s86 offen offset:1024 sc1\n"
    "v_mul_f32 v184, v48, v48\n"
    "v_mul_f32 v185, v49, v49\n"
    "v_mul_f32 v186, v50, v50\n"
    "v_mul_f32 v187, v51, v51\n"
    "v_fmaak_f32 v184, v205, v184, 0xc0135761\n"
    "v_fmaak_f32 v185, v205, v185, 0xc0135761\n"
    "v_fmaak_f32 v186, v205, v186, 0xc0135761\n"
    "v_fmaak_f32 v187, v205, v187, 0xc0135761\n"
    "v_mul_f32 v184, v48, v184\n"
    "v_mul_f32 v185, v49, v185\n"
    "v_mul_f32 v186, v50, v186\n"
    "v_mul_f32 v187, v51, v187\n"
    "v_exp_f32 v184, v184\n"
    "v_exp_f32 v185, v185\n"
    "v_exp_f32 v186, v186\n"
    "v_exp_f32 v187, v187\n"
    "v_mul_f32 v52, v48, v52\n"
    "v_mul_f32 v53, v49, v53\n"
    "v_mul_f32 v54, v50, v54\n"
    "v_mul_f32 v55, v51, v55\n"
    "v_add_f32 v184, 1.0, v184\n"
    "v_add_f32 v185, 1.0, v185\n"
    "v_add_f32 v186, 1.0, v186\n"
    "v_add_f32 v187, 1.0, v187\n"
    "v_rcp_f32 v184, v184\n"
    "v_rcp_f32 v185, v185\n"
    "v_rcp_f32 v186, v186\n"
    "v_rcp_f32 v187, v187\n"
    "v_mul_f32 v52, v52, v184\n"
    "v_mul_f32 v53, v53, v185\n"
    "v_mul_f32 v54, v54, v186\n"
    "v_mul_f32 v55, v55, v187\n"
    "v_cvt_pk_bf16_f32 v48, v52, v53\n"
    "v_cvt_pk_bf16_f32 v49, v54, v55\n"
    "v_mfma_f32_32x32x16_bf16 v[80:95], v[128:131], v[216:219], v[80:95]\n"
    "v_mul_f32 v188, v56, v56\n"
    "v_mul_f32 v189, v57, v57\n"
    "v_mul_f32 v190, v58, v58\n"
    "v_mul_f32 v191, v59, v59\n"
    "v_fmaak_f32 v188, v205, v188, 0xc0135761\n"
    "v_fmaak_f32 v189, v205, v189, 0xc0135761\n"
    "v_fmaak_f32 v190, v205, v190, 0xc0135761\n"
    "v_fmaak_f32 v191, v205, v191, 0xc0135761\n"
    "v_mul_f32 v188, v56, v188\n"
    "v_mul_f32 v189, v57, v189\n"
    "v_mul_f32 v190, v58, v190\n"
    "v_mul_f32 v191, v59, v191\n"
    "v_exp_f32 v188, v188\n"
    "v_exp_f32 v189, v189\n"
    "v_exp_f32 v190, v190\n"
    "v_exp_f32 v191, v191\n"
    "v_mul_f32 v60, v56, v60\n"
    "v_mul_f32 v61, v57, v61\n"
    "v_mul_f32 v62, v58, v62\n"
    "v_mul_f32 v63, v59, v63\n"
    "v_add_f32 v188, 1.0, v188\n"
    "v_add_f32 v189, 1.0, v189\n"
    "v_add_f32 v190, 1.0, v190\n"
    "v_add_f32 v191, 1.0, v191\n"
    "v_rcp_f32 v188, v188\n"
    "v_rcp_f32 v189, v189\n"
    "v_rcp_f32 v190, v190\n"
    "v_rcp_f32 v191, v191\n"
    "v_mul_f32 v60, v60, v188\n"
    "v_mul_f32 v61, v61, v189\n"
    "v_mul_f32 v62, v62, v190\n"
    "v_mul_f32 v63, v63, v191\n"
    "v_cvt_pk_bf16_f32 v50, v60, v61\n"
    "v_cvt_pk_bf16_f32 v51, v62, v63\n"
    "buffer_store_dwordx4 v[48:51], v211, s[24:27], s86 offen offset:1024 sc1\n"
    "v_exp_f32 v64, v64\n"
    "v_exp_f32 v65, v65\n"
    "v_exp_f32 v66, v66\n"
    "v_cvt_pk_bf16_f32 v0, v64, v65\n"
    "ds_read_b64_tr_b16 v[184:185], v230 offset:5632\n"
    "ds_read_b64_tr_b16 v[186:187], v230 offset:6016\n"
    "v_exp_f32 v67, v67\n"
    "v_exp_f32 v68, v68\n"
    "v_cvt_pk_bf16_f32 v1, v66, v67\n"
    "v_exp_f32 v69, v69\n"
    "v_mfma_f32_32x32x4_2b_bf16 v[96:127], v[136:137], v[220:221], 0 cbsz:1 abid:1\n"
    "ds_read_b64_tr_b16 v[188:189], v230 offset:6400\n"
    "ds_read_b64_tr_b16 v[190:191], v230 offset:6784\n"
    "v_exp_f32 v70, v70\n"
    "v_cvt_pk_bf16_f32 v2, v68, v69\n"
    "v_exp_f32 v71, v71\n"
    "v_exp_f32 v72, v72\n"
    "v_cvt_pk_bf16_f32 v3, v70, v71\n"
    "v_exp_f32 v73, v73\n"
    "v_exp_f32 v74, v74\n"
    "v_cvt_pk_bf16_f32 v4, v72, v73\n"
    "v_exp_f32 v75, v75\n"
    "s_waitcnt lgkmcnt(4)\n"
    "v_mfma_f32_32x32x16_bf16 v[32:47], v[140:143], v[0:3], 0\n"
    "ds_read_b128 v[128:131], v228 offset:2048\n"
    "ds_read_b64 v[138:139], v229 offset:10752\n"
    "v_exp_f32 v76, v76\n"
    "v_cvt_pk_bf16_f32 v5, v74, v75\n"
    "v_exp_f32 v77, v77\n"
    "v_exp_f32 v78, v78\n"
    "v_cvt_pk_bf16_f32 v6, v76, v77\n"
    "v_exp_f32 v79, v79\n"
    "v_mfma_f32_32x32x16_bf16 v[96:111], v[132:135], v[212:215], v[96:111]\n"
    "v_exp_f32 v80, v80\n"
    "v_cvt_pk_bf16_f32 v7, v78, v79\n"
    "v_exp_f32 v81, v81\n"
    "v_exp_f32 v82, v82\n"
    "v_cvt_pk_bf16_f32 v8, v80, v81\n"
    "v_exp_f32 v83, v83\n"
    "v_mfma_f32_32x32x16_bf16 v[32:47], v[144:147], v[4:7], v[32:47]\n"
    "v_exp_f32 v84, v84\n"
    "v_cvt_pk_bf16_f32 v9, v82, v83\n"
    "v_exp_f32 v85, v85\n"
    "v_exp_f32 v86, v86\n"
    "v_cvt_pk_bf16_f32 v10, v84, v85\n"
    "v_exp_f32 v87, v87\n"
    "v_mfma_f32_32x32x16_bf16 v[112:127], v[132:135], v[216:219], v[112:127]\n"
    "v_exp_f32 v88, v88\n"
    "v_cvt_pk_bf16_f32 v11, v86, v87\n"
    "v_exp_f32 v89, v89\n"
    "v_exp_f32 v90, v90\n"
    "v_cvt_pk_bf16_f32 v12, v88, v89\n"
    "v_exp_f32 v91, v91\n"
    "v_mfma_f32_32x32x16_bf16 v[48:63], v[140:143], v[8:11], 0\n"
    "v_exp_f32 v92, v92\n"
    "v_cvt_pk_bf16_f32 v13, v90, v91\n"
    "v_exp_f32 v93, v93\n"
    "v_exp_f32 v94, v94\n"
    "v_cvt_pk_bf16_f32 v14, v92, v93\n"
    "v_exp_f32 v95, v95\n"
    "v_exp_f32 v96, v96\n"
    "v_cvt_pk_bf16_f32 v15, v94, v95\n"
    "v_exp_f32 v97, v97\n"
    "v_exp_f32 v98, v98\n"
    "v_cvt_pk_bf16_f32 v0, v96, v97\n"
    "v_mfma_f32_32x32x16_bf16 v[48:63], v[144:147], v[12:15], v[48:63]\n"
    "ds_read_b64_tr_b16 v[140:141], v230 offset:7168\n"
    "ds_read_b64_tr_b16 v[142:143], v230 offset:7552\n"
    "v_exp_f32 v99, v99\n"
    "v_exp_f32 v100, v100\n"
    "v_cvt_pk_bf16_f32 v1, v98, v99\n"
    "v_exp_f32 v101, v101\n"
    "s_waitcnt lgkmcnt(2)\n"
    "v_mfma_f32_32x32x4_2b_bf16 v[64:95], v[138:139], v[220:221], 0 cbsz:1 abid:0\n"
    "ds_read_b64_tr_b16 v[144:145], v230 offset:7936\n"
    "ds_read_b64_tr_b16 v[146:147], v230 offset:8320\n"
    "v_exp_f32 v102, v102\n"
    "v_cvt_pk_bf16_f32 v2, v100, v101\n"
    "v_exp_f32 v103, v103\n"
    "v_exp_f32 v104, v104\n"
    "v_cvt_pk_bf16_f32 v3, v102, v103\n"
    "v_exp_f32 v105, v105\n"
    "v_exp_f32 v106, v106\n"
    "v_cvt_pk_bf16_f32 v4, v104, v105\n"
    "v_exp_f32 v107, v107\n"
    "v_mfma_f32_32x32x16_bf16 v[32:47], v[184:187], v[0:3], v[32:47]\n"
    "ds_read_b128 v[132:135], v228 offset:3072\n"
    "v_exp_f32 v108, v108\n"
    "v_cvt_pk_bf16_f32 v5, v106, v107\n"
    "v_exp_f32 v109, v109\n"
    "v_exp_f32 v110, v110\n"
    "v_cvt_pk_bf16_f32 v6, v108, v109\n"
    "v_exp_f32 v111, v111\n"
    "v_mfma_f32_32x32x16_bf16 v[64:79], v[128:131], v[212:215], v[64:79]\n"
    "v_exp_f32 v112, v112\n"
    "v_cvt_pk_bf16_f32 v7, v110, v111\n"
    "v_exp_f32 v113, v113\n"
    "v_exp_f32 v114, v114\n"
    "v_cvt_pk_bf16_f32 v8, v112, v113\n"
    "v_exp_f32 v115, v115\n"
    "v_mfma_f32_32x32x16_bf16 v[32:47], v[188:191], v[4:7], v[32:47]\n"
    "v_exp_f32 v116, v116\n"
    "v_cvt_pk_bf16_f32 v9, v114, v115\n"
    "v_exp_f32 v117, v117\n"
    "v_exp_f32 v118, v118\n"
    "v_cvt_pk_bf16_f32 v10, v116, v117\n"
    "v_exp_f32 v119, v119\n"
    "v_mfma_f32_32x32x16_bf16 v[80:95], v[128:131], v[216:219], v[80:95]\n"
    "v_exp_f32 v120, v120\n"
    "v_cvt_pk_bf16_f32 v11, v118, v119\n"
    "v_exp_f32 v121, v121\n"
    "v_exp_f32 v122, v122\n"
    "v_cvt_pk_bf16_f32 v12, v120, v121\n"
    "v_exp_f32 v123, v123\n"
    "v_mfma_f32_32x32x16_bf16 v[48:63], v[184:187], v[8:11], v[48:63]\n"
    "v_exp_f32 v124, v124\n"
    "v_cvt_pk_bf16_f32 v13, v122, v123\n"
    "v_exp_f32 v125, v125\n"
    "v_exp_f32 v126, v126\n"
    "v_cvt_pk_bf16_f32 v14, v124, v125\n"
    "v_exp_f32 v127, v127\n"
    "s_waitcnt vmcnt(15)\n"
    "s_barrier\n"
    "v_exp_f32 v64, v64\n"
    "v_cvt_pk_bf16_f32 v15, v126, v127\n"
    "v_exp_f32 v65, v65\n"
    "v_exp_f32 v66, v66\n"
    "v_cvt_pk_bf16_f32 v0, v64, v65\n"
    "v_mfma_f32_32x32x16_bf16 v[48:63], v[188:191], v[12:15], v[48:63]\n"
    "ds_read_b64_tr_b16 v[184:185], v230 offset:8704\n"
    "ds_read_b64_tr_b16 v[186:187], v230 offset:9088\n"
    "v_exp_f32 v67, v67\n"
    "v_exp_f32 v68, v68\n"
    "v_cvt_pk_bf16_f32 v1, v66, v67\n"
    "v_exp_f32 v69, v69\n"
    "v_mfma_f32_32x32x4_2b_bf16 v[96:127], v[138:139], v[220:221], 0 cbsz:1 abid:1\n"
    "ds_read_b64_tr_b16 v[188:189], v230 offset:9472\n"
    "ds_read_b64_tr_b16 v[190:191], v230 offset:9856\n"
    "v_exp_f32 v70, v70\n"
    "v_cvt_pk_bf16_f32 v2, v68, v69\n"
    "v_exp_f32 v71, v71\n"
    "v_exp_f32 v72, v72\n"
    "v_cvt_pk_bf16_f32 v3, v70, v71\n"
    "v_exp_f32 v73, v73\n"
    "v_exp_f32 v74, v74\n"
    "v_cvt_pk_bf16_f32 v4, v72, v73\n"
    "v_exp_f32 v75, v75\n"
    "s_waitcnt lgkmcnt(4)\n"
    "v_mfma_f32_32x32x16_bf16 v[32:47], v[140:143], v[0:3], v[32:47]\n"
    "s_add_u32 m0, s60, 0xf780\n"
    "s_add_u32 s61, s61, 0x3000\n"
    "ds_read_b128 v[128:131], v228 offset:12672\n"
    "ds_read_b64 v[136:137], v229 offset:22912\n"
    "v_exp_f32 v76, v76\n"
    "v_cvt_pk_bf16_f32 v5, v74, v75\n"
    "v_exp_f32 v77, v77\n"
    "v_exp_f32 v78, v78\n"
    "v_cvt_pk_bf16_f32 v6, v76, v77\n"
    "v_exp_f32 v79, v79\n"
    "v_mfma_f32_32x32x16_bf16 v[96:111], v[132:135], v[212:215], v[96:111]\n"
    "buffer_load_dwordx4 v246, s[28:31], s61 offen offset:0 sc1 lds\n"
    "v_exp_f32 v80, v80\n"
    "v_cvt_pk_bf16_f32 v7, v78, v79\n"
    "v_exp_f32 v81, v81\n"
    "v_exp_f32 v82, v82\n"
    "v_cvt_pk_bf16_f32 v8, v80, v81\n"
    "v_exp_f32 v83, v83\n"
    "v_mfma_f32_32x32x16_bf16 v[32:47], v[144:147], v[4:7], v[32:47]\n"
    "v_exp_f32 v84, v84\n"
    "v_cvt_pk_bf16_f32 v9, v82, v83\n"
    "v_exp_f32 v85, v85\n"
    "v_exp_f32 v86, v86\n"
    "v_cvt_pk_bf16_f32 v10, v84, v85\n"
    "v_exp_f32 v87, v87\n"
    "v_mfma_f32_32x32x16_bf16 v[112:127], v[132:135], v[216:219], v[112:127]\n"
    "v_exp_f32 v88, v88\n"
    "v_cvt_pk_bf16_f32 v11, v86, v87\n"
    "v_exp_f32 v89, v89\n"
    "v_exp_f32 v90, v90\n"
    "v_cvt_pk_bf16_f32 v12, v88, v89\n"
    "v_exp_f32 v91, v91\n"
    "v_mfma_f32_32x32x16_bf16 v[48:63], v[140:143], v[8:11], v[48:63]\n"
    "v_exp_f32 v92, v92\n"
    "v_cvt_pk_bf16_f32 v13, v90, v91\n"
    "v_exp_f32 v93, v93\n"
    "v_exp_f32 v94, v94\n"
    "v_cvt_pk_bf16_f32 v14, v92, v93\n"
    "v_exp_f32 v95, v95\n"
    "v_exp_f32 v96, v96\n"
    "v_cvt_pk_bf16_f32 v15, v94, v95\n"
    "v_exp_f32 v97, v97\n"
    "v_exp_f32 v98, v98\n"
    "v_cvt_pk_bf16_f32 v0, v96, v97\n"
    "v_mfma_f32_32x32x16_bf16 v[48:63], v[144:147], v[12:15], v[48:63]\n"
    "ds_read_b64_tr_b16 v[140:141], v230 offset:16768\n"
    "ds_read_b64_tr_b16 v[142:143], v230 offset:17152\n"
    "v_exp_f32 v99, v99\n"
    "v_exp_f32 v100, v100\n"
    "v_cvt_pk_bf16_f32 v1, v98, v99\n"
    "v_exp_f32 v101, v101\n"
    "s_waitcnt lgkmcnt(2)\n"
    "v_mfma_f32_32x32x4_2b_bf16 v[64:95], v[136:137], v[220:221], 0 cbsz:1 abid:0\n"
    "ds_read_b64_tr_b16 v[144:145], v230 offset:17536\n"
    "ds_read_b64_tr_b16 v[146:147], v230 offset:17920\n"
    "v_exp_f32 v102, v102\n"
    "v_cvt_pk_bf16_f32 v2, v100, v101\n"
    "v_exp_f32 v103, v103\n"
    "v_exp_f32 v104, v104\n"
    "v_cvt_pk_bf16_f32 v3, v102, v103\n"
    "v_exp_f32 v105, v105\n"
    "v_exp_f32 v106, v106\n"
    "v_cvt_pk_bf16_f32 v4, v104, v105\n"
    "v_exp_f32 v107, v107\n"
    "v_mfma_f32_32x32x16_bf16 v[32:47], v[184:187], v[0:3], v[32:47]\n"
    "ds_read_b128 v[132:135], v228 offset:13696\n"
    "v_exp_f32 v108, v108\n"
    "v_cvt_pk_bf16_f32 v5, v106, v107\n"
    "v_exp_f32 v109, v109\n"
    "v_exp_f32 v110, v110\n"
    "v_cvt_pk_bf16_f32 v6, v108, v109\n"
    "v_exp_f32 v111, v111\n"
    "v_mfma_f32_32x32x16_bf16 v[64:79], v[128:131], v[212:215], v[64:79]\n"
    "buffer_load_dwordx4 v246, s[28:31], s61 offen offset:1024 sc1 lds\n"
    "v_exp_f32 v112, v112\n"
    "v_cvt_pk_bf16_f32 v7, v110, v111\n"
    "v_exp_f32 v113, v113\n"
    "v_exp_f32 v114, v114\n"
    "v_cvt_pk_bf16_f32 v8, v112, v113\n"
    "v_exp_f32 v115, v115\n"
    "v_mfma_f32_32x32x16_bf16 v[32:47], v[188:191], v[4:7], v[32:47]\n"
    "v_exp_f32 v116, v116\n"
    "v_cvt_pk_bf16_f32 v9, v114, v115\n"
    "v_exp_f32 v117, v117\n"
    "v_exp_f32 v118, v118\n"
    "v_cvt_pk_bf16_f32 v10, v116, v117\n"
    "v_exp_f32 v119, v119\n"
    "v_mfma_f32_32x32x16_bf16 v[80:95], v[128:131], v[216:219], v[80:95]\n"
    "v_exp_f32 v120, v120\n"
    "v_cvt_pk_bf16_f32 v11, v118, v119\n"
    "v_exp_f32 v121, v121\n"
    "v_exp_f32 v122, v122\n"
    "v_cvt_pk_bf16_f32 v12, v120, v121\n"
    "v_exp_f32 v123, v123\n"
    "v_mfma_f32_32x32x16_bf16 v[48:63], v[184:187], v[8:11], v[48:63]\n"
    "v_exp_f32 v124, v124\n"
    "v_cvt_pk_bf16_f32 v13, v122, v123\n"
    "v_exp_f32 v125, v125\n"
    "v_exp_f32 v126, v126\n"
    "v_cvt_pk_bf16_f32 v14, v124, v125\n"
    "v_exp_f32 v127, v127\n"
    "v_exp_f32 v64, v64\n"
    "v_cvt_pk_bf16_f32 v15, v126, v127\n"
    "v_exp_f32 v65, v65\n"
    "v_exp_f32 v66, v66\n"
    "v_cvt_pk_bf16_f32 v0, v64, v65\n"
    "v_mfma_f32_32x32x16_bf16 v[48:63], v[188:191], v[12:15], v[48:63]\n"
    "ds_read_b64_tr_b16 v[184:185], v230 offset:18304\n"
    "ds_read_b64_tr_b16 v[186:187], v230 offset:18688\n"
    "v_exp_f32 v67, v67\n"
    "v_exp_f32 v68, v68\n"
    "v_cvt_pk_bf16_f32 v1, v66, v67\n"
    "v_exp_f32 v69, v69\n"
    "v_mfma_f32_32x32x4_2b_bf16 v[96:127], v[136:137], v[220:221], 0 cbsz:1 abid:1\n"
    "ds_read_b64_tr_b16 v[188:189], v230 offset:19072\n"
    "ds_read_b64_tr_b16 v[190:191], v230 offset:19456\n"
    "v_exp_f32 v70, v70\n"
    "v_cvt_pk_bf16_f32 v2, v68, v69\n"
    "v_exp_f32 v71, v71\n"
    "v_exp_f32 v72, v72\n"
    "v_cvt_pk_bf16_f32 v3, v70, v71\n"
    "v_exp_f32 v73, v73\n"
    "v_exp_f32 v74, v74\n"
    "v_cvt_pk_bf16_f32 v4, v72, v73\n"
    "v_exp_f32 v75, v75\n"
    "s_waitcnt lgkmcnt(4)\n"
    "v_mfma_f32_32x32x16_bf16 v[32:47], v[140:143], v[0:3], v[32:47]\n"
    "ds_read_b128 v[128:131], v228 offset:14720\n"
    "ds_read_b64 v[138:139], v229 offset:23424\n"
    "v_exp_f32 v76, v76\n"
    "v_cvt_pk_bf16_f32 v5, v74, v75\n"
    "v_exp_f32 v77, v77\n"
    "v_exp_f32 v78, v78\n"
    "v_cvt_pk_bf16_f32 v6, v76, v77\n"
    "v_exp_f32 v79, v79\n"
    "v_mfma_f32_32x32x16_bf16 v[96:111], v[132:135], v[212:215], v[96:111]\n"
    "buffer_load_dwordx4 v246, s[28:31], s61 offen offset:2048 sc1 lds\n"
    "v_exp_f32 v80, v80\n"
    "v_cvt_pk_bf16_f32 v7, v78, v79\n"
    "v_exp_f32 v81, v81\n"
    "v_exp_f32 v82, v82\n"
    "v_cvt_pk_bf16_f32 v8, v80, v81\n"
    "v_exp_f32 v83, v83\n"
    "v_mfma_f32_32x32x16_bf16 v[32:47], v[144:147], v[4:7], v[32:47]\n"
    "v_exp_f32 v84, v84\n"
    "v_cvt_pk_bf16_f32 v9, v82, v83\n"
    "v_exp_f32 v85, v85\n"
    "v_exp_f32 v86, v86\n"
    "v_cvt_pk_bf16_f32 v10, v84, v85\n"
    "v_exp_f32 v87, v87\n"
    "v_mfma_f32_32x32x16_bf16 v[112:127], v[132:135], v[216:219], v[112:127]\n"
    "v_exp_f32 v88, v88\n"
    "v_cvt_pk_bf16_f32 v11, v86, v87\n"
    "v_exp_f32 v89, v89\n"
    "v_exp_f32 v90, v90\n"
    "v_cvt_pk_bf16_f32 v12, v88, v89\n"
    "v_exp_f32 v91, v91\n"
    "v_mfma_f32_32x32x16_bf16 v[48:63], v[140:143], v[8:11], v[48:63]\n"
    "v_exp_f32 v92, v92\n"
    "v_cvt_pk_bf16_f32 v13, v90, v91\n"
    "v_exp_f32 v93, v93\n"
    "v_exp_f32 v94, v94\n"
    "v_cvt_pk_bf16_f32 v14, v92, v93\n"
    "v_exp_f32 v95, v95\n"
    "v_exp_f32 v96, v96\n"
    "v_cvt_pk_bf16_f32 v15, v94, v95\n"
    "v_exp_f32 v97, v97\n"
    "v_exp_f32 v98, v98\n"
    "v_cvt_pk_bf16_f32 v0, v96, v97\n"
    "v_mfma_f32_32x32x16_bf16 v[48:63], v[144:147], v[12:15], v[48:63]\n"
    "ds_read_b64_tr_b16 v[140:141], v230 offset:19840\n"
    "ds_read_b64_tr_b16 v[142:143], v230 offset:20224\n"
    "v_exp_f32 v99, v99\n"
    "v_exp_f32 v100, v100\n"
    "v_cvt_pk_bf16_f32 v1, v98, v99\n"
    "v_exp_f32 v101, v101\n"
    "s_waitcnt lgkmcnt(2)\n"
    "v_mfma_f32_32x32x4_2b_bf16 v[64:95], v[138:139], v[220:221], 0 cbsz:1 abid:0\n"
    "ds_read_b64_tr_b16 v[144:145], v230 offset:20608\n"
    "ds_read_b64_tr_b16 v[146:147], v230 offset:20992\n"
    "v_exp_f32 v102, v102\n"
    "v_cvt_pk_bf16_f32 v2, v100, v101\n"
    "v_exp_f32 v103, v103\n"
    "v_exp_f32 v104, v104\n"
    "v_cvt_pk_bf16_f32 v3, v102, v103\n"
    "v_exp_f32 v105, v105\n"
    "v_exp_f32 v106, v106\n"
    "v_cvt_pk_bf16_f32 v4, v104, v105\n"
    "v_exp_f32 v107, v107\n"
    "v_mfma_f32_32x32x16_bf16 v[32:47], v[184:187], v[0:3], v[32:47]\n"
    "s_add_u32 m0, s60, 0x12900\n"
    "s_add_u32 s61, s61, 0x3000\n"
    "ds_read_b128 v[132:135], v228 offset:15744\n"
    "v_exp_f32 v108, v108\n"
    "v_cvt_pk_bf16_f32 v5, v106, v107\n"
    "v_exp_f32 v109, v109\n"
    "v_exp_f32 v110, v110\n"
    "v_cvt_pk_bf16_f32 v6, v108, v109\n"
    "v_exp_f32 v111, v111\n"
    "v_mfma_f32_32x32x16_bf16 v[64:79], v[128:131], v[212:215], v[64:79]\n"
    "buffer_load_dwordx4 v246, s[28:31], s61 offen offset:0 sc1 lds\n"
    "v_exp_f32 v112, v112\n"
    "v_cvt_pk_bf16_f32 v7, v110, v111\n"
    "v_exp_f32 v113, v113\n"
    "v_exp_f32 v114, v114\n"
    "v_cvt_pk_bf16_f32 v8, v112, v113\n"
    "v_exp_f32 v115, v115\n"
    "v_mfma_f32_32x32x16_bf16 v[32:47], v[188:191], v[4:7], v[32:47]\n"
    "v_exp_f32 v116, v116\n"
    "v_cvt_pk_bf16_f32 v9, v114, v115\n"
    "v_exp_f32 v117, v117\n"
    "v_exp_f32 v118, v118\n"
    "v_cvt_pk_bf16_f32 v10, v116, v117\n"
    "v_exp_f32 v119, v119\n"
    "v_mfma_f32_32x32x16_bf16 v[80:95], v[128:131], v[216:219], v[80:95]\n"
    "v_exp_f32 v120, v120\n"
    "v_cvt_pk_bf16_f32 v11, v118, v119\n"
    "v_exp_f32 v121, v121\n"
    "v_exp_f32 v122, v122\n"
    "v_cvt_pk_bf16_f32 v12, v120, v121\n"
    "v_exp_f32 v123, v123\n"
    "v_mfma_f32_32x32x16_bf16 v[48:63], v[184:187], v[8:11], v[48:63]\n"
    "v_exp_f32 v124, v124\n"
    "v_cvt_pk_bf16_f32 v13, v122, v123\n"
    "v_exp_f32 v125, v125\n"
    "v_exp_f32 v126, v126\n"
    "v_cvt_pk_bf16_f32 v14, v124, v125\n"
    "v_exp_f32 v127, v127\n"
    "s_waitcnt vmcnt(4)\n"
    "s_barrier\n"
    "s_lshl_b32 s15, s64, 3\n"
    "s_add_u32 s15, s15, 1024\n"
    "s_cmp_lg_u32 s9, 0\n"
    "s_cselect_b32 s15, 0x8000, s15\n"
    "v_lshl_add_u32 v225, v192, 24, s15\n"
    "v_mov_b32 v226, s48\n"
    "v_mov_b32 v227, s49\n"
    "buffer_store_dwordx2 v[226:227], v225, s[68:71], 0 offen sc1\n"
    "v_exp_f32 v64, v64\n"
    "v_cvt_pk_bf16_f32 v15, v126, v127\n"
    "v_exp_f32 v65, v65\n"
    "v_exp_f32 v66, v66\n"
    "v_cvt_pk_bf16_f32 v0, v64, v65\n"
    "v_mfma_f32_32x32x16_bf16 v[48:63], v[188:191], v[12:15], v[48:63]\n"
    "ds_read_b64_tr_b16 v[184:185], v230 offset:21376\n"
    "ds_read_b64_tr_b16 v[186:187], v230 offset:21760\n"
    "v_exp_f32 v67, v67\n"
    "v_exp_f32 v68, v68\n"
    "v_cvt_pk_bf16_f32 v1, v66, v67\n"
    "v_exp_f32 v69, v69\n"
    "v_mfma_f32_32x32x4_2b_bf16 v[96:127], v[138:139], v[220:221], 0 cbsz:1 abid:1\n"
    "ds_read_b64_tr_b16 v[188:189], v230 offset:22144\n"
    "ds_read_b64_tr_b16 v[190:191], v230 offset:22528\n"
    "v_exp_f32 v70, v70\n"
    "v_cvt_pk_bf16_f32 v2, v68, v69\n"
    "v_exp_f32 v71, v71\n"
    "v_exp_f32 v72, v72\n"
    "v_cvt_pk_bf16_f32 v3, v70, v71\n"
    "v_exp_f32 v73, v73\n"
    "v_exp_f32 v74, v74\n"
    "v_cvt_pk_bf16_f32 v4, v72, v73\n"
    "v_exp_f32 v75, v75\n"
    "s_waitcnt lgkmcnt(4)\n"
    "v_mfma_f32_32x32x16_bf16 v[32:47], v[140:143], v[0:3], v[32:47]\n"
    "ds_read_b128 v[128:131], v228 offset:25344\n"
    "ds_read_b64 v[136:137], v229 offset:35584\n"
    "v_exp_f32 v76, v76\n"
    "v_cvt_pk_bf16_f32 v5, v74, v75\n"
    "v_exp_f32 v77, v77\n"
    "v_exp_f32 v78, v78\n"
    "v_cvt_pk_bf16_f32 v6, v76, v77\n"
    "v_exp_f32 v79, v79\n"
    "v_mfma_f32_32x32x16_bf16 v[96:111], v[132:135], v[212:215], v[96:111]\n"
    "buffer_load_dwordx4 v246, s[28:31], s61 offen offset:1024 sc1 lds\n"
    "v_exp_f32 v80, v80\n"
    "v_cvt_pk_bf16_f32 v7, v78, v79\n"
    "v_exp_f32 v81, v81\n"
    "v_exp_f32 v82, v82\n"
    "v_cvt_pk_bf16_f32 v8, v80, v81\n"
    "v_exp_f32 v83, v83\n"
    "v_mfma_f32_32x32x16_bf16 v[32:47], v[144:147], v[4:7], v[32:47]\n"
    "v_exp_f32 v84, v84\n"
    "v_cvt_pk_bf16_f32 v9, v82, v83\n"
    "v_exp_f32 v85, v85\n"
    "v_exp_f32 v86, v86\n"
    "v_cvt_pk_bf16_f32 v10, v84, v85\n"
    "v_exp_f32 v87, v87\n"
    "v_mfma_f32_32x32x16_bf16 v[112:127], v[132:135], v[216:219], v[112:127]\n"
    "v_exp_f32 v88, v88\n"
    "v_cvt_pk_bf16_f32 v11, v86, v87\n"
    "v_exp_f32 v89, v89\n"
    "v_exp_f32 v90, v90\n"
    "v_cvt_pk_bf16_f32 v12, v88, v89\n"
    "v_exp_f32 v91, v91\n"
    "v_mfma_f32_32x32x16_bf16 v[48:63], v[140:143], v[8:11], v[48:63]\n"
    "v_exp_f32 v92, v92\n"
    "v_cvt_pk_bf16_f32 v13, v90, v91\n"
    "v_exp_f32 v93, v93\n"
    "v_exp_f32 v94, v94\n"
    "v_cvt_pk_bf16_f32 v14, v92, v93\n"
    "v_exp_f32 v95, v95\n"
    "v_exp_f32 v96, v96\n"
    "v_cvt_pk_bf16_f32 v15, v94, v95\n"
    "v_exp_f32 v97, v97\n"
    "v_exp_f32 v98, v98\n"
    "v_cvt_pk_bf16_f32 v0, v96, v97\n"
    "v_mfma_f32_32x32x16_bf16 v[48:63], v[144:147], v[12:15], v[48:63]\n"
    "ds_read_b64_tr_b16 v[140:141], v230 offset:29440\n"
    "ds_read_b64_tr_b16 v[142:143], v230 offset:29824\n"
    "v_exp_f32 v99, v99\n"
    "v_exp_f32 v100, v100\n"
    "v_cvt_pk_bf16_f32 v1, v98, v99\n"
    "v_exp_f32 v101, v101\n"
    "s_waitcnt lgkmcnt(2)\n"
    "v_mfma_f32_32x32x4_2b_bf16 v[64:95], v[136:137], v[220:221], 0 cbsz:1 abid:0\n"
    "ds_read_b64_tr_b16 v[144:145], v230 offset:30208\n"
    "ds_read_b64_tr_b16 v[146:147], v230 offset:30592\n"
    "v_exp_f32 v102, v102\n"
    "v_cvt_pk_bf16_f32 v2, v100, v101\n"
    "v_exp_f32 v103, v103\n"
    "v_exp_f32 v104, v104\n"
    "v_cvt_pk_bf16_f32 v3, v102, v103\n"
    "v_exp_f32 v105, v105\n"
    "v_exp_f32 v106, v106\n"
    "v_cvt_pk_bf16_f32 v4, v104, v105\n"
    "v_exp_f32 v107, v107\n"
    "v_mfma_f32_32x32x16_bf16 v[32:47], v[184:187], v[0:3], v[32:47]\n"
    "ds_read_b128 v[132:135], v228 offset:26368\n"
    "v_exp_f32 v108, v108\n"
    "v_cvt_pk_bf16_f32 v5, v106, v107\n"
    "v_exp_f32 v109, v109\n"
    "v_exp_f32 v110, v110\n"
    "v_cvt_pk_bf16_f32 v6, v108, v109\n"
    "v_exp_f32 v111, v111\n"
    "v_mfma_f32_32x32x16_bf16 v[64:79], v[128:131], v[212:215], v[64:79]\n"
    "buffer_load_dwordx4 v246, s[28:31], s61 offen offset:2048 sc1 lds\n"
    "v_exp_f32 v112, v112\n"
    "v_cvt_pk_bf16_f32 v7, v110, v111\n"
    "v_exp_f32 v113, v113\n"
    "v_exp_f32 v114, v114\n"
    "v_cvt_pk_bf16_f32 v8, v112, v113\n"
    "v_exp_f32 v115, v115\n"
    "v_mfma_f32_32x32x16_bf16 v[32:47], v[188:191], v[4:7], v[32:47]\n"
    "v_exp_f32 v116, v116\n"
    "v_cvt_pk_bf16_f32 v9, v114, v115\n"
    "v_exp_f32 v117, v117\n"
    "v_exp_f32 v118, v118\n"
    "v_cvt_pk_bf16_f32 v10, v116, v117\n"
    "v_exp_f32 v119, v119\n"
    "v_mfma_f32_32x32x16_bf16 v[80:95], v[128:131], v[216:219], v[80:95]\n"
    "v_exp_f32 v120, v120\n"
    "v_cvt_pk_bf16_f32 v11, v118, v119\n"
    "v_exp_f32 v121, v121\n"
    "v_exp_f32 v122, v122\n"
    "v_cvt_pk_bf16_f32 v12, v120, v121\n"
    "v_exp_f32 v123, v123\n"
    "v_mfma_f32_32x32x16_bf16 v[48:63], v[184:187], v[8:11], v[48:63]\n"
    "v_exp_f32 v124, v124\n"
    "v_cvt_pk_bf16_f32 v13, v122, v123\n"
    "v_exp_f32 v125, v125\n"
    "v_exp_f32 v126, v126\n"
    "v_cvt_pk_bf16_f32 v14, v124, v125\n"
    "v_exp_f32 v127, v127\n"
    "v_exp_f32 v64, v64\n"
    "v_cvt_pk_bf16_f32 v15, v126, v127\n"
    "v_exp_f32 v65, v65\n"
    "v_exp_f32 v66, v66\n"
    "v_cvt_pk_bf16_f32 v0, v64, v65\n"
    "v_mfma_f32_32x32x16_bf16 v[48:63], v[188:191], v[12:15], v[48:63]\n"
    "ds_read_b64_tr_b16 v[184:185], v230 offset:30976\n"
    "ds_read_b64_tr_b16 v[186:187], v230 offset:31360\n"
    "v_exp_f32 v67, v67\n"
    "v_exp_f32 v68, v68\n"
    "v_cvt_pk_bf16_f32 v1, v66, v67\n"
    "v_exp_f32 v69, v69\n"
    "v_mfma_f32_32x32x4_2b_bf16 v[96:127], v[136:137], v[220:221], 0 cbsz:1 abid:1\n"
    "ds_read_b64_tr_b16 v[188:189], v230 offset:31744\n"
    "ds_read_b64_tr_b16 v[190:191], v230 offset:32128\n"
    "s_and_b32 s15, s64, 0xfc\n"
    "s_lshl_b32 s15, s15, 3\n"
    "s_add_u32 s15, s15, 1024\n"
    "v_and_b32 v225, 3, v192\n"
    "v_lshl_add_u32 v225, v225, 3, s15\n"
    "v_exp_f32 v70, v70\n"
    "v_cvt_pk_bf16_f32 v2, v68, v69\n"
    "v_exp_f32 v71, v71\n"
    "v_exp_f32 v72, v72\n"
    "v_cvt_pk_bf16_f32 v3, v70, v71\n"
    "v_exp_f32 v73, v73\n"
    "v_exp_f32 v74, v74\n"
    "v_cvt_pk_bf16_f32 v4, v72, v73\n"
    "v_exp_f32 v75, v75\n"
    "s_waitcnt lgkmcnt(4)\n"
    "v_mfma_f32_32x32x16_bf16 v[32:47], v[140:143], v[0:3], v[32:47]\n"
    "s_add_u32 m0, s60, 0x15a80\n"
    "s_add_u32 s61, s61, 0x3000\n"
    "ds_read_b128 v[128:131], v228 offset:27392\n"
    "ds_read_b64 v[138:139], v229 offset:36096\n"
    "v_exp_f32 v76, v76\n"
    "v_cvt_pk_bf16_f32 v5, v74, v75\n"
    "v_exp_f32 v77, v77\n"
    "v_exp_f32 v78, v78\n"
    "v_cvt_pk_bf16_f32 v6, v76, v77\n"
    "v_exp_f32 v79, v79\n"
    "v_mfma_f32_32x32x16_bf16 v[96:111], v[132:135], v[212:215], v[96:111]\n"
    "buffer_load_dwordx4 v246, s[28:31], s61 offen offset:0 sc1 lds\n"
    "v_exp_f32 v80, v80\n"
    "v_cvt_pk_bf16_f32 v7, v78, v79\n"
    "v_exp_f32 v81, v81\n"
    "v_exp_f32 v82, v82\n"
    "v_cvt_pk_bf16_f32 v8, v80, v81\n"
    "v_exp_f32 v83, v83\n"
    "v_mfma_f32_32x32x16_bf16 v[32:47], v[144:147], v[4:7], v[32:47]\n"
    "v_exp_f32 v84, v84\n"
    "v_cvt_pk_bf16_f32 v9, v82, v83\n"
    "v_exp_f32 v85, v85\n"
    "v_exp_f32 v86, v86\n"
    "v_cvt_pk_bf16_f32 v10, v84, v85\n"
    "v_exp_f32 v87, v87\n"
    "v_mfma_f32_32x32x16_bf16 v[112:127], v[132:135], v[216:219], v[112:127]\n"
    "global_load_dwordx2 v[226:227], v225, s[50:51] sc1\n"
    "v_exp_f32 v88, v88\n"
    "v_cvt_pk_bf16_f32 v11, v86, v87\n"
    "v_exp_f32 v89, v89\n"
    "v_exp_f32 v90, v90\n"
    "v_cvt_pk_bf16_f32 v12, v88, v89\n"
    "v_exp_f32 v91, v91\n"
    "v_mfma_f32_32x32x16_bf16 v[48:63], v[140:143], v[8:11], v[48:63]\n"
    "v_exp_f32 v92, v92\n"
    "v_cvt_pk_bf16_f32 v13, v90, v91\n"
    "v_exp_f32 v93, v93\n"
    "v_exp_f32 v94, v94\n"
    "v_cvt_pk_bf16_f32 v14, v92, v93\n"
    "v_exp_f32 v95, v95\n"
    "v_exp_f32 v96, v96\n"
    "v_cvt_pk_bf16_f32 v15, v94, v95\n"
    "v_exp_f32 v97, v97\n"
    "v_exp_f32 v98, v98\n"
    "v_cvt_pk_bf16_f32 v0, v96, v97\n"
    "v_mfma_f32_32x32x16_bf16 v[48:63], v[144:147], v[12:15], v[48:63]\n"
    "ds_read_b64_tr_b16 v[140:141], v230 offset:32512\n"
    "ds_read_b64_tr_b16 v[142:143], v230 offset:32896\n"
    "v_exp_f32 v99, v99\n"
    "v_exp_f32 v100, v100\n"
    "v_cvt_pk_bf16_f32 v1, v98, v99\n"
    "v_exp_f32 v101, v101\n"
    "s_waitcnt lgkmcnt(2)\n"
    "v_mfma_f32_32x32x4_2b_bf16 v[64:95], v[138:139], v[220:221], 0 cbsz:1 abid:0\n"
    "ds_read_b64_tr_b16 v[144:145], v230 offset:33280\n"
    "ds_read_b64_tr_b16 v[146:147], v230 offset:33664\n"
    "v_exp_f32 v102, v102\n"
    "v_cvt_pk_bf16_f32 v2, v100, v101\n"
    "v_exp_f32 v103, v103\n"
    "v_exp_f32 v104, v104\n"
    "v_cvt_pk_bf16_f32 v3, v102, v103\n"
    "v_exp_f32 v105, v105\n"
    "v_exp_f32 v106, v106\n"
    "v_cvt_pk_bf16_f32 v4, v104, v105\n"
    "v_exp_f32 v107, v107\n"
    "v_mfma_f32_32x32x16_bf16 v[32:47], v[184:187], v[0:3], v[32:47]\n"
    "ds_read_b128 v[132:135], v228 offset:28416\n"
    "v_exp_f32 v108, v108\n"
    "v_cvt_pk_bf16_f32 v5, v106, v107\n"
    "v_exp_f32 v109, v109\n"
    "v_exp_f32 v110, v110\n"
    "v_cvt_pk_bf16_f32 v6, v108, v109\n"
    "v_exp_f32 v111, v111\n"
    "v_mfma_f32_32x32x16_bf16 v[64:79], v[128:131], v[212:215], v[64:79]\n"
    "buffer_load_dwordx4 v246, s[28:31], s61 offen offset:1024 sc1 lds\n"
    "v_exp_f32 v112, v112\n"
    "v_cvt_pk_bf16_f32 v7, v110, v111\n"
    "v_exp_f32 v113, v113\n"
    "v_exp_f32 v114, v114\n"
    "v_cvt_pk_bf16_f32 v8, v112, v113\n"
    "v_exp_f32 v115, v115\n"
    "v_mfma_f32_32x32x16_bf16 v[32:47], v[188:191], v[4:7], v[32:47]\n"
    "v_exp_f32 v116, v116\n"
    "v_cvt_pk_bf16_f32 v9, v114, v115\n"
    "v_exp_f32 v117, v117\n"
    "v_exp_f32 v118, v118\n"
    "v_cvt_pk_bf16_f32 v10, v116, v117\n"
    "v_exp_f32 v119, v119\n"
    "v_mfma_f32_32x32x16_bf16 v[80:95], v[128:131], v[216:219], v[80:95]\n"
    "v_exp_f32 v120, v120\n"
    "v_cvt_pk_bf16_f32 v11, v118, v119\n"
    "v_exp_f32 v121, v121\n"
    "v_exp_f32 v122, v122\n"
    "v_cvt_pk_bf16_f32 v12, v120, v121\n"
    "v_exp_f32 v123, v123\n"
    "v_mfma_f32_32x32x16_bf16 v[48:63], v[184:187], v[8:11], v[48:63]\n"
    "v_exp_f32 v124, v124\n"
    "v_cvt_pk_bf16_f32 v13, v122, v123\n"
    "v_exp_f32 v125, v125\n"
    "v_exp_f32 v126, v126\n"
    "v_cvt_pk_bf16_f32 v14, v124, v125\n"
    "v_exp_f32 v127, v127\n"
    "v_exp_f32 v64, v64\n"
    "v_cvt_pk_bf16_f32 v15, v126, v127\n"
    "v_exp_f32 v65, v65\n"
    "v_exp_f32 v66, v66\n"
    "v_cvt_pk_bf16_f32 v0, v64, v65\n"
    "v_mfma_f32_32x32x16_bf16 v[48:63], v[188:191], v[12:15], v[48:63]\n"
    "ds_read_b64_tr_b16 v[184:185], v230 offset:34048\n"
    "ds_read_b64_tr_b16 v[186:187], v230 offset:34432\n"
    "v_exp_f32 v67, v67\n"
    "v_exp_f32 v68, v68\n"
    "v_cvt_pk_bf16_f32 v1, v66, v67\n"
    "v_exp_f32 v69, v69\n"
    "v_mfma_f32_32x32x4_2b_bf16 v[96:127], v[138:139], v[220:221], 0 cbsz:1 abid:1\n"
    "ds_read_b64_tr_b16 v[188:189], v230 offset:34816\n"
    "ds_read_b64_tr_b16 v[190:191], v230 offset:35200\n"
    "v_exp_f32 v70, v70\n"
    "v_cvt_pk_bf16_f32 v2, v68, v69\n"
    "v_exp_f32 v71, v71\n"
    "v_exp_f32 v72, v72\n"
    "v_cvt_pk_bf16_f32 v3, v70, v71\n"
    "v_exp_f32 v73, v73\n"
    "v_exp_f32 v74, v74\n"
    "v_cvt_pk_bf16_f32 v4, v72, v73\n"
    "v_exp_f32 v75, v75\n"
    "s_waitcnt lgkmcnt(4)\n"
    "v_mfma_f32_32x32x16_bf16 v[32:47], v[140:143], v[0:3], v[32:47]\n"
    "ds_read_b128 v[128:131], v228 offset:38016\n"
    "ds_read_b64 v[136:137], v229 offset:48256\n"
    "v_exp_f32 v76, v76\n"
    "v_cvt_pk_bf16_f32 v5, v74, v75\n"
    "v_exp_f32 v77, v77\n"
    "v_exp_f32 v78, v78\n"
    "v_cvt_pk_bf16_f32 v6, v76, v77\n"
    "v_exp_f32 v79, v79\n"
    "v_mfma_f32_32x32x16_bf16 v[96:111], v[132:135], v[212:215], v[96:111]\n"
    "buffer_load_dwordx4 v246, s[28:31], s61 offen offset:2048 sc1 lds\n"
    "v_exp_f32 v80, v80\n"
    "v_cvt_pk_bf16_f32 v7, v78, v79\n"
    "v_exp_f32 v81, v81\n"
    "v_exp_f32 v82, v82\n"
    "v_cvt_pk_bf16_f32 v8, v80, v81\n"
    "v_exp_f32 v83, v83\n"
    "v_mfma_f32_32x32x16_bf16 v[32:47], v[144:147], v[4:7], v[32:47]\n"
    "v_exp_f32 v84, v84\n"
    "v_cvt_pk_bf16_f32 v9, v82, v83\n"
    "v_exp_f32 v85, v85\n"
    "v_exp_f32 v86, v86\n"
    "v_cvt_pk_bf16_f32 v10, v84, v85\n"
    "v_exp_f32 v87, v87\n"
    "v_mfma_f32_32x32x16_bf16 v[112:127], v[132:135], v[216:219], v[112:127]\n"
    "v_exp_f32 v88, v88\n"
    "v_cvt_pk_bf16_f32 v11, v86, v87\n"
    "v_exp_f32 v89, v89\n"
    "v_exp_f32 v90, v90\n"
    "v_cvt_pk_bf16_f32 v12, v88, v89\n"
    "v_exp_f32 v91, v91\n"
    "v_mfma_f32_32x32x16_bf16 v[48:63], v[140:143], v[8:11], v[48:63]\n"
    "v_exp_f32 v92, v92\n"
    "v_cvt_pk_bf16_f32 v13, v90, v91\n"
    "v_exp_f32 v93, v93\n"
    "v_exp_f32 v94, v94\n"
    "v_cvt_pk_bf16_f32 v14, v92, v93\n"
    "v_exp_f32 v95, v95\n"
    "v_exp_f32 v96, v96\n"
    "v_cvt_pk_bf16_f32 v15, v94, v95\n"
    "v_exp_f32 v97, v97\n"
    "v_exp_f32 v98, v98\n"
    "v_cvt_pk_bf16_f32 v0, v96, v97\n"
    "v_mfma_f32_32x32x16_bf16 v[48:63], v[144:147], v[12:15], v[48:63]\n"
    "ds_read_b64_tr_b16 v[140:141], v230 offset:42112\n"
    "ds_read_b64_tr_b16 v[142:143], v230 offset:42496\n"
    "v_exp_f32 v99, v99\n"
    "v_exp_f32 v100, v100\n"
    "v_cvt_pk_bf16_f32 v1, v98, v99\n"
    "v_exp_f32 v101, v101\n"
    "s_waitcnt lgkmcnt(2)\n"
    "v_mfma_f32_32x32x4_2b_bf16 v[64:95], v[136:137], v[220:221], 0 cbsz:1 abid:0\n"
    "ds_read_b64_tr_b16 v[144:145], v230 offset:42880\n"
    "ds_read_b64_tr_b16 v[146:147], v230 offset:43264\n"
    "v_exp_f32 v102, v102\n"
    "v_cvt_pk_bf16_f32 v2, v100, v101\n"
    "v_exp_f32 v103, v103\n"
    "v_exp_f32 v104, v104\n"
    "v_cvt_pk_bf16_f32 v3, v102, v103\n"
    "v_exp_f32 v105, v105\n"
    "v_exp_f32 v106, v106\n"
    "v_cvt_pk_bf16_f32 v4, v104, v105\n"
    "v_exp_f32 v107, v107\n"
    "v_mfma_f32_32x32x16_bf16 v[32:47], v[184:187], v[0:3], v[32:47]\n"
    "s_add_u32 m0, s60, 0x18c00\n"
    "s_add_u32 s61, s61, 0x3000\n"
    "ds_read_b128 v[132:135], v228 offset:39040\n"
    "v_exp_f32 v108, v108\n"
    "v_cvt_pk_bf16_f32 v5, v106, v107\n"
    "v_exp_f32 v109, v109\n"
    "v_exp_f32 v110, v110\n"
    "v_cvt_pk_bf16_f32 v6, v108, v109\n"
    "v_exp_f32 v111, v111\n"
    "v_mfma_f32_32x32x16_bf16 v[64:79], v[128:131], v[212:215], v[64:79]\n"
    "buffer_load_dwordx4 v246, s[28:31], s61 offen offset:0 sc1 lds\n"
    "v_exp_f32 v112, v112\n"
    "v_cvt_pk_bf16_f32 v7, v110, v111\n"
    "v_exp_f32 v113, v113\n"
    "v_exp_f32 v114, v114\n"
    "v_cvt_pk_bf16_f32 v8, v112, v113\n"
    "v_exp_f32 v115, v115\n"
    "v_mfma_f32_32x32x16_bf16 v[32:47], v[188:191], v[4:7], v[32:47]\n"
    "v_exp_f32 v116, v116\n"
    "v_cvt_pk_bf16_f32 v9, v114, v115\n"
    "v_exp_f32 v117, v117\n"
    "v_exp_f32 v118, v118\n"
    "v_cvt_pk_bf16_f32 v10, v116, v117\n"
    "v_exp_f32 v119, v119\n"
    "v_mfma_f32_32x32x16_bf16 v[80:95], v[128:131], v[216:219], v[80:95]\n"
    "v_exp_f32 v120, v120\n"
    "v_cvt_pk_bf16_f32 v11, v118, v119\n"
    "v_exp_f32 v121, v121\n"
    "v_exp_f32 v122, v122\n"
    "v_cvt_pk_bf16_f32 v12, v120, v121\n"
    "v_exp_f32 v123, v123\n"
    "v_mfma_f32_32x32x16_bf16 v[48:63], v[184:187], v[8:11], v[48:63]\n"
    "v_exp_f32 v124, v124\n"
    "v_cvt_pk_bf16_f32 v13, v122, v123\n"
    "v_exp_f32 v125, v125\n"
    "v_exp_f32 v126, v126\n"
    "v_cvt_pk_bf16_f32 v14, v124, v125\n"
    "v_exp_f32 v127, v127\n"
    "v_exp_f32 v64, v64\n"
    "v_cvt_pk_bf16_f32 v15, v126, v127\n"
    "v_exp_f32 v65, v65\n"
    "v_exp_f32 v66, v66\n"
    "v_cvt_pk_bf16_f32 v0, v64, v65\n"
    "v_mfma_f32_32x32x16_bf16 v[48:63], v[188:191], v[12:15], v[48:63]\n"
    "ds_read_b64_tr_b16 v[184:185], v230 offset:43648\n"
    "ds_read_b64_tr_b16 v[186:187], v230 offset:44032\n"
    "v_exp_f32 v67, v67\n"
    "v_exp_f32 v68, v68\n"
    "v_cvt_pk_bf16_f32 v1, v66, v67\n"
    "v_exp_f32 v69, v69\n"
    "v_mfma_f32_32x32x4_2b_bf16 v[96:127], v[136:137], v[220:221], 0 cbsz:1 abid:1\n"
    "ds_read_b64_tr_b16 v[188:189], v230 offset:44416\n"
    "ds_read_b64_tr_b16 v[190:191], v230 offset:44800\n"
    "v_exp_f32 v70, v70\n"
    "v_cvt_pk_bf16_f32 v2, v68, v69\n"
    "v_exp_f32 v71, v71\n"
    "v_exp_f32 v72, v72\n"
    "v_cvt_pk_bf16_f32 v3, v70, v71\n"
    "v_exp_f32 v73, v73\n"
    "v_exp_f32 v74, v74\n"
    "v_cvt_pk_bf16_f32 v4, v72, v73\n"
    "v_exp_f32 v75, v75\n"
    "s_waitcnt lgkmcnt(4)\n"
    "v_mfma_f32_32x32x16_bf16 v[32:47], v[140:143], v[0:3], v[32:47]\n"
    "ds_read_b128 v[128:131], v228 offset:40064\n"
    "ds_read_b64 v[138:139], v229 offset:48768\n"
    "v_exp_f32 v76, v76\n"
    "v_cvt_pk_bf16_f32 v5, v74, v75\n"
    "v_exp_f32 v77, v77\n"
    "v_exp_f32 v78, v78\n"
    "v_cvt_pk_bf16_f32 v6, v76, v77\n"
    "v_exp_f32 v79, v79\n"
    "v_mfma_f32_32x32x16_bf16 v[96:111], v[132:135], v[212:215], v[96:111]\n"
    "buffer_load_dwordx4 v246, s[28:31], s61 offen offset:1024 sc1 lds\n"
    "v_exp_f32 v80, v80\n"
    "v_cvt_pk_bf16_f32 v7, v78, v79\n"
    "v_exp_f32 v81, v81\n"
    "v_exp_f32 v82, v82\n"
    "v_cvt_pk_bf16_f32 v8, v80, v81\n"
    "v_exp_f32 v83, v83\n"
    "v_mfma_f32_32x32x16_bf16 v[32:47], v[144:147], v[4:7], v[32:47]\n"
    "v_exp_f32 v84, v84\n"
    "v_cvt_pk_bf16_f32 v9, v82, v83\n"
    "v_exp_f32 v85, v85\n"
    "v_exp_f32 v86, v86\n"
    "v_cvt_pk_bf16_f32 v10, v84, v85\n"
    "v_exp_f32 v87, v87\n"
    "v_mfma_f32_32x32x16_bf16 v[112:127], v[132:135], v[216:219], v[112:127]\n"
    "v_exp_f32 v88, v88\n"
    "v_cvt_pk_bf16_f32 v11, v86, v87\n"
    "v_exp_f32 v89, v89\n"
    "v_exp_f32 v90, v90\n"
    "v_cvt_pk_bf16_f32 v12, v88, v89\n"
    "v_exp_f32 v91, v91\n"
    "v_mfma_f32_32x32x16_bf16 v[48:63], v[140:143], v[8:11], v[48:63]\n"
    "v_exp_f32 v92, v92\n"
    "v_cvt_pk_bf16_f32 v13, v90, v91\n"
    "v_exp_f32 v93, v93\n"
    "v_exp_f32 v94, v94\n"
    "v_cvt_pk_bf16_f32 v14, v92, v93\n"
    "v_exp_f32 v95, v95\n"
    "s_waitcnt vmcnt(4)\n"
    "v_cmp_ne_u64 s[54:55], s[48:49], v[226:227]\n"
    "s_nop 1\n"
    "s_cmp_eq_u64 s[54:55], 0\n"
    "s_cbranch_scc0 .Lpoll1\n"
    ".Lpollok1:\n"
    "v_exp_f32 v96, v96\n"
    "v_cvt_pk_bf16_f32 v15, v94, v95\n"
    "v_exp_f32 v97, v97\n"
    "v_exp_f32 v98, v98\n"
    "v_cvt_pk_bf16_f32 v0, v96, v97\n"
    "v_mfma_f32_32x32x16_bf16 v[48:63], v[144:147], v[12:15], v[48:63]\n"
    "ds_read_b64_tr_b16 v[140:141], v230 offset:45184\n"
    "ds_read_b64_tr_b16 v[142:143], v230 offset:45568\n"
    "v_exp_f32 v99, v99\n"
    "v_exp_f32 v100, v100\n"
    "v_cvt_pk_bf16_f32 v1, v98, v99\n"
    "v_exp_f32 v101, v101\n"
    "s_waitcnt lgkmcnt(2)\n"
    "v_mfma_f32_32x32x4_2b_bf16 v[64:95], v[138:139], v[220:221], 0 cbsz:1 abid:0\n"
    "ds_read_b64_tr_b16 v[144:145], v230 offset:45952\n"
    "ds_read_b64_tr_b16 v[146:147], v230 offset:46336\n"
    "v_exp_f32 v102, v102\n"
    "v_cvt_pk_bf16_f32 v2, v100, v101\n"
    "v_exp_f32 v103, v103\n"
    "v_exp_f32 v104, v104\n"
    "v_cvt_pk_bf16_f32 v3, v102, v103\n"
    "v_exp_f32 v105, v105\n"
    "v_exp_f32 v106, v106\n"
    "v_cvt_pk_bf16_f32 v4, v104, v105\n"
    "v_exp_f32 v107, v107\n"
    "v_mfma_f32_32x32x16_bf16 v[32:47], v[184:187], v[0:3], v[32:47]\n"
    "ds_read_b128 v[132:135], v228 offset:41088\n"
    "v_exp_f32 v108, v108\n"
    "v_cvt_pk_bf16_f32 v5, v106, v107\n"
    "v_exp_f32 v109, v109\n"
    "v_exp_f32 v110, v110\n"
    "v_cvt_pk_bf16_f32 v6, v108, v109\n"
    "v_exp_f32 v111, v111\n"
    "v_mfma_f32_32x32x16_bf16 v[64:79], v[128:131], v[212:215], v[64:79]\n"
    "buffer_load_dwordx4 v246, s[28:31], s61 offen offset:2048 sc1 lds\n"
    "v_exp_f32 v112, v112\n"
    "v_cvt_pk_bf16_f32 v7, v110, v111\n"
    "v_exp_f32 v113, v113\n"
    "v_exp_f32 v114, v114\n"
    "v_cvt_pk_bf16_f32 v8, v112, v113\n"
    "v_exp_f32 v115, v115\n"
    "v_mfma_f32_32x32x16_bf16 v[32:47], v[188:191], v[4:7], v[32:47]\n"
    "v_exp_f32 v116, v116\n"
    "v_cvt_pk_bf16_f32 v9, v114, v115\n"
    "v_exp_f32 v117, v117\n"
    "v_exp_f32 v118, v118\n"
    "v_cvt_pk_bf16_f32 v10, v116, v117\n"
    "v_exp_f32 v119, v119\n"
    "v_mfma_f32_32x32x16_bf16 v[80:95], v[128:131], v[216:219], v[80:95]\n"
    "buffer_load_dwordx4 a[0:3], v231, s[24:27], s72 offen offset:0 sc1\n"
    "v_exp_f32 v120, v120\n"
    "v_cvt_pk_bf16_f32 v11, v118, v119\n"
    "v_exp_f32 v121, v121\n"
    "v_exp_f32 v122, v122\n"
    "v_cvt_pk_bf16_f32 v12, v120, v121\n"
    "v_exp_f32 v123, v123\n"
    "v_mfma_f32_32x32x16_bf16 v[48:63], v[184:187], v[8:11], v[48:63]\n"
    "buffer_load_dwordx4 a[4:7], v232, s[24:27], s72 offen offset:0 sc1\n"
    "v_exp_f32 v124, v124\n"
    "v_cvt_pk_bf16_f32 v13, v122, v123\n"
    "v_exp_f32 v125, v125\n"
    "v_exp_f32 v126, v126\n"
    "v_cvt_pk_bf16_f32 v14, v124, v125\n"
    "v_exp_f32 v127, v127\n"
    "s_barrier\n"
    "v_exp_f32 v64, v64\n"
    "v_cvt_pk_bf16_f32 v15, v126, v127\n"
    "v_exp_f32 v65, v65\n"
    "v_exp_f32 v66, v66\n"
    "v_cvt_pk_bf16_f32 v0, v64, v65\n"
    "v_mfma_f32_32x32x16_bf16 v[48:63], v[188:191], v[12:15], v[48:63]\n"
    "ds_read_b64_tr_b16 v[184:185], v230 offset:46720\n"
    "ds_read_b64_tr_b16 v[186:187], v230 offset:47104\n"
    "v_exp_f32 v67, v67\n"
    "v_exp_f32 v68, v68\n"
    "v_cvt_pk_bf16_f32 v1, v66, v67\n"
    "v_exp_f32 v69, v69\n"
    "v_mfma_f32_32x32x4_2b_bf16 v[96:127], v[138:139], v[220:221], 0 cbsz:1 abid:1\n"
    "ds_read_b64_tr_b16 v[188:189], v230 offset:47488\n"
    "ds_read_b64_tr_b16 v[190:191], v230 offset:47872\n"
    "v_exp_f32 v70, v70\n"
    "v_cvt_pk_bf16_f32 v2, v68, v69\n"
    "v_exp_f32 v71, v71\n"
    "v_exp_f32 v72, v72\n"
    "v_cvt_pk_bf16_f32 v3, v70, v71\n"
    "v_exp_f32 v73, v73\n"
    "v_exp_f32 v74, v74\n"
    "v_cvt_pk_bf16_f32 v4, v72, v73\n"
    "v_exp_f32 v75, v75\n"
    "s_waitcnt lgkmcnt(4)\n"
    "v_mfma_f32_32x32x16_bf16 v[32:47], v[140:143], v[0:3], v[32:47]\n"
    "s_add_u32 m0, s60, 0x1bd80\n"
    "s_add_u32 s61, s61, 0x3000\n"
    "ds_read_b128 v[128:131], v228 offset:50688\n"
    "ds_read_b64 v[136:137], v229 offset:60928\n"
    "v_exp_f32 v76, v76\n"
    "v_cvt_pk_bf16_f32 v5, v74, v75\n"
    "v_exp_f32 v77, v77\n"
    "v_exp_f32 v78, v78\n"
    "v_cvt_pk_bf16_f32 v6, v76, v77\n"
    "v_exp_f32 v79, v79\n"
    "v_mfma_f32_32x32x16_bf16 v[96:111], v[132:135], v[212:215], v[96:111]\n"
    "buffer_load_dwordx4 v246, s[28:31], s61 offen offset:0 sc1 lds\n"
    "v_exp_f32 v80, v80\n"
    "v_cvt_pk_bf16_f32 v7, v78, v79\n"
    "v_exp_f32 v81, v81\n"
    "v_exp_f32 v82, v82\n"
    "v_cvt_pk_bf16_f32 v8, v80, v81\n"
    "v_exp_f32 v83, v83\n"
    "v_mfma_f32_32x32x16_bf16 v[32:47], v[144:147], v[4:7], v[32:47]\n"
    "v_exp_f32 v84, v84\n"
    "v_cvt_pk_bf16_f32 v9, v82, v83\n"
    "v_exp_f32 v85, v85\n"
    "v_exp_f32 v86, v86\n"
    "v_cvt_pk_bf16_f32 v10, v84, v85\n"
    "v_exp_f32 v87, v87\n"
    "v_mfma_f32_32x32x16_bf16 v[112:127], v[132:135], v[216:219], v[112:127]\n"
    "buffer_load_dwordx4 a[8:11], v231, s[24:27], s72 offen offset:1024 sc1\n"
    "v_exp_f32 v88, v88\n"
    "v_cvt_pk_bf16_f32 v11, v86, v87\n"
    "v_exp_f32 v89, v89\n"
    "v_exp_f32 v90, v90\n"
    "v_cvt_pk_bf16_f32 v12, v88, v89\n"
    "v_exp_f32 v91, v91\n"
    "v_mfma_f32_32x32x16_bf16 v[48:63], v[140:143], v[8:11], v[48:63]\n"
    "v_exp_f32 v92, v92\n"
    "v_cvt_pk_bf16_f32 v13, v90, v91\n"
    "v_exp_f32 v93, v93\n"
    "v_exp_f32 v94, v94\n"
    "v_cvt_pk_bf16_f32 v14, v92, v93\n"
    "v_exp_f32 v95, v95\n"
    "v_exp_f32 v96, v96\n"
    "v_cvt_pk_bf16_f32 v15, v94, v95\n"
    "v_exp_f32 v97, v97\n"
    "v_exp_f32 v98, v98\n"
    "v_cvt_pk_bf16_f32 v0, v96, v97\n"
    "v_mfma_f32_32x32x16_bf16 v[48:63], v[144:147], v[12:15], v[48:63]\n"
    "ds_read_b64_tr_b16 v[140:141], v230 offset:54784\n"
    "ds_read_b64_tr_b16 v[142:143], v230 offset:55168\n"
    "v_exp_f32 v99, v99\n"
    "v_exp_f32 v100, v100\n"
    "v_cvt_pk_bf16_f32 v1, v98, v99\n"
    "v_exp_f32 v101, v101\n"
    "s_waitcnt lgkmcnt(2)\n"
    "v_mfma_f32_32x32x4_2b_bf16 v[64:95], v[136:137], v[220:221], 0 cbsz:1 abid:0\n"
    "ds_read_b64_tr_b16 v[144:145], v230 offset:55552\n"
    "ds_read_b64_tr_b16 v[146:147], v230 offset:55936\n"
    "v_exp_f32 v102, v102\n"
    "v_cvt_pk_bf16_f32 v2, v100, v101\n"
    "v_exp_f32 v103, v103\n"
    "v_exp_f32 v104, v104\n"
    "v_cvt_pk_bf16_f32 v3, v102, v103\n"
    "v_exp_f32 v105, v105\n"
    "v_exp_f32 v106, v106\n"
    "v_cvt_pk_bf16_f32 v4, v104, v105\n"
    "v_exp_f32 v107, v107\n"
    "v_mfma_f32_32x32x16_bf16 v[32:47], v[184:187], v[0:3], v[32:47]\n"
    "ds_read_b128 v[132:135], v228 offset:51712\n"
    "v_exp_f32 v108, v108\n"
    "v_cvt_pk_bf16_f32 v5, v106, v107\n"
    "v_exp_f32 v109, v109\n"
    "v_exp_f32 v110, v110\n"
    "v_cvt_pk_bf16_f32 v6, v108, v109\n"
    "v_exp_f32 v111, v111\n"
    "v_mfma_f32_32x32x16_bf16 v[64:79], v[128:131], v[212:215], v[64:79]\n"
    "buffer_load_dwordx4 v246, s[28:31], s61 offen offset:1024 sc1 lds\n"
    "v_exp_f32 v112, v112\n"
    "v_cvt_pk_bf16_f32 v7, v110, v111\n"
    "v_exp_f32 v113, v113\n"
    "v_exp_f32 v114, v114\n"
    "v_cvt_pk_bf16_f32 v8, v112, v113\n"
    "v_exp_f32 v115, v115\n"
    "v_mfma_f32_32x32x16_bf16 v[32:47], v[188:191], v[4:7], v[32:47]\n"
    "v_exp_f32 v116, v116\n"
    "v_cvt_pk_bf16_f32 v9, v114, v115\n"
    "v_exp_f32 v117, v117\n"
    "v_exp_f32 v118, v118\n"
    "v_cvt_pk_bf16_f32 v10, v116, v117\n"
    "v_exp_f32 v119, v119\n"
    "v_mfma_f32_32x32x16_bf16 v[80:95], v[128:131], v[216:219], v[80:95]\n"
    "buffer_load_dwordx4 a[12:15], v232, s[24:27], s72 offen offset:1024 sc1\n"
    "v_exp_f32 v120, v120\n"
    "v_cvt_pk_bf16_f32 v11, v118, v119\n"
    "v_exp_f32 v121, v121\n"
    "v_exp_f32 v122, v122\n"
    "v_cvt_pk_bf16_f32 v12, v120, v121\n"
    "v_exp_f32 v123, v123\n"
    "v_mfma_f32_32x32x16_bf16 v[48:63], v[184:187], v[8:11], v[48:63]\n"
    "buffer_load_dwordx4 a[16:19], v231, s[24:27], s72 offen offset:2048 sc1\n"
    "v_exp_f32 v124, v124\n"
    "v_cvt_pk_bf16_f32 v13, v122, v123\n"
    "v_exp_f32 v125, v125\n"
    "v_exp_f32 v126, v126\n"
    "v_cvt_pk_bf16_f32 v14, v124, v125\n"
    "v_exp_f32 v127, v127\n"
    "v_exp_f32 v64, v64\n"
    "v_cvt_pk_bf16_f32 v15, v126, v127\n"
    "v_exp_f32 v65, v65\n"
    "v_exp_f32 v66, v66\n"
    "v_cvt_pk_bf16_f32 v0, v64, v65\n"
    "v_mfma_f32_32x32x16_bf16 v[48:63], v[188:191], v[12:15], v[48:63]\n"
    "ds_read_b64_tr_b16 v[184:185], v230 offset:56320\n"
    "ds_read_b64_tr_b16 v[186:187], v230 offset:56704\n"
    "v_exp_f32 v67, v67\n"
    "v_exp_f32 v68, v68\n"
    "v_cvt_pk_bf16_f32 v1, v66, v67\n"
    "v_exp_f32 v69, v69\n"
    "v_mfma_f32_32x32x4_2b_bf16 v[96:127], v[136:137], v[220:221], 0 cbsz:1 abid:1\n"
    "ds_read_b64_tr_b16 v[188:189], v230 offset:57088\n"
    "ds_read_b64_tr_b16 v[190:191], v230 offset:57472\n"
    "v_exp_f32 v70, v70\n"
    "v_cvt_pk_bf16_f32 v2, v68, v69\n"
    "v_exp_f32 v71, v71\n"
    "v_exp_f32 v72, v72\n"
    "v_cvt_pk_bf16_f32 v3, v70, v71\n"
    "v_exp_f32 v73, v73\n"
    "v_exp_f32 v74, v74\n"
    "v_cvt_pk_bf16_f32 v4, v72, v73\n"
    "v_exp_f32 v75, v75\n"
    "s_waitcnt lgkmcnt(4)\n"
    "v_mfma_f32_32x32x16_bf16 v[32:47], v[140:143], v[0:3], v[32:47]\n"
    "ds_read_b128 v[128:131], v228 offset:52736\n"
    "ds_read_b64 v[138:139], v229 offset:61440\n"
    "v_exp_f32 v76, v76\n"
    "v_cvt_pk_bf16_f32 v5, v74, v75\n"
    "v_exp_f32 v77, v77\n"
    "v_exp_f32 v78, v78\n"
    "v_cvt_pk_bf16_f32 v6, v76, v77\n"
    "v_exp_f32 v79, v79\n"
    "v_mfma_f32_32x32x16_bf16 v[96:111], v[132:135], v[212:215], v[96:111]\n"
    "buffer_load_dwordx4 v246, s[28:31], s61 offen offset:2048 sc1 lds\n"
    "v_exp_f32 v80, v80\n"
    "v_cvt_pk_bf16_f32 v7, v78, v79\n"
    "v_exp_f32 v81, v81\n"
    "v_exp_f32 v82, v82\n"
    "v_cvt_pk_bf16_f32 v8, v80, v81\n"
    "v_exp_f32 v83, v83\n"
    "v_mfma_f32_32x32x16_bf16 v[32:47], v[144:147], v[4:7], v[32:47]\n"
    "v_exp_f32 v84, v84\n"
    "v_cvt_pk_bf16_f32 v9, v82, v83\n"
    "v_exp_f32 v85, v85\n"
    "v_exp_f32 v86, v86\n"
    "v_cvt_pk_bf16_f32 v10, v84, v85\n"
    "v_exp_f32 v87, v87\n"
    "v_mfma_f32_32x32x16_bf16 v[112:127], v[132:135], v[216:219], v[112:127]\n"
    "buffer_load_dwordx4 a[20:23], v232, s[24:27], s72 offen offset:2048 sc1\n"
    "v_exp_f32 v88, v88\n"
    "v_cvt_pk_bf16_f32 v11, v86, v87\n"
    "v_exp_f32 v89, v89\n"
    "v_exp_f32 v90, v90\n"
    "v_cvt_pk_bf16_f32 v12, v88, v89\n"
    "v_exp_f32 v91, v91\n"
    "v_mfma_f32_32x32x16_bf16 v[48:63], v[140:143], v[8:11], v[48:63]\n"
    "v_exp_f32 v92, v92\n"
    "v_cvt_pk_bf16_f32 v13, v90, v91\n"
    "v_exp_f32 v93, v93\n"
    "v_exp_f32 v94, v94\n"
    "v_cvt_pk_bf16_f32 v14, v92, v93\n"
    "v_exp_f32 v95, v95\n"
    "v_exp_f32 v96, v96\n"
    "v_cvt_pk_bf16_f32 v15, v94, v95\n"
    "v_exp_f32 v97, v97\n"
    "v_exp_f32 v98, v98\n"
    "v_cvt_pk_bf16_f32 v0, v96, v97\n"
    "v_mfma_f32_32x32x16_bf16 v[48:63], v[144:147], v[12:15], v[48:63]\n"
    "ds_read_b64_tr_b16 v[140:141], v230 offset:57856\n"
    "ds_read_b64_tr_b16 v[142:143], v230 offset:58240\n"
    "v_exp_f32 v99, v99\n"
    "v_exp_f32 v100, v100\n"
    "v_cvt_pk_bf16_f32 v1, v98, v99\n"
    "v_exp_f32 v101, v101\n"
    "s_waitcnt lgkmcnt(2)\n"
    "v_mfma_f32_32x32x4_2b_bf16 v[64:95], v[138:139], v[220:221], 0 cbsz:1 abid:0\n"
    "ds_read_b64_tr_b16 v[144:145], v230 offset:58624\n"
    "ds_read_b64_tr_b16 v[146:147], v230 offset:59008\n"
    "v_exp_f32 v102, v102\n"
    "v_cvt_pk_bf16_f32 v2, v100, v101\n"
    "v_exp_f32 v103, v103\n"
    "v_exp_f32 v104, v104\n"
    "v_cvt_pk_bf16_f32 v3, v102, v103\n"
    "v_exp_f32 v105, v105\n"
    "v_exp_f32 v106, v106\n"
    "v_cvt_pk_bf16_f32 v4, v104, v105\n"
    "v_exp_f32 v107, v107\n"
    "v_mfma_f32_32x32x16_bf16 v[32:47], v[184:187], v[0:3], v[32:47]\n"
    "s_add_u32 m0, s60, 0x1ef00\n"
    "s_add_u32 s61, s61, 0x3000\n"
    "ds_read_b128 v[132:135], v228 offset:53760\n"
    "v_exp_f32 v108, v108\n"
    "v_cvt_pk_bf16_f32 v5, v106, v107\n"
    "v_exp_f32 v109, v109\n"
    "v_exp_f32 v110, v110\n"
    "v_cvt_pk_bf16_f32 v6, v108, v109\n"
    "v_exp_f32 v111, v111\n"
    "v_mfma_f32_32x32x16_bf16 v[64:79], v[128:131], v[212:215], v[64:79]\n"
    "buffer_load_dwordx4 v246, s[28:31], s61 offen offset:0 sc1 lds\n"
    "v_exp_f32 v112, v112\n"
    "v_cvt_pk_bf16_f32 v7, v110, v111\n"
    "v_exp_f32 v113, v113\n"
    "v_exp_f32 v114, v114\n"
    "v_cvt_pk_bf16_f32 v8, v112, v113\n"
    "v_exp_f32 v115, v115\n"
    "v_mfma_f32_32x32x16_bf16 v[32:47], v[188:191], v[4:7], v[32:47]\n"
    "v_exp_f32 v116, v116\n"
    "v_cvt_pk_bf16_f32 v9, v114, v115\n"
    "v_exp_f32 v117, v117\n"
    "v_exp_f32 v118, v118\n"
    "v_cvt_pk_bf16_f32 v10, v116, v117\n"
    "v_exp_f32 v119, v119\n"
    "v_mfma_f32_32x32x16_bf16 v[80:95], v[128:131], v[216:219], v[80:95]\n"
    "buffer_load_dwordx4 a[24:27], v231, s[24:27], s72 offen offset:3072 sc1\n"
    "v_exp_f32 v120, v120\n"
    "v_cvt_pk_bf16_f32 v11, v118, v119\n"
    "v_exp_f32 v121, v121\n"
    "v_exp_f32 v122, v122\n"
    "v_cvt_pk_bf16_f32 v12, v120, v121\n"
    "v_exp_f32 v123, v123\n"
    "v_mfma_f32_32x32x16_bf16 v[48:63], v[184:187], v[8:11], v[48:63]\n"
    "buffer_load_dwordx4 a[28:31], v232, s[24:27], s72 offen offset:3072 sc1\n"
    "v_exp_f32 v124, v124\n"
    "v_cvt_pk_bf16_f32 v13, v122, v123\n"
    "v_exp_f32 v125, v125\n"
    "v_exp_f32 v126, v126\n"
    "v_cvt_pk_bf16_f32 v14, v124, v125\n"
    "v_exp_f32 v127, v127\n"
    "v_exp_f32 v64, v64\n"
    "v_cvt_pk_bf16_f32 v15, v126, v127\n"
    "v_exp_f32 v65, v65\n"
    "v_exp_f32 v66, v66\n"
    "v_cvt_pk_bf16_f32 v0, v64, v65\n"
    "v_mfma_f32_32x32x16_bf16 v[48:63], v[188:191], v[12:15], v[48:63]\n"
    "ds_read_b64_tr_b16 v[184:185], v230 offset:59392\n"
    "ds_read_b64_tr_b16 v[186:187], v230 offset:59776\n"
    "v_exp_f32 v67, v67\n"
    "v_exp_f32 v68, v68\n"
    "v_cvt_pk_bf16_f32 v1, v66, v67\n"
    "v_exp_f32 v69, v69\n"
    "v_mfma_f32_32x32x4_2b_bf16 v[96:127], v[138:139], v[220:221], 0 cbsz:1 abid:1\n"
    "ds_read_b64_tr_b16 v[188:189], v230 offset:60160\n"
    "ds_read_b64_tr_b16 v[190:191], v230 offset:60544\n"
    "v_exp_f32 v70, v70\n"
    "v_cvt_pk_bf16_f32 v2, v68, v69\n"
    "v_exp_f32 v71, v71\n"
    "v_exp_f32 v72, v72\n"
    "v_cvt_pk_bf16_f32 v3, v70, v71\n"
    "v_exp_f32 v73, v73\n"
    "v_exp_f32 v74, v74\n"
    "v_cvt_pk_bf16_f32 v4, v72, v73\n"
    "v_exp_f32 v75, v75\n"
    "s_waitcnt lgkmcnt(4)\n"
    "v_mfma_f32_32x32x16_bf16 v[32:47], v[140:143], v[0:3], v[32:47]\n"
    "ds_read_b128 v[128:131], v228 offset:63360\n"
    "v_add_u32 v229, 0xff80, v229\n"
    "ds_read_b64 v[136:137], v229 offset:8192\n"
    "v_exp_f32 v76, v76\n"
    "v_cvt_pk_bf16_f32 v5, v74, v75\n"
    "v_exp_f32 v77, v77\n"
    "v_exp_f32 v78, v78\n"
    "v_cvt_pk_bf16_f32 v6, v76, v77\n"
    "v_exp_f32 v79, v79\n"
    "v_mfma_f32_32x32x16_bf16 v[96:111], v[132:135], v[212:215], v[96:111]\n"
    "buffer_load_dwordx4 v246, s[28:31], s61 offen offset:1024 sc1 lds\n"
    "v_exp_f32 v80, v80\n"
    "v_cvt_pk_bf16_f32 v7, v78, v79\n"
    "v_exp_f32 v81, v81\n"
    "v_exp_f32 v82, v82\n"
    "v_cvt_pk_bf16_f32 v8, v80, v81\n"
    "v_exp_f32 v83, v83\n"
    "v_mfma_f32_32x32x16_bf16 v[32:47], v[144:147], v[4:7], v[32:47]\n"
    "s_add_u32 s72, s72, 0x1000\n"
    "v_exp_f32 v84, v84\n"
    "v_cvt_pk_bf16_f32 v9, v82, v83\n"
    "v_exp_f32 v85, v85\n"
    "v_exp_f32 v86, v86\n"
    "v_cvt_pk_bf16_f32 v10, v84, v85\n"
    "v_exp_f32 v87, v87\n"
    "v_mfma_f32_32x32x16_bf16 v[112:127], v[132:135], v[216:219], v[112:127]\n"
    "buffer_load_dwordx4 a[32:35], v231, s[24:27], s72 offen offset:0 sc1\n"
    "v_exp_f32 v88, v88\n"
    "v_cvt_pk_bf16_f32 v11, v86, v87\n"
    "v_exp_f32 v89, v89\n"
    "v_exp_f32 v90, v90\n"
    "v_cvt_pk_bf16_f32 v12, v88, v89\n"
    "v_exp_f32 v91, v91\n"
    "v_mfma_f32_32x32x16_bf16 v[48:63], v[140:143], v[8:11], v[48:63]\n"
    "v_exp_f32 v92, v92\n"
    "v_cvt_pk_bf16_f32 v13, v90, v91\n"
    "v_exp_f32 v93, v93\n"
    "v_exp_f32 v94, v94\n"
    "v_cvt_pk_bf16_f32 v14, v92, v93\n"
    "v_exp_f32 v95, v95\n"
    "v_exp_f32 v96, v96\n"
    "v_cvt_pk_bf16_f32 v15, v94, v95\n"
    "v_exp_f32 v97, v97\n"
    "v_exp_f32 v98, v98\n"
    "v_cvt_pk_bf16_f32 v0, v96, v97\n"
    "v_mfma_f32_32x32x16_bf16 v[48:63], v[144:147], v[12:15], v[48:63]\n"
    "v_add_u32 v230, 0xe780, v230\n"
    "ds_read_b64_tr_b16 v[140:141], v230 offset:8192\n"
    "ds_read_b64_tr_b16 v[142:143], v230 offset:8576\n"
    "v_exp_f32 v99, v99\n"
    "v_exp_f32 v100, v100\n"
    "v_cvt_pk_bf16_f32 v1, v98, v99\n"
    "v_exp_f32 v101, v101\n"
    "s_waitcnt lgkmcnt(2)\n"
    "v_mfma_f32_32x32x4_2b_bf16 v[64:95], v[136:137], v[220:221], 0 cbsz:1 abid:0\n"
    "ds_read_b64_tr_b16 v[144:145], v230 offset:8960\n"
    "ds_read_b64_tr_b16 v[146:147], v230 offset:9344\n"
    "v_exp_f32 v102, v102\n"
    "v_cvt_pk_bf16_f32 v2, v100, v101\n"
    "v_exp_f32 v103, v103\n"
    "v_exp_f32 v104, v104\n"
    "v_cvt_pk_bf16_f32 v3, v102, v103\n"
    "v_exp_f32 v105, v105\n"
    "v_exp_f32 v106, v106\n"
    "v_cvt_pk_bf16_f32 v4, v104, v105\n"
    "v_exp_f32 v107, v107\n"
    "v_mfma_f32_32x32x16_bf16 v[32:47], v[184:187], v[0:3], v[32:47]\n"
    "ds_read_b128 v[132:135], v228 offset:64384\n"
    "v_exp_f32 v108, v108\n"
    "v_cvt_pk_bf16_f32 v5, v106, v107\n"
    "v_exp_f32 v109, v109\n"
    "v_exp_f32 v110, v110\n"
    "v_cvt_pk_bf16_f32 v6, v108, v109\n"
    "v_exp_f32 v111, v111\n"
    "v_mfma_f32_32x32x16_bf16 v[64:79], v[128:131], v[212:215], v[64:79]\n"
    "buffer_load_dwordx4 v246, s[28:31], s61 offen offset:2048 sc1 lds\n"
    "v_exp_f32 v112, v112\n"
    "v_cvt_pk_bf16_f32 v7, v110, v111\n"
    "v_exp_f32 v113, v113\n"
    "v_exp_f32 v114, v114\n"
    "v_cvt_pk_bf16_f32 v8, v112, v113\n"
    "v_exp_f32 v115, v115\n"
    "v_mfma_f32_32x32x16_bf16 v[32:47], v[188:191], v[4:7], v[32:47]\n"
    "v_exp_f32 v116, v116\n"
    "v_cvt_pk_bf16_f32 v9, v114, v115\n"
    "v_exp_f32 v117, v117\n"
    "v_exp_f32 v118, v118\n"
    "v_cvt_pk_bf16_f32 v10, v116, v117\n"
    "v_exp_f32 v119, v119\n"
    "v_mfma_f32_32x32x16_bf16 v[80:95], v[128:131], v[216:219], v[80:95]\n"
    "buffer_load_dwordx4 a[36:39], v232, s[24:27], s72 offen offset:0 sc1\n"
    "v_exp_f32 v120, v120\n"
    "v_cvt_pk_bf16_f32 v11, v118, v119\n"
    "v_exp_f32 v121, v121\n"
    "v_exp_f32 v122, v122\n"
    "v_cvt_pk_bf16_f32 v12, v120, v121\n"
    "v_exp_f32 v123, v123\n"
    "v_mfma_f32_32x32x16_bf16 v[48:63], v[184:187], v[8:11], v[48:63]\n"
    "buffer_load_dwordx4 a[40:43], v231, s[24:27], s72 offen offset:1024 sc1\n"
    "v_exp_f32 v124, v124\n"
    "v_cvt_pk_bf16_f32 v13, v122, v123\n"
    "v_exp_f32 v125, v125\n"
    "v_exp_f32 v126, v126\n"
    "v_cvt_pk_bf16_f32 v14, v124, v125\n"
    "v_exp_f32 v127, v127\n"
    "v_exp_f32 v64, v64\n"
    "v_cvt_pk_bf16_f32 v15, v126, v127\n"
    "v_exp_f32 v65, v65\n"
    "v_exp_f32 v66, v66\n"
    "v_cvt_pk_bf16_f32 v0, v64, v65\n"
    "v_mfma_f32_32x32x16_bf16 v[48:63], v[188:191], v[12:15], v[48:63]\n"
    "ds_read_b64_tr_b16 v[184:185], v230 offset:9728\n"
    "ds_read_b64_tr_b16 v[186:187], v230 offset:10112\n"
    "v_exp_f32 v67, v67\n"
    "v_exp_f32 v68, v68\n"
    "v_cvt_pk_bf16_f32 v1, v66, v67\n"
    "v_exp_f32 v69, v69\n"
    "v_mfma_f32_32x32x4_2b_bf16 v[96:127], v[136:137], v[220:221], 0 cbsz:1 abid:1\n"
    "ds_read_b64_tr_b16 v[188:189], v230 offset:10496\n"
    "ds_read_b64_tr_b16 v[190:191], v230 offset:10880\n"
    "v_exp_f32 v70, v70\n"
    "v_cvt_pk_bf16_f32 v2, v68, v69\n"
    "v_exp_f32 v71, v71\n"
    "v_exp_f32 v72, v72\n"
    "v_cvt_pk_bf16_f32 v3, v70, v71\n"
    "v_exp_f32 v73, v73\n"
    "v_exp_f32 v74, v74\n"
    "v_cvt_pk_bf16_f32 v4, v72, v73\n"
    "v_exp_f32 v75, v75\n"
    "s_waitcnt lgkmcnt(4)\n"
    "v_mfma_f32_32x32x16_bf16 v[32:47], v[140:143], v[0:3], v[32:47]\n"
    "s_add_u32 m0, s60, 0x22080\n"
    "s_add_u32 s61, s61, 0x3000\n"
    "ds_read_b128 v[128:131], v228 offset:65408\n"
    "ds_read_b64 v[138:139], v229 offset:8704\n"
    "v_exp_f32 v76, v76\n"
    "v_cvt_pk_bf16_f32 v5, v74, v75\n"
    "v_exp_f32 v77, v77\n"
    "v_exp_f32 v78, v78\n"
    "v_cvt_pk_bf16_f32 v6, v76, v77\n"
    "v_exp_f32 v79, v79\n"
    "v_mfma_f32_32x32x16_bf16 v[96:111], v[132:135], v[212:215], v[96:111]\n"
    "buffer_load_dwordx4 v246, s[28:31], s61 offen offset:0 sc1 lds\n"
    "v_exp_f32 v80, v80\n"
    "v_cvt_pk_bf16_f32 v7, v78, v79\n"
    "v_exp_f32 v81, v81\n"
    "v_exp_f32 v82, v82\n"
    "v_cvt_pk_bf16_f32 v8, v80, v81\n"
    "v_exp_f32 v83, v83\n"
    "v_mfma_f32_32x32x16_bf16 v[32:47], v[144:147], v[4:7], v[32:47]\n"
    "v_exp_f32 v84, v84\n"
    "v_cvt_pk_bf16_f32 v9, v82, v83\n"
    "v_exp_f32 v85, v85\n"
    "v_exp_f32 v86, v86\n"
    "v_cvt_pk_bf16_f32 v10, v84, v85\n"
    "v_exp_f32 v87, v87\n"
    "v_mfma_f32_32x32x16_bf16 v[112:127], v[132:135], v[216:219], v[112:127]\n"
    "buffer_load_dwordx4 a[44:47], v232, s[24:27], s72 offen offset:1024 sc1\n"
    "v_exp_f32 v88, v88\n"
    "v_cvt_pk_bf16_f32 v11, v86, v87\n"
    "v_exp_f32 v89, v89\n"
    "v_exp_f32 v90, v90\n"
    "v_cvt_pk_bf16_f32 v12, v88, v89\n"
    "v_exp_f32 v91, v91\n"
    "v_mfma_f32_32x32x16_bf16 v[48:63], v[140:143], v[8:11], v[48:63]\n"
    "v_exp_f32 v92, v92\n"
    "v_cvt_pk_bf16_f32 v13, v90, v91\n"
    "v_exp_f32 v93, v93\n"
    "v_exp_f32 v94, v94\n"
    "v_cvt_pk_bf16_f32 v14, v92, v93\n"
    "v_exp_f32 v95, v95\n"
    "v_exp_f32 v96, v96\n"
    "v_cvt_pk_bf16_f32 v15, v94, v95\n"
    "v_exp_f32 v97, v97\n"
    "v_exp_f32 v98, v98\n"
    "v_cvt_pk_bf16_f32 v0, v96, v97\n"
    "v_mfma_f32_32x32x16_bf16 v[48:63], v[144:147], v[12:15], v[48:63]\n"
    "ds_read_b64_tr_b16 v[140:141], v230 offset:11264\n"
    "ds_read_b64_tr_b16 v[142:143], v230 offset:11648\n"
    "v_exp_f32 v99, v99\n"
    "v_exp_f32 v100, v100\n"
    "v_cvt_pk_bf16_f32 v1, v98, v99\n"
    "v_exp_f32 v101, v101\n"
    "s_waitcnt lgkmcnt(2)\n"
    "v_mfma_f32_32x32x4_2b_bf16 v[64:95], v[138:139], v[220:221], 0 cbsz:1 abid:0\n"
    "ds_read_b64_tr_b16 v[144:145], v230 offset:12032\n"
    "ds_read_b64_tr_b16 v[146:147], v230 offset:12416\n"
    "v_exp_f32 v102, v102\n"
    "v_cvt_pk_bf16_f32 v2, v100, v101\n"
    "v_exp_f32 v103, v103\n"
    "v_exp_f32 v104, v104\n"
    "v_cvt_pk_bf16_f32 v3, v102, v103\n"
    "v_exp_f32 v105, v105\n"
    "v_exp_f32 v106, v106\n"
    "v_cvt_pk_bf16_f32 v4, v104, v105\n"
    "v_exp_f32 v107, v107\n"
    "v_mfma_f32_32x32x16_bf16 v[32:47], v[184:187], v[0:3], v[32:47]\n"
    "v_add_u32 v228, 0xe380, v228\n"
    "ds_read_b128 v[132:135], v228 offset:8192\n"
    "v_exp_f32 v108, v108\n"
    "v_cvt_pk_bf16_f32 v5, v106, v107\n"
    "v_exp_f32 v109, v109\n"
    "v_exp_f32 v110, v110\n"
    "v_cvt_pk_bf16_f32 v6, v108, v109\n"
    "v_exp_f32 v111, v111\n"
    "v_mfma_f32_32x32x16_bf16 v[64:79], v[128:131], v[212:215], v[64:79]\n"
    "buffer_load_dwordx4 v246, s[28:31], s61 offen offset:1024 sc1 lds\n"
    "v_exp_f32 v112, v112\n"
    "v_cvt_pk_bf16_f32 v7, v110, v111\n"
    "v_exp_f32 v113, v113\n"
    "v_exp_f32 v114, v114\n"
    "v_cvt_pk_bf16_f32 v8, v112, v113\n"
    "v_exp_f32 v115, v115\n"
    "v_mfma_f32_32x32x16_bf16 v[32:47], v[188:191], v[4:7], v[32:47]\n"
    "v_exp_f32 v116, v116\n"
    "v_cvt_pk_bf16_f32 v9, v114, v115\n"
    "v_exp_f32 v117, v117\n"
    "v_exp_f32 v118, v118\n"
    "v_cvt_pk_bf16_f32 v10, v116, v117\n"
    "v_exp_f32 v119, v119\n"
    "v_mfma_f32_32x32x16_bf16 v[80:95], v[128:131], v[216:219], v[80:95]\n"
    "buffer_load_dwordx4 a[48:51], v231, s[24:27], s72 offen offset:2048 sc1\n"
    "v_exp_f32 v120, v120\n"
    "v_cvt_pk_bf16_f32 v11, v118, v119\n"
    "v_exp_f32 v121, v121\n"
    "v_exp_f32 v122, v122\n"
    "v_cvt_pk_bf16_f32 v12, v120, v121\n"
    "v_exp_f32 v123, v123\n"
    "v_mfma_f32_32x32x16_bf16 v[48:63], v[184:187], v[8:11], v[48:63]\n"
    "buffer_load_dwordx4 a[52:55], v232, s[24:27], s72 offen offset:2048 sc1\n"
    "v_exp_f32 v124, v124\n"
    "v_cvt_pk_bf16_f32 v13, v122, v123\n"
    "v_exp_f32 v125, v125\n"
    "v_exp_f32 v126, v126\n"
    "v_cvt_pk_bf16_f32 v14, v124, v125\n"
    "v_exp_f32 v127, v127\n"
    "v_exp_f32 v64, v64\n"
    "v_cvt_pk_bf16_f32 v15, v126, v127\n"
    "v_exp_f32 v65, v65\n"
    "v_exp_f32 v66, v66\n"
    "v_cvt_pk_bf16_f32 v0, v64, v65\n"
    "v_mfma_f32_32x32x16_bf16 v[48:63], v[188:191], v[12:15], v[48:63]\n"
    "ds_read_b64_tr_b16 v[184:185], v230 offset:12800\n"
    "ds_read_b64_tr_b16 v[186:187], v230 offset:13184\n"
    "v_exp_f32 v67, v67\n"
    "v_exp_f32 v68, v68\n"
    "v_cvt_pk_bf16_f32 v1, v66, v67\n"
    "v_exp_f32 v69, v69\n"
    "v_mfma_f32_32x32x4_2b_bf16 v[96:127], v[138:139], v[220:221], 0 cbsz:1 abid:1\n"
    "ds_read_b64_tr_b16 v[188:189], v230 offset:13568\n"
    "ds_read_b64_tr_b16 v[190:191], v230 offset:13952\n"
    "v_exp_f32 v70, v70\n"
    "v_cvt_pk_bf16_f32 v2, v68, v69\n"
    "v_exp_f32 v71, v71\n"
    "v_exp_f32 v72, v72\n"
    "v_cvt_pk_bf16_f32 v3, v70, v71\n"
    "v_exp_f32 v73, v73\n"
    "v_exp_f32 v74, v74\n"
    "v_cvt_pk_bf16_f32 v4, v72, v73\n"
    "v_exp_f32 v75, v75\n"
    "s_waitcnt lgkmcnt(4)\n"
    "v_mfma_f32_32x32x16_bf16 v[32:47], v[140:143], v[0:3], v[32:47]\n"
    "ds_read_b128 v[128:131], v228 offset:17792\n"
    "ds_read_b64 v[136:137], v229 offset:20864\n"
    "v_exp_f32 v76, v76\n"
    "v_cvt_pk_bf16_f32 v5, v74, v75\n"
    "v_exp_f32 v77, v77\n"
    "v_exp_f32 v78, v78\n"
    "v_cvt_pk_bf16_f32 v6, v76, v77\n"
    "v_exp_f32 v79, v79\n"
    "v_mfma_f32_32x32x16_bf16 v[96:111], v[132:135], v[212:215], v[96:111]\n"
    "buffer_load_dwordx4 v246, s[28:31], s61 offen offset:2048 sc1 lds\n"
    "v_exp_f32 v80, v80\n"
    "v_cvt_pk_bf16_f32 v7, v78, v79\n"
    "v_exp_f32 v81, v81\n"
    "v_exp_f32 v82, v82\n"
    "v_cvt_pk_bf16_f32 v8, v80, v81\n"
    "v_exp_f32 v83, v83\n"
    "v_mfma_f32_32x32x16_bf16 v[32:47], v[144:147], v[4:7], v[32:47]\n"
    "v_exp_f32 v84, v84\n"
    "v_cvt_pk_bf16_f32 v9, v82, v83\n"
    "v_exp_f32 v85, v85\n"
    "v_exp_f32 v86, v86\n"
    "v_cvt_pk_bf16_f32 v10, v84, v85\n"
    "v_exp_f32 v87, v87\n"
    "v_mfma_f32_32x32x16_bf16 v[112:127], v[132:135], v[216:219], v[112:127]\n"
    "buffer_load_dwordx4 a[56:59], v231, s[24:27], s72 offen offset:3072 sc1\n"
    "v_exp_f32 v88, v88\n"
    "v_cvt_pk_bf16_f32 v11, v86, v87\n"
    "v_exp_f32 v89, v89\n"
    "v_exp_f32 v90, v90\n"
    "v_cvt_pk_bf16_f32 v12, v88, v89\n"
    "v_exp_f32 v91, v91\n"
    "v_mfma_f32_32x32x16_bf16 v[48:63], v[140:143], v[8:11], v[48:63]\n"
    "v_exp_f32 v92, v92\n"
    "v_cvt_pk_bf16_f32 v13, v90, v91\n"
    "v_exp_f32 v93, v93\n"
    "v_exp_f32 v94, v94\n"
    "v_cvt_pk_bf16_f32 v14, v92, v93\n"
    "v_exp_f32 v95, v95\n"
    "v_exp_f32 v96, v96\n"
    "v_cvt_pk_bf16_f32 v15, v94, v95\n"
    "v_exp_f32 v97, v97\n"
    "v_exp_f32 v98, v98\n"
    "v_cvt_pk_bf16_f32 v0, v96, v97\n"
    "v_mfma_f32_32x32x16_bf16 v[48:63], v[144:147], v[12:15], v[48:63]\n"
    "ds_read_b64_tr_b16 v[140:141], v230 offset:20864\n"
    "ds_read_b64_tr_b16 v[142:143], v230 offset:21248\n"
    "v_exp_f32 v99, v99\n"
    "v_exp_f32 v100, v100\n"
    "v_cvt_pk_bf16_f32 v1, v98, v99\n"
    "v_exp_f32 v101, v101\n"
    "s_waitcnt lgkmcnt(2)\n"
    "v_mfma_f32_32x32x4_2b_bf16 v[64:95], v[136:137], v[220:221], 0 cbsz:1 abid:0\n"
    "ds_read_b64_tr_b16 v[144:145], v230 offset:21632\n"
    "ds_read_b64_tr_b16 v[146:147], v230 offset:22016\n"
    "v_exp_f32 v102, v102\n"
    "v_cvt_pk_bf16_f32 v2, v100, v101\n"
    "v_exp_f32 v103, v103\n"
    "v_exp_f32 v104, v104\n"
    "v_cvt_pk_bf16_f32 v3, v102, v103\n"
    "v_exp_f32 v105, v105\n"
    "v_exp_f32 v106, v106\n"
    "v_cvt_pk_bf16_f32 v4, v104, v105\n"
    "v_exp_f32 v107, v107\n"
    "v_mfma_f32_32x32x16_bf16 v[32:47], v[184:187], v[0:3], v[32:47]\n"
    "s_add_u32 m0, s60, 0x0\n"
    "s_add_u32 s61, s61, 0x3000\n"
    "ds_read_b128 v[132:135], v228 offset:18816\n"
    "v_exp_f32 v108, v108\n"
    "v_cvt_pk_bf16_f32 v5, v106, v107\n"
    "v_exp_f32 v109, v109\n"
    "v_exp_f32 v110, v110\n"
    "v_cvt_pk_bf16_f32 v6, v108, v109\n"
    "v_exp_f32 v111, v111\n"
    "v_mfma_f32_32x32x16_bf16 v[64:79], v[128:131], v[212:215], v[64:79]\n"
    "buffer_load_dwordx4 v246, s[28:31], s61 offen offset:0 sc1 lds\n"
    "v_exp_f32 v112, v112\n"
    "v_cvt_pk_bf16_f32 v7, v110, v111\n"
    "v_exp_f32 v113, v113\n"
    "v_exp_f32 v114, v114\n"
    "v_cvt_pk_bf16_f32 v8, v112, v113\n"
    "v_exp_f32 v115, v115\n"
    "v_mfma_f32_32x32x16_bf16 v[32:47], v[188:191], v[4:7], v[32:47]\n"
    "v_exp_f32 v116, v116\n"
    "v_cvt_pk_bf16_f32 v9, v114, v115\n"
    "v_exp_f32 v117, v117\n"
    "v_exp_f32 v118, v118\n"
    "v_cvt_pk_bf16_f32 v10, v116, v117\n"
    "v_exp_f32 v119, v119\n"
    "v_mfma_f32_32x32x16_bf16 v[80:95], v[128:131], v[216:219], v[80:95]\n"
    "buffer_load_dwordx4 a[60:63], v232, s[24:27], s72 offen offset:3072 sc1\n"
    "s_add_u32 s72, s72, 0x1000\n"
    "v_exp_f32 v120, v120\n"
    "v_cvt_pk_bf16_f32 v11, v118, v119\n"
    "v_exp_f32 v121, v121\n"
    "v_exp_f32 v122, v122\n"
    "v_cvt_pk_bf16_f32 v12, v120, v121\n"
    "v_exp_f32 v123, v123\n"
    "v_mfma_f32_32x32x16_bf16 v[48:63], v[184:187], v[8:11], v[48:63]\n"
    "buffer_load_dwordx4 a[64:67], v231, s[24:27], s72 offen offset:0 sc1\n"
    "v_exp_f32 v124, v124\n"
    "v_cvt_pk_bf16_f32 v13, v122, v123\n"
    "v_exp_f32 v125, v125\n"
    "v_exp_f32 v126, v126\n"
    "v_cvt_pk_bf16_f32 v14, v124, v125\n"
    "v_exp_f32 v127, v127\n"
    "v_exp_f32 v64, v64\n"
    "v_cvt_pk_bf16_f32 v15, v126, v127\n"
    "v_exp_f32 v65, v65\n"
    "v_exp_f32 v66, v66\n"
    "v_cvt_pk_bf16_f32 v0, v64, v65\n"
    "v_mfma_f32_32x32x16_bf16 v[48:63], v[188:191], v[12:15], v[48:63]\n"
    "ds_read_b64_tr_b16 v[184:185], v230 offset:22400\n"
    "ds_read_b64_tr_b16 v[186:187], v230 offset:22784\n"
    "v_exp_f32 v67, v67\n"
    "v_exp_f32 v68, v68\n"
    "v_cvt_pk_bf16_f32 v1, v66, v67\n"
    "v_exp_f32 v69, v69\n"
    "v_mfma_f32_32x32x4_2b_bf16 v[96:127], v[136:137], v[220:221], 0 cbsz:1 abid:1\n"
    "ds_read_b64_tr_b16 v[188:189], v230 offset:23168\n"
    "ds_read_b64_tr_b16 v[190:191], v230 offset:23552\n"
    "v_exp_f32 v70, v70\n"
    "v_cvt_pk_bf16_f32 v2, v68, v69\n"
    "v_exp_f32 v71, v71\n"
    "v_exp_f32 v72, v72\n"
    "v_cvt_pk_bf16_f32 v3, v70, v71\n"
    "v_exp_f32 v73, v73\n"
    "v_exp_f32 v74, v74\n"
    "v_cvt_pk_bf16_f32 v4, v72, v73\n"
    "v_exp_f32 v75, v75\n"
    "s_waitcnt lgkmcnt(4)\n"
    "v_mfma_f32_32x32x16_bf16 v[32:47], v[140:143], v[0:3], v[32:47]\n"
    "ds_read_b128 v[128:131], v228 offset:19840\n"
    "ds_read_b64 v[138:139], v229 offset:21376\n"
    "v_exp_f32 v76, v76\n"
    "v_cvt_pk_bf16_f32 v5, v74, v75\n"
    "v_exp_f32 v77, v77\n"
    "v_exp_f32 v78, v78\n"
    "v_cvt_pk_bf16_f32 v6, v76, v77\n"
    "v_exp_f32 v79, v79\n"
    "v_mfma_f32_32x32x16_bf16 v[96:111], v[132:135], v[212:215], v[96:111]\n"
    "buffer_load_dwordx4 v246, s[28:31], s61 offen offset:1024 sc1 lds\n"
    "v_exp_f32 v80, v80\n"
    "v_cvt_pk_bf16_f32 v7, v78, v79\n"
    "v_exp_f32 v81, v81\n"
    "v_exp_f32 v82, v82\n"
    "v_cvt_pk_bf16_f32 v8, v80, v81\n"
    "v_exp_f32 v83, v83\n"
    "v_mfma_f32_32x32x16_bf16 v[32:47], v[144:147], v[4:7], v[32:47]\n"
    "v_exp_f32 v84, v84\n"
    "v_cvt_pk_bf16_f32 v9, v82, v83\n"
    "v_exp_f32 v85, v85\n"
    "v_exp_f32 v86, v86\n"
    "v_cvt_pk_bf16_f32 v10, v84, v85\n"
    "v_exp_f32 v87, v87\n"
    "v_mfma_f32_32x32x16_bf16 v[112:127], v[132:135], v[216:219], v[112:127]\n"
    "buffer_load_dwordx4 a[68:71], v232, s[24:27], s72 offen offset:0 sc1\n"
    "v_exp_f32 v88, v88\n"
    "v_cvt_pk_bf16_f32 v11, v86, v87\n"
    "v_exp_f32 v89, v89\n"
    "v_exp_f32 v90, v90\n"
    "v_cvt_pk_bf16_f32 v12, v88, v89\n"
    "v_exp_f32 v91, v91\n"
    "v_mfma_f32_32x32x16_bf16 v[48:63], v[140:143], v[8:11], v[48:63]\n"
    "v_exp_f32 v92, v92\n"
    "v_cvt_pk_bf16_f32 v13, v90, v91\n"
    "v_exp_f32 v93, v93\n"
    "v_exp_f32 v94, v94\n"
    "v_cvt_pk_bf16_f32 v14, v92, v93\n"
    "v_exp_f32 v95, v95\n"
    "v_exp_f32 v96, v96\n"
    "v_cvt_pk_bf16_f32 v15, v94, v95\n"
    "v_exp_f32 v97, v97\n"
    "v_exp_f32 v98, v98\n"
    "v_cvt_pk_bf16_f32 v0, v96, v97\n"
    "v_mfma_f32_32x32x16_bf16 v[48:63], v[144:147], v[12:15], v[48:63]\n"
    "ds_read_b64_tr_b16 v[140:141], v230 offset:23936\n"
    "ds_read_b64_tr_b16 v[142:143], v230 offset:24320\n"
    "v_exp_f32 v99, v99\n"
    "v_exp_f32 v100, v100\n"
    "v_cvt_pk_bf16_f32 v1, v98, v99\n"
    "v_exp_f32 v101, v101\n"
    "s_waitcnt lgkmcnt(2)\n"
    "v_mfma_f32_32x32x4_2b_bf16 v[64:95], v[138:139], v[220:221], 0 cbsz:1 abid:0\n"
    "ds_read_b64_tr_b16 v[144:145], v230 offset:24704\n"
    "ds_read_b64_tr_b16 v[146:147], v230 offset:25088\n"
    "v_exp_f32 v102, v102\n"
    "v_cvt_pk_bf16_f32 v2, v100, v101\n"
    "v_exp_f32 v103, v103\n"
    "v_exp_f32 v104, v104\n"
    "v_cvt_pk_bf16_f32 v3, v102, v103\n"
    "v_exp_f32 v105, v105\n"
    "v_exp_f32 v106, v106\n"
    "v_cvt_pk_bf16_f32 v4, v104, v105\n"
    "v_exp_f32 v107, v107\n"
    "v_mfma_f32_32x32x16_bf16 v[32:47], v[184:187], v[0:3], v[32:47]\n"
    "ds_read_b128 v[132:135], v228 offset:20864\n"
    "v_exp_f32 v108, v108\n"
    "v_cvt_pk_bf16_f32 v5, v106, v107\n"
    "v_exp_f32 v109, v109\n"
    "v_exp_f32 v110, v110\n"
    "v_cvt_pk_bf16_f32 v6, v108, v109\n"
    "v_exp_f32 v111, v111\n"
    "v_mfma_f32_32x32x16_bf16 v[64:79], v[128:131], v[212:215], v[64:79]\n"
    "buffer_load_dwordx4 v246, s[28:31], s61 offen offset:2048 sc1 lds\n"
    "v_exp_f32 v112, v112\n"
    "v_cvt_pk_bf16_f32 v7, v110, v111\n"
    "v_exp_f32 v113, v113\n"
    "v_exp_f32 v114, v114\n"
    "v_cvt_pk_bf16_f32 v8, v112, v113\n"
    "v_exp_f32 v115, v115\n"
    "v_mfma_f32_32x32x16_bf16 v[32:47], v[188:191], v[4:7], v[32:47]\n"
    "v_exp_f32 v116, v116\n"
    "v_cvt_pk_bf16_f32 v9, v114, v115\n"
    "v_exp_f32 v117, v117\n"
    "v_exp_f32 v118, v118\n"
    "v_cvt_pk_bf16_f32 v10, v116, v117\n"
    "v_exp_f32 v119, v119\n"
    "v_mfma_f32_32x32x16_bf16 v[80:95], v[128:131], v[216:219], v[80:95]\n"
    "buffer_load_dwordx4 a[72:75], v231, s[24:27], s72 offen offset:1024 sc1\n"
    "v_exp_f32 v120, v120\n"
    "v_cvt_pk_bf16_f32 v11, v118, v119\n"
    "v_exp_f32 v121, v121\n"
    "v_exp_f32 v122, v122\n"
    "v_cvt_pk_bf16_f32 v12, v120, v121\n"
    "v_exp_f32 v123, v123\n"
    "v_mfma_f32_32x32x16_bf16 v[48:63], v[184:187], v[8:11], v[48:63]\n"
    "buffer_load_dwordx4 a[76:79], v232, s[24:27], s72 offen offset:1024 sc1\n"
    "v_exp_f32 v124, v124\n"
    "v_cvt_pk_bf16_f32 v13, v122, v123\n"
    "v_exp_f32 v125, v125\n"
    "v_exp_f32 v126, v126\n"
    "v_cvt_pk_bf16_f32 v14, v124, v125\n"
    "v_exp_f32 v127, v127\n"
    "s_waitcnt vmcnt(24)\n"
    "s_barrier\n"
    "v_exp_f32 v64, v64\n"
    "v_cvt_pk_bf16_f32 v15, v126, v127\n"
    "v_exp_f32 v65, v65\n"
    "v_exp_f32 v66, v66\n"
    "v_cvt_pk_bf16_f32 v0, v64, v65\n"
    "v_mfma_f32_32x32x16_bf16 v[48:63], v[188:191], v[12:15], v[48:63]\n"
    "ds_read_b64_tr_b16 v[184:185], v230 offset:25472\n"
    "ds_read_b64_tr_b16 v[186:187], v230 offset:25856\n"
    "v_exp_f32 v67, v67\n"
    "v_exp_f32 v68, v68\n"
    "v_cvt_pk_bf16_f32 v1, v66, v67\n"
    "v_exp_f32 v69, v69\n"
    "v_mfma_f32_32x32x4_2b_bf16 v[96:127], v[138:139], v[220:221], 0 cbsz:1 abid:1\n"
    "ds_read_b64_tr_b16 v[188:189], v230 offset:26240\n"
    "ds_read_b64_tr_b16 v[190:191], v230 offset:26624\n"
    "v_exp_f32 v70, v70\n"
    "v_cvt_pk_bf16_f32 v2, v68, v69\n"
    "v_exp_f32 v71, v71\n"
    "v_exp_f32 v72, v72\n"
    "v_cvt_pk_bf16_f32 v3, v70, v71\n"
    "v_exp_f32 v73, v73\n"
    "v_exp_f32 v74, v74\n"
    "v_cvt_pk_bf16_f32 v4, v72, v73\n"
    "v_exp_f32 v75, v75\n"
    "s_waitcnt lgkmcnt(4)\n"
    "v_mfma_f32_32x32x16_bf16 v[32:47], v[140:143], v[0:3], v[32:47]\n"
    "s_add_u32 m0, s60, 0x3180\n"
    "s_add_u32 s61, s61, 0x3000\n"
    "ds_read_b128 v[128:131], v228 offset:30464\n"
    "ds_read_b64 v[136:137], v229 offset:33536\n"
    "v_exp_f32 v76, v76\n"
    "v_cvt_pk_bf16_f32 v5, v74, v75\n"
    "v_exp_f32 v77, v77\n"
    "v_exp_f32 v78, v78\n"
    "v_cvt_pk_bf16_f32 v6, v76, v77\n"
    "v_exp_f32 v79, v79\n"
    "v_mfma_f32_32x32x16_bf16 v[96:111], v[132:135], v[212:215], v[96:111]\n"
    "buffer_load_dwordx4 v246, s[28:31], s61 offen offset:0 sc1 lds\n"
    "v_exp_f32 v80, v80\n"
    "v_cvt_pk_bf16_f32 v7, v78, v79\n"
    "v_exp_f32 v81, v81\n"
    "v_exp_f32 v82, v82\n"
    "v_cvt_pk_bf16_f32 v8, v80, v81\n"
    "v_exp_f32 v83, v83\n"
    "v_mfma_f32_32x32x16_bf16 v[32:47], v[144:147], v[4:7], v[32:47]\n"
    "v_exp_f32 v84, v84\n"
    "v_cvt_pk_bf16_f32 v9, v82, v83\n"
    "v_exp_f32 v85, v85\n"
    "v_exp_f32 v86, v86\n"
    "v_cvt_pk_bf16_f32 v10, v84, v85\n"
    "v_exp_f32 v87, v87\n"
    "v_mfma_f32_32x32x16_bf16 v[112:127], v[132:135], v[216:219], v[112:127]\n"
    "buffer_load_dwordx4 a[80:83], v231, s[24:27], s72 offen offset:2048 sc1\n"
    "v_exp_f32 v88, v88\n"
    "v_cvt_pk_bf16_f32 v11, v86, v87\n"
    "v_exp_f32 v89, v89\n"
    "v_exp_f32 v90, v90\n"
    "v_cvt_pk_bf16_f32 v12, v88, v89\n"
    "v_exp_f32 v91, v91\n"
    "v_mfma_f32_32x32x16_bf16 v[48:63], v[140:143], v[8:11], v[48:63]\n"
    "v_exp_f32 v92, v92\n"
    "v_cvt_pk_bf16_f32 v13, v90, v91\n"
    "v_exp_f32 v93, v93\n"
    "v_exp_f32 v94, v94\n"
    "v_cvt_pk_bf16_f32 v14, v92, v93\n"
    "v_exp_f32 v95, v95\n"
    "v_exp_f32 v96, v96\n"
    "v_cvt_pk_bf16_f32 v15, v94, v95\n"
    "v_exp_f32 v97, v97\n"
    "v_exp_f32 v98, v98\n"
    "v_cvt_pk_bf16_f32 v0, v96, v97\n"
    "v_mfma_f32_32x32x16_bf16 v[48:63], v[144:147], v[12:15], v[48:63]\n"
    "ds_read_b64_tr_b16 v[140:141], v230 offset:33536\n"
    "ds_read_b64_tr_b16 v[142:143], v230 offset:33920\n"
    "v_exp_f32 v99, v99\n"
    "v_exp_f32 v100, v100\n"
    "v_cvt_pk_bf16_f32 v1, v98, v99\n"
    "v_exp_f32 v101, v101\n"
    "s_waitcnt lgkmcnt(2)\n"
    "v_mfma_f32_32x32x4_2b_bf16 v[64:95], v[136:137], v[220:221], 0 cbsz:1 abid:0\n"
    "ds_read_b64_tr_b16 v[144:145], v230 offset:34304\n"
    "ds_read_b64_tr_b16 v[146:147], v230 offset:34688\n"
    "v_exp_f32 v102, v102\n"
    "v_cvt_pk_bf16_f32 v2, v100, v101\n"
    "v_exp_f32 v103, v103\n"
    "v_exp_f32 v104, v104\n"
    "v_cvt_pk_bf16_f32 v3, v102, v103\n"
    "v_exp_f32 v105, v105\n"
    "v_exp_f32 v106, v106\n"
    "v_cvt_pk_bf16_f32 v4, v104, v105\n"
    "v_exp_f32 v107, v107\n"
    "v_mfma_f32_32x32x16_bf16 v[32:47], v[184:187], v[0:3], v[32:47]\n"
    "ds_read_b128 v[132:135], v228 offset:31488\n"
    "v_exp_f32 v108, v108\n"
    "v_cvt_pk_bf16_f32 v5, v106, v107\n"
    "v_exp_f32 v109, v109\n"
    "v_exp_f32 v110, v110\n"
    "v_cvt_pk_bf16_f32 v6, v108, v109\n"
    "v_exp_f32 v111, v111\n"
    "v_mfma_f32_32x32x16_bf16 v[64:79], v[128:131], v[212:215], v[64:79]\n"
    "buffer_load_dwordx4 v246, s[28:31], s61 offen offset:1024 sc1 lds\n"
    "v_exp_f32 v112, v112\n"
    "v_cvt_pk_bf16_f32 v7, v110, v111\n"
    "v_exp_f32 v113, v113\n"
    "v_exp_f32 v114, v114\n"
    "v_cvt_pk_bf16_f32 v8, v112, v113\n"
    "v_exp_f32 v115, v115\n"
    "v_mfma_f32_32x32x16_bf16 v[32:47], v[188:191], v[4:7], v[32:47]\n"
    "v_exp_f32 v116, v116\n"
    "v_cvt_pk_bf16_f32 v9, v114, v115\n"
    "v_exp_f32 v117, v117\n"
    "v_exp_f32 v118, v118\n"
    "v_cvt_pk_bf16_f32 v10, v116, v117\n"
    "v_exp_f32 v119, v119\n"
    "v_mfma_f32_32x32x16_bf16 v[80:95], v[128:131], v[216:219], v[80:95]\n"
    "buffer_load_dwordx4 a[84:87], v232, s[24:27], s72 offen offset:2048 sc1\n"
    "v_exp_f32 v120, v120\n"
    "v_cvt_pk_bf16_f32 v11, v118, v119\n"
    "v_exp_f32 v121, v121\n"
    "v_exp_f32 v122, v122\n"
    "v_cvt_pk_bf16_f32 v12, v120, v121\n"
    "v_exp_f32 v123, v123\n"
    "v_mfma_f32_32x32x16_bf16 v[48:63], v[184:187], v[8:11], v[48:63]\n"
    "buffer_load_dwordx4 a[88:91], v231, s[24:27], s72 offen offset:3072 sc1\n"
    "v_exp_f32 v124, v124\n"
    "v_cvt_pk_bf16_f32 v13, v122, v123\n"
    "v_exp_f32 v125, v125\n"
    "v_exp_f32 v126, v126\n"
    "v_cvt_pk_bf16_f32 v14, v124, v125\n"
    "v_exp_f32 v127, v127\n"
    "v_exp_f32 v64, v64\n"
    "v_cvt_pk_bf16_f32 v15, v126, v127\n"
    "v_exp_f32 v65, v65\n"
    "v_exp_f32 v66, v66\n"
    "v_cvt_pk_bf16_f32 v0, v64, v65\n"
    "v_mfma_f32_32x32x16_bf16 v[48:63], v[188:191], v[12:15], v[48:63]\n"
    "ds_read_b64_tr_b16 v[184:185], v230 offset:35072\n"
    "ds_read_b64_tr_b16 v[186:187], v230 offset:35456\n"
    "v_exp_f32 v67, v67\n"
    "v_exp_f32 v68, v68\n"
    "v_cvt_pk_bf16_f32 v1, v66, v67\n"
    "v_exp_f32 v69, v69\n"
    "v_mfma_f32_32x32x4_2b_bf16 v[96:127], v[136:137], v[220:221], 0 cbsz:1 abid:1\n"
    "ds_read_b64_tr_b16 v[188:189], v230 offset:35840\n"
    "ds_read_b64_tr_b16 v[190:191], v230 offset:36224\n"
    "v_exp_f32 v70, v70\n"
    "v_cvt_pk_bf16_f32 v2, v68, v69\n"
    "v_exp_f32 v71, v71\n"
    "v_exp_f32 v72, v72\n"
    "v_cvt_pk_bf16_f32 v3, v70, v71\n"
    "v_exp_f32 v73, v73\n"
    "v_exp_f32 v74, v74\n"
    "v_cvt_pk_bf16_f32 v4, v72, v73\n"
    "v_exp_f32 v75, v75\n"
    "s_waitcnt lgkmcnt(4)\n"
    "v_mfma_f32_32x32x16_bf16 v[32:47], v[140:143], v[0:3], v[32:47]\n"
    "ds_read_b128 v[128:131], v228 offset:32512\n"
    "ds_read_b64 v[138:139], v229 offset:34048\n"
    "v_exp_f32 v76, v76\n"
    "v_cvt_pk_bf16_f32 v5, v74, v75\n"
    "v_exp_f32 v77, v77\n"
    "v_exp_f32 v78, v78\n"
    "v_cvt_pk_bf16_f32 v6, v76, v77\n"
    "v_exp_f32 v79, v79\n"
    "v_mfma_f32_32x32x16_bf16 v[96:111], v[132:135], v[212:215], v[96:111]\n"
    "buffer_load_dwordx4 v246, s[28:31], s61 offen offset:2048 sc1 lds\n"
    "v_exp_f32 v80, v80\n"
    "v_cvt_pk_bf16_f32 v7, v78, v79\n"
    "v_exp_f32 v81, v81\n"
    "v_exp_f32 v82, v82\n"
    "v_cvt_pk_bf16_f32 v8, v80, v81\n"
    "v_exp_f32 v83, v83\n"
    "v_mfma_f32_32x32x16_bf16 v[32:47], v[144:147], v[4:7], v[32:47]\n"
    "v_exp_f32 v84, v84\n"
    "v_cvt_pk_bf16_f32 v9, v82, v83\n"
    "v_exp_f32 v85, v85\n"
    "v_exp_f32 v86, v86\n"
    "v_cvt_pk_bf16_f32 v10, v84, v85\n"
    "v_exp_f32 v87, v87\n"
    "v_mfma_f32_32x32x16_bf16 v[112:127], v[132:135], v[216:219], v[112:127]\n"
    "buffer_load_dwordx4 a[92:95], v232, s[24:27], s72 offen offset:3072 sc1\n"
    "v_exp_f32 v88, v88\n"
    "v_cvt_pk_bf16_f32 v11, v86, v87\n"
    "v_exp_f32 v89, v89\n"
    "v_exp_f32 v90, v90\n"
    "v_cvt_pk_bf16_f32 v12, v88, v89\n"
    "v_exp_f32 v91, v91\n"
    "v_mfma_f32_32x32x16_bf16 v[48:63], v[140:143], v[8:11], v[48:63]\n"
    "v_exp_f32 v92, v92\n"
    "v_cvt_pk_bf16_f32 v13, v90, v91\n"
    "v_exp_f32 v93, v93\n"
    "v_exp_f32 v94, v94\n"
    "v_cvt_pk_bf16_f32 v14, v92, v93\n"
    "v_exp_f32 v95, v95\n"
    "v_exp_f32 v96, v96\n"
    "v_cvt_pk_bf16_f32 v15, v94, v95\n"
    "v_exp_f32 v97, v97\n"
    "v_exp_f32 v98, v98\n"
    "v_cvt_pk_bf16_f32 v0, v96, v97\n"
    "v_mfma_f32_32x32x16_bf16 v[48:63], v[144:147], v[12:15], v[48:63]\n"
    "ds_read_b64_tr_b16 v[140:141], v230 offset:36608\n"
    "ds_read_b64_tr_b16 v[142:143], v230 offset:36992\n"
    "v_exp_f32 v99, v99\n"
    "v_exp_f32 v100, v100\n"
    "v_cvt_pk_bf16_f32 v1, v98, v99\n"
    "v_exp_f32 v101, v101\n"
    "s_waitcnt lgkmcnt(2)\n"
    "v_mfma_f32_32x32x4_2b_bf16 v[64:95], v[138:139], v[220:221], 0 cbsz:1 abid:0\n"
    "ds_read_b64_tr_b16 v[144:145], v230 offset:37376\n"
    "ds_read_b64_tr_b16 v[146:147], v230 offset:37760\n"
    "v_exp_f32 v102, v102\n"
    "v_cvt_pk_bf16_f32 v2, v100, v101\n"
    "v_exp_f32 v103, v103\n"
    "v_exp_f32 v104, v104\n"
    "v_cvt_pk_bf16_f32 v3, v102, v103\n"
    "v_exp_f32 v105, v105\n"
    "v_exp_f32 v106, v106\n"
    "v_cvt_pk_bf16_f32 v4, v104, v105\n"
    "v_exp_f32 v107, v107\n"
    "v_mfma_f32_32x32x16_bf16 v[32:47], v[184:187], v[0:3], v[32:47]\n"
    "s_add_u32 m0, s60, 0x6300\n"
    "s_add_u32 s61, s61, 0x3000\n"
    "ds_read_b128 v[132:135], v228 offset:33536\n"
    "v_exp_f32 v108, v108\n"
    "v_cvt_pk_bf16_f32 v5, v106, v107\n"
    "v_exp_f32 v109, v109\n"
    "v_exp_f32 v110, v110\n"
    "v_cvt_pk_bf16_f32 v6, v108, v109\n"
    "v_exp_f32 v111, v111\n"
    "v_mfma_f32_32x32x16_bf16 v[64:79], v[128:131], v[212:215], v[64:79]\n"
    "buffer_load_dwordx4 v246, s[28:31], s61 offen offset:0 sc1 lds\n"
    "v_exp_f32 v112, v112\n"
    "v_cvt_pk_bf16_f32 v7, v110, v111\n"
    "v_exp_f32 v113, v113\n"
    "v_exp_f32 v114, v114\n"
    "v_cvt_pk_bf16_f32 v8, v112, v113\n"
    "v_exp_f32 v115, v115\n"
    "v_mfma_f32_32x32x16_bf16 v[32:47], v[188:191], v[4:7], v[32:47]\n"
    "s_add_u32 s72, s72, 0x1000\n"
    "v_exp_f32 v116, v116\n"
    "v_cvt_pk_bf16_f32 v9, v114, v115\n"
    "v_exp_f32 v117, v117\n"
    "v_exp_f32 v118, v118\n"
    "v_cvt_pk_bf16_f32 v10, v116, v117\n"
    "v_exp_f32 v119, v119\n"
    "v_mfma_f32_32x32x16_bf16 v[80:95], v[128:131], v[216:219], v[80:95]\n"
    "buffer_load_dwordx4 a[96:99], v231, s[24:27], s72 offen offset:0 sc1\n"
    "v_exp_f32 v120, v120\n"
    "v_cvt_pk_bf16_f32 v11, v118, v119\n"
    "v_exp_f32 v121, v121\n"
    "v_exp_f32 v122, v122\n"
    "v_cvt_pk_bf16_f32 v12, v120, v121\n"
    "v_exp_f32 v123, v123\n"
    "v_mfma_f32_32x32x16_bf16 v[48:63], v[184:187], v[8:11], v[48:63]\n"
    "buffer_load_dwordx4 a[100:103], v232, s[24:27], s72 offen offset:0 sc1\n"
    "v_exp_f32 v124, v124\n"
    "v_cvt_pk_bf16_f32 v13, v122, v123\n"
    "v_exp_f32 v125, v125\n"
    "v_exp_f32 v126, v126\n"
    "v_cvt_pk_bf16_f32 v14, v124, v125\n"
    "v_exp_f32 v127, v127\n"
    "v_exp_f32 v64, v64\n"
    "v_cvt_pk_bf16_f32 v15, v126, v127\n"
    "v_exp_f32 v65, v65\n"
    "v_exp_f32 v66, v66\n"
    "v_cvt_pk_bf16_f32 v0, v64, v65\n"
    "v_mfma_f32_32x32x16_bf16 v[48:63], v[188:191], v[12:15], v[48:63]\n"
    "ds_read_b64_tr_b16 v[184:185], v230 offset:38144\n"
    "ds_read_b64_tr_b16 v[186:187], v230 offset:38528\n"
    "v_exp_f32 v67, v67\n"
    "v_exp_f32 v68, v68\n"
    "v_cvt_pk_bf16_f32 v1, v66, v67\n"
    "v_exp_f32 v69, v69\n"
    "v_mfma_f32_32x32x4_2b_bf16 v[96:127], v[138:139], v[220:221], 0 cbsz:1 abid:1\n"
    "ds_read_b64_tr_b16 v[188:189], v230 offset:38912\n"
    "ds_read_b64_tr_b16 v[190:191], v230 offset:39296\n"
    "v_exp_f32 v70, v70\n"
    "v_cvt_pk_bf16_f32 v2, v68, v69\n"
    "v_exp_f32 v71, v71\n"
    "v_exp_f32 v72, v72\n"
    "v_cvt_pk_bf16_f32 v3, v70, v71\n"
    "v_exp_f32 v73, v73\n"
    "v_exp_f32 v74, v74\n"
    "v_cvt_pk_bf16_f32 v4, v72, v73\n"
    "v_exp_f32 v75, v75\n"
    "s_waitcnt lgkmcnt(4)\n"
    "v_mfma_f32_32x32x16_bf16 v[32:47], v[140:143], v[0:3], v[32:47]\n"
    "ds_read_b128 v[128:131], v228 offset:43136\n"
    "ds_read_b64 v[136:137], v229 offset:46208\n"
    "v_exp_f32 v76, v76\n"
    "v_cvt_pk_bf16_f32 v5, v74, v75\n"
    "v_exp_f32 v77, v77\n"
    "v_exp_f32 v78, v78\n"
    "v_cvt_pk_bf16_f32 v6, v76, v77\n"
    "v_exp_f32 v79, v79\n"
    "v_mfma_f32_32x32x16_bf16 v[96:111], v[132:135], v[212:215], v[96:111]\n"
    "buffer_load_dwordx4 v246, s[28:31], s61 offen offset:1024 sc1 lds\n"
    "v_exp_f32 v80, v80\n"
    "v_cvt_pk_bf16_f32 v7, v78, v79\n"
    "v_exp_f32 v81, v81\n"
    "v_exp_f32 v82, v82\n"
    "v_cvt_pk_bf16_f32 v8, v80, v81\n"
    "v_exp_f32 v83, v83\n"
    "v_mfma_f32_32x32x16_bf16 v[32:47], v[144:147], v[4:7], v[32:47]\n"
    "v_exp_f32 v84, v84\n"
    "v_cvt_pk_bf16_f32 v9, v82, v83\n"
    "v_exp_f32 v85, v85\n"
    "v_exp_f32 v86, v86\n"
    "v_cvt_pk_bf16_f32 v10, v84, v85\n"
    "v_exp_f32 v87, v87\n"
    "v_mfma_f32_32x32x16_bf16 v[112:127], v[132:135], v[216:219], v[112:127]\n"
    "buffer_load_dwordx4 a[104:107], v231, s[24:27], s72 offen offset:1024 sc1\n"
    "v_exp_f32 v88, v88\n"
    "v_cvt_pk_bf16_f32 v11, v86, v87\n"
    "v_exp_f32 v89, v89\n"
    "v_exp_f32 v90, v90\n"
    "v_cvt_pk_bf16_f32 v12, v88, v89\n"
    "v_exp_f32 v91, v91\n"
    "v_mfma_f32_32x32x16_bf16 v[48:63], v[140:143], v[8:11], v[48:63]\n"
    "v_exp_f32 v92, v92\n"
    "v_cvt_pk_bf16_f32 v13, v90, v91\n"
    "v_exp_f32 v93, v93\n"
    "v_exp_f32 v94, v94\n"
    "v_cvt_pk_bf16_f32 v14, v92, v93\n"
    "v_exp_f32 v95, v95\n"
    "v_exp_f32 v96, v96\n"
    "v_cvt_pk_bf16_f32 v15, v94, v95\n"
    "v_exp_f32 v97, v97\n"
    "v_exp_f32 v98, v98\n"
    "v_cvt_pk_bf16_f32 v0, v96, v97\n"
    "v_mfma_f32_32x32x16_bf16 v[48:63], v[144:147], v[12:15], v[48:63]\n"
    "ds_read_b64_tr_b16 v[140:141], v230 offset:46208\n"
    "ds_read_b64_tr_b16 v[142:143], v230 offset:46592\n"
    "v_exp_f32 v99, v99\n"
    "v_exp_f32 v100, v100\n"
    "v_cvt_pk_bf16_f32 v1, v98, v99\n"
    "v_exp_f32 v101, v101\n"
    "s_waitcnt lgkmcnt(2)\n"
    "v_mfma_f32_32x32x4_2b_bf16 v[64:95], v[136:137], v[220:221], 0 cbsz:1 abid:0\n"
    "ds_read_b64_tr_b16 v[144:145], v230 offset:46976\n"
    "ds_read_b64_tr_b16 v[146:147], v230 offset:47360\n"
    "v_exp_f32 v102, v102\n"
    "v_cvt_pk_bf16_f32 v2, v100, v101\n"
    "v_exp_f32 v103, v103\n"
    "v_exp_f32 v104, v104\n"
    "v_cvt_pk_bf16_f32 v3, v102, v103\n"
    "v_exp_f32 v105, v105\n"
    "v_exp_f32 v106, v106\n"
    "v_cvt_pk_bf16_f32 v4, v104, v105\n"
    "v_exp_f32 v107, v107\n"
    "v_mfma_f32_32x32x16_bf16 v[32:47], v[184:187], v[0:3], v[32:47]\n"
    "ds_read_b128 v[132:135], v228 offset:44160\n"
    "v_exp_f32 v108, v108\n"
    "v_cvt_pk_bf16_f32 v5, v106, v107\n"
    "v_exp_f32 v109, v109\n"
    "v_exp_f32 v110, v110\n"
    "v_cvt_pk_bf16_f32 v6, v108, v109\n"
    "v_exp_f32 v111, v111\n"
    "v_mfma_f32_32x32x16_bf16 v[64:79], v[128:131], v[212:215], v[64:79]\n"
    "buffer_load_dwordx4 v246, s[28:31], s61 offen offset:2048 sc1 lds\n"
    "v_exp_f32 v112, v112\n"
    "v_cvt_pk_bf16_f32 v7, v110, v111\n"
    "v_exp_f32 v113, v113\n"
    "v_exp_f32 v114, v114\n"
    "v_cvt_pk_bf16_f32 v8, v112, v113\n"
    "v_exp_f32 v115, v115\n"
    "v_mfma_f32_32x32x16_bf16 v[32:47], v[188:191], v[4:7], v[32:47]\n"
    "v_exp_f32 v116, v116\n"
    "v_cvt_pk_bf16_f32 v9, v114, v115\n"
    "v_exp_f32 v117, v117\n"
    "v_exp_f32 v118, v118\n"
    "v_cvt_pk_bf16_f32 v10, v116, v117\n"
    "v_exp_f32 v119, v119\n"
    "v_mfma_f32_32x32x16_bf16 v[80:95], v[128:131], v[216:219], v[80:95]\n"
    "buffer_load_dwordx4 a[108:111], v232, s[24:27], s72 offen offset:1024 sc1\n"
    "v_exp_f32 v120, v120\n"
    "v_cvt_pk_bf16_f32 v11, v118, v119\n"
    "v_exp_f32 v121, v121\n"
    "v_exp_f32 v122, v122\n"
    "v_cvt_pk_bf16_f32 v12, v120, v121\n"
    "v_exp_f32 v123, v123\n"
    "v_mfma_f32_32x32x16_bf16 v[48:63], v[184:187], v[8:11], v[48:63]\n"
    "buffer_load_dwordx4 a[112:115], v231, s[24:27], s72 offen offset:2048 sc1\n"
    "v_exp_f32 v124, v124\n"
    "v_cvt_pk_bf16_f32 v13, v122, v123\n"
    "v_exp_f32 v125, v125\n"
    "v_exp_f32 v126, v126\n"
    "v_cvt_pk_bf16_f32 v14, v124, v125\n"
    "v_exp_f32 v127, v127\n"
    "v_exp_f32 v64, v64\n"
    "v_cvt_pk_bf16_f32 v15, v126, v127\n"
    "v_exp_f32 v65, v65\n"
    "v_exp_f32 v66, v66\n"
    "v_cvt_pk_bf16_f32 v0, v64, v65\n"
    "v_mfma_f32_32x32x16_bf16 v[48:63], v[188:191], v[12:15], v[48:63]\n"
    "ds_read_b64_tr_b16 v[184:185], v230 offset:47744\n"
    "ds_read_b64_tr_b16 v[186:187], v230 offset:48128\n"
    "v_exp_f32 v67, v67\n"
    "v_exp_f32 v68, v68\n"
    "v_cvt_pk_bf16_f32 v1, v66, v67\n"
    "v_exp_f32 v69, v69\n"
    "v_mfma_f32_32x32x4_2b_bf16 v[96:127], v[136:137], v[220:221], 0 cbsz:1 abid:1\n"
    "ds_read_b64_tr_b16 v[188:189], v230 offset:48512\n"
    "ds_read_b64_tr_b16 v[190:191], v230 offset:48896\n"
    "v_exp_f32 v70, v70\n"
    "v_cvt_pk_bf16_f32 v2, v68, v69\n"
    "v_exp_f32 v71, v71\n"
    "v_exp_f32 v72, v72\n"
    "v_cvt_pk_bf16_f32 v3, v70, v71\n"
    "v_exp_f32 v73, v73\n"
    "v_exp_f32 v74, v74\n"
    "v_cvt_pk_bf16_f32 v4, v72, v73\n"
    "v_exp_f32 v75, v75\n"
    "s_waitcnt lgkmcnt(4)\n"
    "v_mfma_f32_32x32x16_bf16 v[32:47], v[140:143], v[0:3], v[32:47]\n"
    "s_add_u32 m0, s60, 0x9480\n"
    "s_add_u32 s61, s61, 0x3000\n"
    "ds_read_b128 v[128:131], v228 offset:45184\n"
    "ds_read_b64 v[138:139], v229 offset:46720\n"
    "v_exp_f32 v76, v76\n"
    "v_cvt_pk_bf16_f32 v5, v74, v75\n"
    "v_exp_f32 v77, v77\n"
    "v_exp_f32 v78, v78\n"
    "v_cvt_pk_bf16_f32 v6, v76, v77\n"
    "v_exp_f32 v79, v79\n"
    "v_mfma_f32_32x32x16_bf16 v[96:111], v[132:135], v[212:215], v[96:111]\n"
    "buffer_load_dwordx4 v246, s[28:31], s61 offen offset:0 sc1 lds\n"
    "v_exp_f32 v80, v80\n"
    "v_cvt_pk_bf16_f32 v7, v78, v79\n"
    "v_exp_f32 v81, v81\n"
    "v_exp_f32 v82, v82\n"
    "v_cvt_pk_bf16_f32 v8, v80, v81\n"
    "v_exp_f32 v83, v83\n"
    "v_mfma_f32_32x32x16_bf16 v[32:47], v[144:147], v[4:7], v[32:47]\n"
    "v_exp_f32 v84, v84\n"
    "v_cvt_pk_bf16_f32 v9, v82, v83\n"
    "v_exp_f32 v85, v85\n"
    "v_exp_f32 v86, v86\n"
    "v_cvt_pk_bf16_f32 v10, v84, v85\n"
    "v_exp_f32 v87, v87\n"
    "v_mfma_f32_32x32x16_bf16 v[112:127], v[132:135], v[216:219], v[112:127]\n"
    "buffer_load_dwordx4 a[116:119], v232, s[24:27], s72 offen offset:2048 sc1\n"
    "v_exp_f32 v88, v88\n"
    "v_cvt_pk_bf16_f32 v11, v86, v87\n"
    "v_exp_f32 v89, v89\n"
    "v_exp_f32 v90, v90\n"
    "v_cvt_pk_bf16_f32 v12, v88, v89\n"
    "v_exp_f32 v91, v91\n"
    "v_mfma_f32_32x32x16_bf16 v[48:63], v[140:143], v[8:11], v[48:63]\n"
    "v_exp_f32 v92, v92\n"
    "v_cvt_pk_bf16_f32 v13, v90, v91\n"
    "v_exp_f32 v93, v93\n"
    "v_exp_f32 v94, v94\n"
    "v_cvt_pk_bf16_f32 v14, v92, v93\n"
    "v_exp_f32 v95, v95\n"
    "v_exp_f32 v96, v96\n"
    "v_cvt_pk_bf16_f32 v15, v94, v95\n"
    "v_exp_f32 v97, v97\n"
    "v_exp_f32 v98, v98\n"
    "v_cvt_pk_bf16_f32 v0, v96, v97\n"
    "v_mfma_f32_32x32x16_bf16 v[48:63], v[144:147], v[12:15], v[48:63]\n"
    "ds_read_b64_tr_b16 v[140:141], v230 offset:49280\n"
    "ds_read_b64_tr_b16 v[142:143], v230 offset:49664\n"
    "v_exp_f32 v99, v99\n"
    "v_exp_f32 v100, v100\n"
    "v_cvt_pk_bf16_f32 v1, v98, v99\n"
    "v_exp_f32 v101, v101\n"
    "s_waitcnt lgkmcnt(2)\n"
    "v_mfma_f32_32x32x4_2b_bf16 v[64:95], v[138:139], v[220:221], 0 cbsz:1 abid:0\n"
    "ds_read_b64_tr_b16 v[144:145], v230 offset:50048\n"
    "ds_read_b64_tr_b16 v[146:147], v230 offset:50432\n"
    "v_exp_f32 v102, v102\n"
    "v_cvt_pk_bf16_f32 v2, v100, v101\n"
    "v_exp_f32 v103, v103\n"
    "v_exp_f32 v104, v104\n"
    "v_cvt_pk_bf16_f32 v3, v102, v103\n"
    "v_exp_f32 v105, v105\n"
    "v_exp_f32 v106, v106\n"
    "v_cvt_pk_bf16_f32 v4, v104, v105\n"
    "v_exp_f32 v107, v107\n"
    "v_mfma_f32_32x32x16_bf16 v[32:47], v[184:187], v[0:3], v[32:47]\n"
    "ds_read_b128 v[132:135], v228 offset:46208\n"
    "v_exp_f32 v108, v108\n"
    "v_cvt_pk_bf16_f32 v5, v106, v107\n"
    "v_exp_f32 v109, v109\n"
    "v_exp_f32 v110, v110\n"
    "v_cvt_pk_bf16_f32 v6, v108, v109\n"
    "v_exp_f32 v111, v111\n"
    "v_mfma_f32_32x32x16_bf16 v[64:79], v[128:131], v[212:215], v[64:79]\n"
    "buffer_load_dwordx4 v246, s[28:31], s61 offen offset:1024 sc1 lds\n"
    "v_exp_f32 v112, v112\n"
    "v_cvt_pk_bf16_f32 v7, v110, v111\n"
    "v_exp_f32 v113, v113\n"
    "v_exp_f32 v114, v114\n"
    "v_cvt_pk_bf16_f32 v8, v112, v113\n"
    "v_exp_f32 v115, v115\n"
    "v_mfma_f32_32x32x16_bf16 v[32:47], v[188:191], v[4:7], v[32:47]\n"
    "v_exp_f32 v116, v116\n"
    "v_cvt_pk_bf16_f32 v9, v114, v115\n"
    "v_exp_f32 v117, v117\n"
    "v_exp_f32 v118, v118\n"
    "v_cvt_pk_bf16_f32 v10, v116, v117\n"
    "v_exp_f32 v119, v119\n"
    "v_mfma_f32_32x32x16_bf16 v[80:95], v[128:131], v[216:219], v[80:95]\n"
    "buffer_load_dwordx4 a[120:123], v231, s[24:27], s72 offen offset:3072 sc1\n"
    "v_exp_f32 v120, v120\n"
    "v_cvt_pk_bf16_f32 v11, v118, v119\n"
    "v_exp_f32 v121, v121\n"
    "v_exp_f32 v122, v122\n"
    "v_cvt_pk_bf16_f32 v12, v120, v121\n"
    "v_exp_f32 v123, v123\n"
    "v_mfma_f32_32x32x16_bf16 v[48:63], v[184:187], v[8:11], v[48:63]\n"
    "buffer_load_dwordx4 a[124:127], v232, s[24:27], s72 offen offset:3072 sc1\n"
    "v_exp_f32 v124, v124\n"
    "v_cvt_pk_bf16_f32 v13, v122, v123\n"
    "v_exp_f32 v125, v125\n"
    "v_exp_f32 v126, v126\n"
    "v_cvt_pk_bf16_f32 v14, v124, v125\n"
    "v_exp_f32 v127, v127\n"
    "v_exp_f32 v64, v64\n"
    "v_cvt_pk_bf16_f32 v15, v126, v127\n"
    "v_exp_f32 v65, v65\n"
    "v_exp_f32 v66, v66\n"
    "v_cvt_pk_bf16_f32 v0, v64, v65\n"
    "v_mfma_f32_32x32x16_bf16 v[48:63], v[188:191], v[12:15], v[48:63]\n"
    "ds_read_b64_tr_b16 v[184:185], v230 offset:50816\n"
    "ds_read_b64_tr_b16 v[186:187], v230 offset:51200\n"
    "v_exp_f32 v67, v67\n"
    "v_exp_f32 v68, v68\n"
    "v_cvt_pk_bf16_f32 v1, v66, v67\n"
    "v_exp_f32 v69, v69\n"
    "v_mfma_f32_32x32x4_2b_bf16 v[96:127], v[138:139], v[220:221], 0 cbsz:1 abid:1\n"
    "ds_read_b64_tr_b16 v[188:189], v230 offset:51584\n"
    "ds_read_b64_tr_b16 v[190:191], v230 offset:51968\n"
    "v_exp_f32 v70, v70\n"
    "v_cvt_pk_bf16_f32 v2, v68, v69\n"
    "v_exp_f32 v71, v71\n"
    "v_exp_f32 v72, v72\n"
    "v_cvt_pk_bf16_f32 v3, v70, v71\n"
    "v_exp_f32 v73, v73\n"
    "v_exp_f32 v74, v74\n"
    "v_cvt_pk_bf16_f32 v4, v72, v73\n"
    "v_exp_f32 v75, v75\n"
    "s_waitcnt lgkmcnt(4)\n"
    "v_mfma_f32_32x32x16_bf16 v[32:47], v[140:143], v[0:3], v[32:47]\n"
    "ds_read_b128 v[128:131], v228 offset:55808\n"
    "ds_read_b64 v[136:137], v229 offset:58880\n"
    "v_exp_f32 v76, v76\n"
    "v_cvt_pk_bf16_f32 v5, v74, v75\n"
    "v_exp_f32 v77, v77\n"
    "v_exp_f32 v78, v78\n"
    "v_cvt_pk_bf16_f32 v6, v76, v77\n"
    "v_exp_f32 v79, v79\n"
    "v_mfma_f32_32x32x16_bf16 v[96:111], v[132:135], v[212:215], v[96:111]\n"
    "buffer_load_dwordx4 v246, s[28:31], s61 offen offset:2048 sc1 lds\n"
    "v_exp_f32 v80, v80\n"
    "v_cvt_pk_bf16_f32 v7, v78, v79\n"
    "v_exp_f32 v81, v81\n"
    "v_exp_f32 v82, v82\n"
    "v_cvt_pk_bf16_f32 v8, v80, v81\n"
    "v_exp_f32 v83, v83\n"
    "v_mfma_f32_32x32x16_bf16 v[32:47], v[144:147], v[4:7], v[32:47]\n"
    "s_add_u32 s72, s72, 0x1000\n"
    "v_exp_f32 v84, v84\n"
    "v_cvt_pk_bf16_f32 v9, v82, v83\n"
    "v_exp_f32 v85, v85\n"
    "v_exp_f32 v86, v86\n"
    "v_cvt_pk_bf16_f32 v10, v84, v85\n"
    "v_exp_f32 v87, v87\n"
    "v_mfma_f32_32x32x16_bf16 v[112:127], v[132:135], v[216:219], v[112:127]\n"
    "buffer_load_dwordx4 a[128:131], v231, s[24:27], s72 offen offset:0 sc1\n"
    "v_exp_f32 v88, v88\n"
    "v_cvt_pk_bf16_f32 v11, v86, v87\n"
    "v_exp_f32 v89, v89\n"
    "v_exp_f32 v90, v90\n"
    "v_cvt_pk_bf16_f32 v12, v88, v89\n"
    "v_exp_f32 v91, v91\n"
    "v_mfma_f32_32x32x16_bf16 v[48:63], v[140:143], v[8:11], v[48:63]\n"
    "v_exp_f32 v92, v92\n"
    "v_cvt_pk_bf16_f32 v13, v90, v91\n"
    "v_exp_f32 v93, v93\n"
    "v_exp_f32 v94, v94\n"
    "v_cvt_pk_bf16_f32 v14, v92, v93\n"
    "v_exp_f32 v95, v95\n"
    "v_exp_f32 v96, v96\n"
    "v_cvt_pk_bf16_f32 v15, v94, v95\n"
    "v_exp_f32 v97, v97\n"
    "v_exp_f32 v98, v98\n"
    "v_cvt_pk_bf16_f32 v0, v96, v97\n"
    "v_mfma_f32_32x32x16_bf16 v[48:63], v[144:147], v[12:15], v[48:63]\n"
    "ds_read_b64_tr_b16 v[140:141], v230 offset:58880\n"
    "ds_read_b64_tr_b16 v[142:143], v230 offset:59264\n"
    "v_exp_f32 v99, v99\n"
    "v_exp_f32 v100, v100\n"
    "v_cvt_pk_bf16_f32 v1, v98, v99\n"
    "v_exp_f32 v101, v101\n"
    "s_waitcnt lgkmcnt(2)\n"
    "v_mfma_f32_32x32x4_2b_bf16 v[64:95], v[136:137], v[220:221], 0 cbsz:1 abid:0\n"
    "ds_read_b64_tr_b16 v[144:145], v230 offset:59648\n"
    "ds_read_b64_tr_b16 v[146:147], v230 offset:60032\n"
    "v_exp_f32 v102, v102\n"
    "v_cvt_pk_bf16_f32 v2, v100, v101\n"
    "v_exp_f32 v103, v103\n"
    "v_exp_f32 v104, v104\n"
    "v_cvt_pk_bf16_f32 v3, v102, v103\n"
    "v_exp_f32 v105, v105\n"
    "v_exp_f32 v106, v106\n"
    "v_cvt_pk_bf16_f32 v4, v104, v105\n"
    "v_exp_f32 v107, v107\n"
    "v_mfma_f32_32x32x16_bf16 v[32:47], v[184:187], v[0:3], v[32:47]\n"
    "s_mul_i32 s2, s65, 10368\n"
    "ds_read_b128 v[132:135], v228 offset:56832\n"
    "v_exp_f32 v108, v108\n"
    "v_cvt_pk_bf16_f32 v5, v106, v107\n"
    "v_exp_f32 v109, v109\n"
    "v_exp_f32 v110, v110\n"
    "v_cvt_pk_bf16_f32 v6, v108, v109\n"
    "v_exp_f32 v111, v111\n"
    "v_mfma_f32_32x32x16_bf16 v[64:79], v[128:131], v[212:215], v[64:79]\n"
    "buffer_load_dword v196, v207, s[32:35], s2 offen\n"
    "v_exp_f32 v112, v112\n"
    "v_cvt_pk_bf16_f32 v7, v110, v111\n"
    "v_exp_f32 v113, v113\n"
    "v_exp_f32 v114, v114\n"
    "v_cvt_pk_bf16_f32 v8, v112, v113\n"
    "v_exp_f32 v115, v115\n"
    "v_mfma_f32_32x32x16_bf16 v[32:47], v[188:191], v[4:7], v[32:47]\n"
    "v_exp_f32 v116, v116\n"
    "v_cvt_pk_bf16_f32 v9, v114, v115\n"
    "v_exp_f32 v117, v117\n"
    "v_exp_f32 v118, v118\n"
    "v_cvt_pk_bf16_f32 v10, v116, v117\n"
    "v_exp_f32 v119, v119\n"
    "v_mfma_f32_32x32x16_bf16 v[80:95], v[128:131], v[216:219], v[80:95]\n"
    "buffer_load_dwordx4 a[132:135], v232, s[24:27], s72 offen offset:0 sc1\n"
    "v_exp_f32 v120, v120\n"
    "v_cvt_pk_bf16_f32 v11, v118, v119\n"
    "v_exp_f32 v121, v121\n"
    "v_exp_f32 v122, v122\n"
    "v_cvt_pk_bf16_f32 v12, v120, v121\n"
    "v_exp_f32 v123, v123\n"
    "v_mfma_f32_32x32x16_bf16 v[48:63], v[184:187], v[8:11], v[48:63]\n"
    "buffer_load_dwordx4 a[136:139], v231, s[24:27], s72 offen offset:1024 sc1\n"
    "v_exp_f32 v124, v124\n"
    "v_cvt_pk_bf16_f32 v13, v122, v123\n"
    "v_exp_f32 v125, v125\n"
    "v_exp_f32 v126, v126\n"
    "v_cvt_pk_bf16_f32 v14, v124, v125\n"
    "v_exp_f32 v127, v127\n"
    "v_exp_f32 v64, v64\n"
    "v_cvt_pk_bf16_f32 v15, v126, v127\n"
    "v_exp_f32 v65, v65\n"
    "v_exp_f32 v66, v66\n"
    "v_cvt_pk_bf16_f32 v0, v64, v65\n"
    "v_mfma_f32_32x32x16_bf16 v[48:63], v[188:191], v[12:15], v[48:63]\n"
    "ds_read_b64_tr_b16 v[184:185], v230 offset:60416\n"
    "ds_read_b64_tr_b16 v[186:187], v230 offset:60800\n"
    "v_exp_f32 v67, v67\n"
    "v_exp_f32 v68, v68\n"
    "v_cvt_pk_bf16_f32 v1, v66, v67\n"
    "v_exp_f32 v69, v69\n"
    "v_mfma_f32_32x32x4_2b_bf16 v[96:127], v[136:137], v[220:221], 0 cbsz:1 abid:1\n"
    "ds_read_b64_tr_b16 v[188:189], v230 offset:61184\n"
    "ds_read_b64_tr_b16 v[190:191], v230 offset:61568\n"
    "v_exp_f32 v70, v70\n"
    "v_cvt_pk_bf16_f32 v2, v68, v69\n"
    "v_exp_f32 v71, v71\n"
    "v_exp_f32 v72, v72\n"
    "v_cvt_pk_bf16_f32 v3, v70, v71\n"
    "v_exp_f32 v73, v73\n"
    "v_exp_f32 v74, v74\n"
    "v_cvt_pk_bf16_f32 v4, v72, v73\n"
    "v_exp_f32 v75, v75\n"
    "s_waitcnt lgkmcnt(4)\n"
    "v_mfma_f32_32x32x16_bf16 v[32:47], v[140:143], v[0:3], v[32:47]\n"
    "s_mul_i32 s2, s65, 10368\n"
    "s_add_u32 s2, s2, 0x2000\n"
    "ds_read_b128 v[128:131], v228 offset:57856\n"
    "ds_read_b64 v[138:139], v229 offset:59392\n"
    "v_exp_f32 v76, v76\n"
    "v_cvt_pk_bf16_f32 v5, v74, v75\n"
    "v_exp_f32 v77, v77\n"
    "v_exp_f32 v78, v78\n"
    "v_cvt_pk_bf16_f32 v6, v76, v77\n"
    "v_exp_f32 v79, v79\n"
    "v_mfma_f32_32x32x16_bf16 v[96:111], v[132:135], v[212:215], v[96:111]\n"
    "s_mov_b64 exec, 0x1ffff\n"
    "buffer_load_dword v197, v207, s[32:35], s2 offen\n"
    "s_mov_b64 exec, -1\n"
    "v_exp_f32 v80, v80\n"
    "v_cvt_pk_bf16_f32 v7, v78, v79\n"
    "v_exp_f32 v81, v81\n"
    "v_exp_f32 v82, v82\n"
    "v_cvt_pk_bf16_f32 v8, v80, v81\n"
    "v_exp_f32 v83, v83\n"
    "v_mfma_f32_32x32x16_bf16 v[32:47], v[144:147], v[4:7], v[32:47]\n"
    "v_exp_f32 v84, v84\n"
    "v_cvt_pk_bf16_f32 v9, v82, v83\n"
    "v_exp_f32 v85, v85\n"
    "v_exp_f32 v86, v86\n"
    "v_cvt_pk_bf16_f32 v10, v84, v85\n"
    "v_exp_f32 v87, v87\n"
    "v_mfma_f32_32x32x16_bf16 v[112:127], v[132:135], v[216:219], v[112:127]\n"
    "buffer_load_dwordx4 a[140:143], v232, s[24:27], s72 offen offset:1024 sc1\n"
    "v_exp_f32 v88, v88\n"
    "v_cvt_pk_bf16_f32 v11, v86, v87\n"
    "v_exp_f32 v89, v89\n"
    "v_exp_f32 v90, v90\n"
    "v_cvt_pk_bf16_f32 v12, v88, v89\n"
    "v_exp_f32 v91, v91\n"
    "v_mfma_f32_32x32x16_bf16 v[48:63], v[140:143], v[8:11], v[48:63]\n"
    "v_exp_f32 v92, v92\n"
    "v_cvt_pk_bf16_f32 v13, v90, v91\n"
    "v_exp_f32 v93, v93\n"
    "v_exp_f32 v94, v94\n"
    "v_cvt_pk_bf16_f32 v14, v92, v93\n"
    "v_exp_f32 v95, v95\n"
    "v_exp_f32 v96, v96\n"
    "v_cvt_pk_bf16_f32 v15, v94, v95\n"
    "v_exp_f32 v97, v97\n"
    "v_exp_f32 v98, v98\n"
    "v_cvt_pk_bf16_f32 v0, v96, v97\n"
    "v_mfma_f32_32x32x16_bf16 v[48:63], v[144:147], v[12:15], v[48:63]\n"
    "ds_read_b64_tr_b16 v[140:141], v230 offset:61952\n"
    "ds_read_b64_tr_b16 v[142:143], v230 offset:62336\n"
    "v_exp_f32 v99, v99\n"
    "v_exp_f32 v100, v100\n"
    "v_cvt_pk_bf16_f32 v1, v98, v99\n"
    "v_exp_f32 v101, v101\n"
    "s_waitcnt lgkmcnt(2)\n"
    "v_mfma_f32_32x32x4_2b_bf16 v[64:95], v[138:139], v[220:221], 0 cbsz:1 abid:0\n"
    "ds_read_b64_tr_b16 v[144:145], v230 offset:62720\n"
    "ds_read_b64_tr_b16 v[146:147], v230 offset:63104\n"
    "v_exp_f32 v102, v102\n"
    "v_cvt_pk_bf16_f32 v2, v100, v101\n"
    "v_exp_f32 v103, v103\n"
    "v_exp_f32 v104, v104\n"
    "v_cvt_pk_bf16_f32 v3, v102, v103\n"
    "v_exp_f32 v105, v105\n"
    "v_exp_f32 v106, v106\n"
    "v_cvt_pk_bf16_f32 v4, v104, v105\n"
    "v_exp_f32 v107, v107\n"
    "v_mfma_f32_32x32x16_bf16 v[32:47], v[184:187], v[0:3], v[32:47]\n"
    "s_mul_i32 s2, s65, 1408\n"
    "ds_read_b128 v[132:135], v228 offset:58880\n"
    "v_exp_f32 v108, v108\n"
    "v_cvt_pk_bf16_f32 v5, v106, v107\n"
    "v_exp_f32 v109, v109\n"
    "v_exp_f32 v110, v110\n"
    "v_cvt_pk_bf16_f32 v6, v108, v109\n"
    "v_exp_f32 v111, v111\n"
    "v_mfma_f32_32x32x16_bf16 v[64:79], v[128:131], v[212:215], v[64:79]\n"
    "s_mov_b64 exec, 0x7ff\n"
    "buffer_load_dword v199, v207, s[36:39], s2 offen\n"
    "s_mov_b64 exec, -1\n"
    "v_exp_f32 v112, v112\n"
    "v_cvt_pk_bf16_f32 v7, v110, v111\n"
    "v_exp_f32 v113, v113\n"
    "v_exp_f32 v114, v114\n"
    "v_cvt_pk_bf16_f32 v8, v112, v113\n"
    "v_exp_f32 v115, v115\n"
    "v_mfma_f32_32x32x16_bf16 v[32:47], v[188:191], v[4:7], v[32:47]\n"
    "v_exp_f32 v116, v116\n"
    "v_cvt_pk_bf16_f32 v9, v114, v115\n"
    "v_exp_f32 v117, v117\n"
    "v_exp_f32 v118, v118\n"
    "v_cvt_pk_bf16_f32 v10, v116, v117\n"
    "v_exp_f32 v119, v119\n"
    "v_mfma_f32_32x32x16_bf16 v[80:95], v[128:131], v[216:219], v[80:95]\n"
    "buffer_load_dwordx4 a[144:147], v231, s[24:27], s72 offen offset:2048 sc1\n"
    "v_exp_f32 v120, v120\n"
    "v_cvt_pk_bf16_f32 v11, v118, v119\n"
    "v_exp_f32 v121, v121\n"
    "v_exp_f32 v122, v122\n"
    "v_cvt_pk_bf16_f32 v12, v120, v121\n"
    "v_exp_f32 v123, v123\n"
    "v_mfma_f32_32x32x16_bf16 v[48:63], v[184:187], v[8:11], v[48:63]\n"
    "buffer_load_dwordx4 a[148:151], v232, s[24:27], s72 offen offset:2048 sc1\n"
    "v_exp_f32 v124, v124\n"
    "v_cvt_pk_bf16_f32 v13, v122, v123\n"
    "v_exp_f32 v125, v125\n"
    "v_exp_f32 v126, v126\n"
    "v_cvt_pk_bf16_f32 v14, v124, v125\n"
    "v_exp_f32 v127, v127\n"
    "s_waitcnt vmcnt(32)\n"
    "s_barrier\n"
    "v_exp_f32 v64, v64\n"
    "v_cvt_pk_bf16_f32 v15, v126, v127\n"
    "v_exp_f32 v65, v65\n"
    "v_exp_f32 v66, v66\n"
    "v_cvt_pk_bf16_f32 v0, v64, v65\n"
    "v_mfma_f32_32x32x16_bf16 v[48:63], v[188:191], v[12:15], v[48:63]\n"
    "ds_read_b64_tr_b16 v[184:185], v230 offset:63488\n"
    "ds_read_b64_tr_b16 v[186:187], v230 offset:63872\n"
    "v_exp_f32 v67, v67\n"
    "v_exp_f32 v68, v68\n"
    "v_cvt_pk_bf16_f32 v1, v66, v67\n"
    "v_exp_f32 v69, v69\n"
    "v_mfma_f32_32x32x4_2b_bf16 v[96:127], v[138:139], v[220:221], 0 cbsz:1 abid:1\n"
    "ds_read_b64_tr_b16 v[188:189], v230 offset:64256\n"
    "ds_read_b64_tr_b16 v[190:191], v230 offset:64640\n"
    "v_exp_f32 v70, v70\n"
    "v_cvt_pk_bf16_f32 v2, v68, v69\n"
    "v_exp_f32 v71, v71\n"
    "v_exp_f32 v72, v72\n"
    "v_cvt_pk_bf16_f32 v3, v70, v71\n"
    "v_exp_f32 v73, v73\n"
    "v_exp_f32 v74, v74\n"
    "v_cvt_pk_bf16_f32 v4, v72, v73\n"
    "v_exp_f32 v75, v75\n"
    "s_waitcnt lgkmcnt(4)\n"
    "v_mfma_f32_32x32x16_bf16 v[32:47], v[140:143], v[0:3], v[32:47]\n"
    "s_add_u32 m0, s62, 0x12900\n"
    "v_add_u32 v228, 0x9c80, v228\n"
    "ds_read_b128 v[128:131], v228 offset:28416\n"
    "v_add_u32 v229, 0x8080, v229\n"
    "ds_read_b64 v[136:137], v229 offset:38656\n"
    "v_exp_f32 v76, v76\n"
    "v_cvt_pk_bf16_f32 v5, v74, v75\n"
    "v_exp_f32 v77, v77\n"
    "v_exp_f32 v78, v78\n"
    "v_cvt_pk_bf16_f32 v6, v76, v77\n"
    "v_exp_f32 v79, v79\n"
    "v_mfma_f32_32x32x16_bf16 v[96:111], v[132:135], v[212:215], v[96:111]\n"
    "buffer_load_dwordx4 v236, s[32:35], s63 offen offset:0 lds\n"
    "v_exp_f32 v80, v80\n"
    "v_cvt_pk_bf16_f32 v7, v78, v79\n"
    "v_exp_f32 v81, v81\n"
    "v_exp_f32 v82, v82\n"
    "v_cvt_pk_bf16_f32 v8, v80, v81\n"
    "v_exp_f32 v83, v83\n"
    "v_mfma_f32_32x32x16_bf16 v[32:47], v[144:147], v[4:7], v[32:47]\n"
    "v_exp_f32 v84, v84\n"
    "v_cvt_pk_bf16_f32 v9, v82, v83\n"
    "v_exp_f32 v85, v85\n"
    "v_exp_f32 v86, v86\n"
    "v_cvt_pk_bf16_f32 v10, v84, v85\n"
    "v_exp_f32 v87, v87\n"
    "v_mfma_f32_32x32x16_bf16 v[112:127], v[132:135], v[216:219], v[112:127]\n"
    "buffer_load_dwordx4 a[152:155], v231, s[24:27], s72 offen offset:3072 sc1\n"
    "v_exp_f32 v88, v88\n"
    "v_cvt_pk_bf16_f32 v11, v86, v87\n"
    "v_exp_f32 v89, v89\n"
    "v_exp_f32 v90, v90\n"
    "v_cvt_pk_bf16_f32 v12, v88, v89\n"
    "v_exp_f32 v91, v91\n"
    "v_mfma_f32_32x32x16_bf16 v[48:63], v[140:143], v[8:11], v[48:63]\n"
    "v_exp_f32 v92, v92\n"
    "v_cvt_pk_bf16_f32 v13, v90, v91\n"
    "v_exp_f32 v93, v93\n"
    "v_exp_f32 v94, v94\n"
    "v_cvt_pk_bf16_f32 v14, v92, v93\n"
    "v_exp_f32 v95, v95\n"
    "v_exp_f32 v96, v96\n"
    "v_cvt_pk_bf16_f32 v15, v94, v95\n"
    "v_exp_f32 v97, v97\n"
    "v_exp_f32 v98, v98\n"
    "v_cvt_pk_bf16_f32 v0, v96, v97\n"
    "v_mfma_f32_32x32x16_bf16 v[48:63], v[144:147], v[12:15], v[48:63]\n"
    "v_add_u32 v230, 0x9880, v230\n"
    "ds_read_b64_tr_b16 v[140:141], v230 offset:32512\n"
    "ds_read_b64_tr_b16 v[142:143], v230 offset:32896\n"
    "v_exp_f32 v99, v99\n"
    "v_exp_f32 v100, v100\n"
    "v_cvt_pk_bf16_f32 v1, v98, v99\n"
    "v_exp_f32 v101, v101\n"
    "s_waitcnt lgkmcnt(2)\n"
    "v_mfma_f32_32x32x4_2b_bf16 v[64:95], v[136:137], v[220:221], 0 cbsz:1 abid:0\n"
    "ds_read_b64_tr_b16 v[144:145], v230 offset:33280\n"
    "ds_read_b64_tr_b16 v[146:147], v230 offset:33664\n"
    "v_exp_f32 v102, v102\n"
    "v_cvt_pk_bf16_f32 v2, v100, v101\n"
    "v_exp_f32 v103, v103\n"
    "v_exp_f32 v104, v104\n"
    "v_cvt_pk_bf16_f32 v3, v102, v103\n"
    "v_exp_f32 v105, v105\n"
    "v_exp_f32 v106, v106\n"
    "v_cvt_pk_bf16_f32 v4, v104, v105\n"
    "v_exp_f32 v107, v107\n"
    "v_mfma_f32_32x32x16_bf16 v[32:47], v[184:187], v[0:3], v[32:47]\n"
    "ds_read_b128 v[132:135], v228 offset:29440\n"
    "v_exp_f32 v108, v108\n"
    "v_cvt_pk_bf16_f32 v5, v106, v107\n"
    "v_exp_f32 v109, v109\n"
    "v_exp_f32 v110, v110\n"
    "v_cvt_pk_bf16_f32 v6, v108, v109\n"
    "v_exp_f32 v111, v111\n"
    "v_mfma_f32_32x32x16_bf16 v[64:79], v[128:131], v[212:215], v[64:79]\n"
    "buffer_load_dwordx4 v237, s[32:35], s63 offen offset:1024 lds\n"
    "v_exp_f32 v112, v112\n"
    "v_cvt_pk_bf16_f32 v7, v110, v111\n"
    "v_exp_f32 v113, v113\n"
    "v_exp_f32 v114, v114\n"
    "v_cvt_pk_bf16_f32 v8, v112, v113\n"
    "v_exp_f32 v115, v115\n"
    "v_mfma_f32_32x32x16_bf16 v[32:47], v[188:191], v[4:7], v[32:47]\n"
    "v_exp_f32 v116, v116\n"
    "v_cvt_pk_bf16_f32 v9, v114, v115\n"
    "v_exp_f32 v117, v117\n"
    "v_exp_f32 v118, v118\n"
    "v_cvt_pk_bf16_f32 v10, v116, v117\n"
    "v_exp_f32 v119, v119\n"
    "v_mfma_f32_32x32x16_bf16 v[80:95], v[128:131], v[216:219], v[80:95]\n"
    "buffer_load_dwordx4 a[156:159], v232, s[24:27], s72 offen offset:3072 sc1\n"
    "s_add_u32 s72, s72, 0x1000\n"
    "v_exp_f32 v120, v120\n"
    "v_cvt_pk_bf16_f32 v11, v118, v119\n"
    "v_exp_f32 v121, v121\n"
    "v_exp_f32 v122, v122\n"
    "v_cvt_pk_bf16_f32 v12, v120, v121\n"
    "v_exp_f32 v123, v123\n"
    "v_mfma_f32_32x32x16_bf16 v[48:63], v[184:187], v[8:11], v[48:63]\n"
    "buffer_load_dwordx4 a[160:163], v231, s[24:27], s72 offen offset:0 sc1\n"
    "v_exp_f32 v124, v124\n"
    "v_cvt_pk_bf16_f32 v13, v122, v123\n"
    "v_exp_f32 v125, v125\n"
    "v_exp_f32 v126, v126\n"
    "v_cvt_pk_bf16_f32 v14, v124, v125\n"
    "v_exp_f32 v127, v127\n"
    "v_exp_f32 v64, v64\n"
    "v_cvt_pk_bf16_f32 v15, v126, v127\n"
    "v_exp_f32 v65, v65\n"
    "v_exp_f32 v66, v66\n"
    "v_cvt_pk_bf16_f32 v0, v64, v65\n"
    "v_mfma_f32_32x32x16_bf16 v[48:63], v[188:191], v[12:15], v[48:63]\n"
    "ds_read_b64_tr_b16 v[184:185], v230 offset:34048\n"
    "ds_read_b64_tr_b16 v[186:187], v230 offset:34432\n"
    "v_exp_f32 v67, v67\n"
    "v_exp_f32 v68, v68\n"
    "v_cvt_pk_bf16_f32 v1, v66, v67\n"
    "v_exp_f32 v69, v69\n"
    "v_mfma_f32_32x32x4_2b_bf16 v[96:127], v[136:137], v[220:221], 0 cbsz:1 abid:1\n"
    "ds_read_b64_tr_b16 v[188:189], v230 offset:34816\n"
    "ds_read_b64_tr_b16 v[190:191], v230 offset:35200\n"
    "v_exp_f32 v70, v70\n"
    "v_cvt_pk_bf16_f32 v2, v68, v69\n"
    "v_exp_f32 v71, v71\n"
    "v_exp_f32 v72, v72\n"
    "v_cvt_pk_bf16_f32 v3, v70, v71\n"
    "v_exp_f32 v73, v73\n"
    "v_exp_f32 v74, v74\n"
    "v_cvt_pk_bf16_f32 v4, v72, v73\n"
    "v_exp_f32 v75, v75\n"
    "s_waitcnt lgkmcnt(4)\n"
    "v_mfma_f32_32x32x16_bf16 v[32:47], v[140:143], v[0:3], v[32:47]\n"
    "ds_read_b128 v[128:131], v228 offset:30464\n"
    "ds_read_b64 v[138:139], v229 offset:39168\n"
    "v_exp_f32 v76, v76\n"
    "v_cvt_pk_bf16_f32 v5, v74, v75\n"
    "v_exp_f32 v77, v77\n"
    "v_exp_f32 v78, v78\n"
    "v_cvt_pk_bf16_f32 v6, v76, v77\n"
    "v_exp_f32 v79, v79\n"
    "v_mfma_f32_32x32x16_bf16 v[96:111], v[132:135], v[212:215], v[96:111]\n"
    "buffer_load_dwordx4 v238, s[32:35], s63 offen offset:2048 lds\n"
    "v_exp_f32 v80, v80\n"
    "v_cvt_pk_bf16_f32 v7, v78, v79\n"
    "v_exp_f32 v81, v81\n"
    "v_exp_f32 v82, v82\n"
    "v_cvt_pk_bf16_f32 v8, v80, v81\n"
    "v_exp_f32 v83, v83\n"
    "v_mfma_f32_32x32x16_bf16 v[32:47], v[144:147], v[4:7], v[32:47]\n"
    "v_exp_f32 v84, v84\n"
    "v_cvt_pk_bf16_f32 v9, v82, v83\n"
    "v_exp_f32 v85, v85\n"
    "v_exp_f32 v86, v86\n"
    "v_cvt_pk_bf16_f32 v10, v84, v85\n"
    "v_exp_f32 v87, v87\n"
    "v_mfma_f32_32x32x16_bf16 v[112:127], v[132:135], v[216:219], v[112:127]\n"
    "buffer_load_dwordx4 a[164:167], v232, s[24:27], s72 offen offset:0 sc1\n"
    "v_exp_f32 v88, v88\n"
    "v_cvt_pk_bf16_f32 v11, v86, v87\n"
    "v_exp_f32 v89, v89\n"
    "v_exp_f32 v90, v90\n"
    "v_cvt_pk_bf16_f32 v12, v88, v89\n"
    "v_exp_f32 v91, v91\n"
    "v_mfma_f32_32x32x16_bf16 v[48:63], v[140:143], v[8:11], v[48:63]\n"
    "v_exp_f32 v92, v92\n"
    "v_cvt_pk_bf16_f32 v13, v90, v91\n"
    "v_exp_f32 v93, v93\n"
    "v_exp_f32 v94, v94\n"
    "v_cvt_pk_bf16_f32 v14, v92, v93\n"
    "v_exp_f32 v95, v95\n"
    "v_exp_f32 v96, v96\n"
    "v_cvt_pk_bf16_f32 v15, v94, v95\n"
    "v_exp_f32 v97, v97\n"
    "v_exp_f32 v98, v98\n"
    "v_cvt_pk_bf16_f32 v0, v96, v97\n"
    "v_mfma_f32_32x32x16_bf16 v[48:63], v[144:147], v[12:15], v[48:63]\n"
    "ds_read_b64_tr_b16 v[140:141], v230 offset:35584\n"
    "ds_read_b64_tr_b16 v[142:143], v230 offset:35968\n"
    "v_exp_f32 v99, v99\n"
    "v_exp_f32 v100, v100\n"
    "v_cvt_pk_bf16_f32 v1, v98, v99\n"
    "v_exp_f32 v101, v101\n"
    "s_waitcnt lgkmcnt(2)\n"
    "v_mfma_f32_32x32x4_2b_bf16 v[64:95], v[138:139], v[220:221], 0 cbsz:1 abid:0\n"
    "ds_read_b64_tr_b16 v[144:145], v230 offset:36352\n"
    "ds_read_b64_tr_b16 v[146:147], v230 offset:36736\n"
    "v_exp_f32 v102, v102\n"
    "v_cvt_pk_bf16_f32 v2, v100, v101\n"
    "v_exp_f32 v103, v103\n"
    "v_exp_f32 v104, v104\n"
    "v_cvt_pk_bf16_f32 v3, v102, v103\n"
    "v_exp_f32 v105, v105\n"
    "v_exp_f32 v106, v106\n"
    "v_cvt_pk_bf16_f32 v4, v104, v105\n"
    "v_exp_f32 v107, v107\n"
    "v_mfma_f32_32x32x16_bf16 v[32:47], v[184:187], v[0:3], v[32:47]\n"
    "ds_read_b128 v[132:135], v228 offset:31488\n"
    "v_exp_f32 v108, v108\n"
    "v_cvt_pk_bf16_f32 v5, v106, v107\n"
    "v_exp_f32 v109, v109\n"
    "v_exp_f32 v110, v110\n"
    "v_cvt_pk_bf16_f32 v6, v108, v109\n"
    "v_exp_f32 v111, v111\n"
    "v_mfma_f32_32x32x16_bf16 v[64:79], v[128:131], v[212:215], v[64:79]\n"
    "buffer_load_dwordx4 v239, s[32:35], s63 offen offset:3072 lds\n"
    "v_exp_f32 v112, v112\n"
    "v_cvt_pk_bf16_f32 v7, v110, v111\n"
    "v_exp_f32 v113, v113\n"
    "v_exp_f32 v114, v114\n"
    "v_cvt_pk_bf16_f32 v8, v112, v113\n"
    "v_exp_f32 v115, v115\n"
    "v_mfma_f32_32x32x16_bf16 v[32:47], v[188:191], v[4:7], v[32:47]\n"
    "v_exp_f32 v116, v116\n"
    "v_cvt_pk_bf16_f32 v9, v114, v115\n"
    "v_exp_f32 v117, v117\n"
    "v_exp_f32 v118, v118\n"
    "v_cvt_pk_bf16_f32 v10, v116, v117\n"
    "v_exp_f32 v119, v119\n"
    "v_mfma_f32_32x32x16_bf16 v[80:95], v[128:131], v[216:219], v[80:95]\n"
    "buffer_load_dwordx4 a[168:171], v231, s[24:27], s72 offen offset:1024 sc1\n"
    "v_exp_f32 v120, v120\n"
    "v_cvt_pk_bf16_f32 v11, v118, v119\n"
    "v_exp_f32 v121, v121\n"
    "v_exp_f32 v122, v122\n"
    "v_cvt_pk_bf16_f32 v12, v120, v121\n"
    "v_exp_f32 v123, v123\n"
    "v_mfma_f32_32x32x16_bf16 v[48:63], v[184:187], v[8:11], v[48:63]\n"
    "buffer_load_dwordx4 a[172:175], v232, s[24:27], s72 offen offset:1024 sc1\n"
    "v_exp_f32 v124, v124\n"
    "v_cvt_pk_bf16_f32 v13, v122, v123\n"
    "v_exp_f32 v125, v125\n"
    "v_exp_f32 v126, v126\n"
    "v_cvt_pk_bf16_f32 v14, v124, v125\n"
    "v_exp_f32 v127, v127\n"
    "v_exp_f32 v64, v64\n"
    "v_cvt_pk_bf16_f32 v15, v126, v127\n"
    "v_exp_f32 v65, v65\n"
    "v_exp_f32 v66, v66\n"
    "v_cvt_pk_bf16_f32 v0, v64, v65\n"
    "v_mfma_f32_32x32x16_bf16 v[48:63], v[188:191], v[12:15], v[48:63]\n"
    "ds_read_b64_tr_b16 v[184:185], v230 offset:37120\n"
    "ds_read_b64_tr_b16 v[186:187], v230 offset:37504\n"
    "v_exp_f32 v67, v67\n"
    "v_exp_f32 v68, v68\n"
    "v_cvt_pk_bf16_f32 v1, v66, v67\n"
    "v_exp_f32 v69, v69\n"
    "v_mfma_f32_32x32x4_2b_bf16 v[96:127], v[138:139], v[220:221], 0 cbsz:1 abid:1\n"
    "ds_read_b64_tr_b16 v[188:189], v230 offset:37888\n"
    "ds_read_b64_tr_b16 v[190:191], v230 offset:38272\n"
    "v_exp_f32 v70, v70\n"
    "v_cvt_pk_bf16_f32 v2, v68, v69\n"
    "v_exp_f32 v71, v71\n"
    "v_exp_f32 v72, v72\n"
    "v_cvt_pk_bf16_f32 v3, v70, v71\n"
    "v_exp_f32 v73, v73\n"
    "v_exp_f32 v74, v74\n"
    "v_cvt_pk_bf16_f32 v4, v72, v73\n"
    "v_exp_f32 v75, v75\n"
    "s_waitcnt lgkmcnt(4)\n"
    "v_mfma_f32_32x32x16_bf16 v[32:47], v[140:143], v[0:3], v[32:47]\n"
    "s_add_u32 m0, s62, 0x13900\n"
    "ds_read_b128 v[128:131], v228 offset:41088\n"
    "ds_read_b64 v[136:137], v229 offset:51328\n"
    "v_exp_f32 v76, v76\n"
    "v_cvt_pk_bf16_f32 v5, v74, v75\n"
    "v_exp_f32 v77, v77\n"
    "v_exp_f32 v78, v78\n"
    "v_cvt_pk_bf16_f32 v6, v76, v77\n"
    "v_exp_f32 v79, v79\n"
    "v_mfma_f32_32x32x16_bf16 v[96:111], v[132:135], v[212:215], v[96:111]\n"
    "buffer_load_dwordx4 v240, s[32:35], s63 offen offset:0 lds\n"
    "v_exp_f32 v80, v80\n"
    "v_cvt_pk_bf16_f32 v7, v78, v79\n"
    "v_exp_f32 v81, v81\n"
    "v_exp_f32 v82, v82\n"
    "v_cvt_pk_bf16_f32 v8, v80, v81\n"
    "v_exp_f32 v83, v83\n"
    "v_mfma_f32_32x32x16_bf16 v[32:47], v[144:147], v[4:7], v[32:47]\n"
    "v_exp_f32 v84, v84\n"
    "v_cvt_pk_bf16_f32 v9, v82, v83\n"
    "v_exp_f32 v85, v85\n"
    "v_exp_f32 v86, v86\n"
    "v_cvt_pk_bf16_f32 v10, v84, v85\n"
    "v_exp_f32 v87, v87\n"
    "v_mfma_f32_32x32x16_bf16 v[112:127], v[132:135], v[216:219], v[112:127]\n"
    "buffer_load_dwordx4 a[176:179], v231, s[24:27], s72 offen offset:2048 sc1\n"
    "v_exp_f32 v88, v88\n"
    "v_cvt_pk_bf16_f32 v11, v86, v87\n"
    "v_exp_f32 v89, v89\n"
    "v_exp_f32 v90, v90\n"
    "v_cvt_pk_bf16_f32 v12, v88, v89\n"
    "v_exp_f32 v91, v91\n"
    "v_mfma_f32_32x32x16_bf16 v[48:63], v[140:143], v[8:11], v[48:63]\n"
    "v_exp_f32 v92, v92\n"
    "v_cvt_pk_bf16_f32 v13, v90, v91\n"
    "v_exp_f32 v93, v93\n"
    "v_exp_f32 v94, v94\n"
    "v_cvt_pk_bf16_f32 v14, v92, v93\n"
    "v_exp_f32 v95, v95\n"
    "v_exp_f32 v96, v96\n"
    "v_cvt_pk_bf16_f32 v15, v94, v95\n"
    "v_exp_f32 v97, v97\n"
    "v_exp_f32 v98, v98\n"
    "v_cvt_pk_bf16_f32 v0, v96, v97\n"
    "v_mfma_f32_32x32x16_bf16 v[48:63], v[144:147], v[12:15], v[48:63]\n"
    "ds_read_b64_tr_b16 v[140:141], v230 offset:45184\n"
    "ds_read_b64_tr_b16 v[142:143], v230 offset:45568\n"
    "v_exp_f32 v99, v99\n"
    "v_exp_f32 v100, v100\n"
    "v_cvt_pk_bf16_f32 v1, v98, v99\n"
    "v_exp_f32 v101, v101\n"
    "s_waitcnt lgkmcnt(2)\n"
    "v_mfma_f32_32x32x4_2b_bf16 v[64:95], v[136:137], v[220:221], 0 cbsz:1 abid:0\n"
    "ds_read_b64_tr_b16 v[144:145], v230 offset:45952\n"
    "ds_read_b64_tr_b16 v[146:147], v230 offset:46336\n"
    "v_exp_f32 v102, v102\n"
    "v_cvt_pk_bf16_f32 v2, v100, v101\n"
    "v_exp_f32 v103, v103\n"
    "v_exp_f32 v104, v104\n"
    "v_cvt_pk_bf16_f32 v3, v102, v103\n"
    "v_exp_f32 v105, v105\n"
    "v_exp_f32 v106, v106\n"
    "v_cvt_pk_bf16_f32 v4, v104, v105\n"
    "v_exp_f32 v107, v107\n"
    "v_mfma_f32_32x32x16_bf16 v[32:47], v[184:187], v[0:3], v[32:47]\n"
    "ds_read_b128 v[132:135], v228 offset:42112\n"
    "v_exp_f32 v108, v108\n"
    "v_cvt_pk_bf16_f32 v5, v106, v107\n"
    "v_exp_f32 v109, v109\n"
    "v_exp_f32 v110, v110\n"
    "v_cvt_pk_bf16_f32 v6, v108, v109\n"
    "v_exp_f32 v111, v111\n"
    "v_mfma_f32_32x32x16_bf16 v[64:79], v[128:131], v[212:215], v[64:79]\n"
    "buffer_load_dwordx4 v241, s[32:35], s63 offen offset:1024 lds\n"
    "v_exp_f32 v112, v112\n"
    "v_cvt_pk_bf16_f32 v7, v110, v111\n"
    "v_exp_f32 v113, v113\n"
    "v_exp_f32 v114, v114\n"
    "v_cvt_pk_bf16_f32 v8, v112, v113\n"
    "v_exp_f32 v115, v115\n"
    "v_mfma_f32_32x32x16_bf16 v[32:47], v[188:191], v[4:7], v[32:47]\n"
    "v_exp_f32 v116, v116\n"
    "v_cvt_pk_bf16_f32 v9, v114, v115\n"
    "v_exp_f32 v117, v117\n"
    "v_exp_f32 v118, v118\n"
    "v_cvt_pk_bf16_f32 v10, v116, v117\n"
    "v_exp_f32 v119, v119\n"
    "v_mfma_f32_32x32x16_bf16 v[80:95], v[128:131], v[216:219], v[80:95]\n"
    "buffer_load_dwordx4 a[180:183], v232, s[24:27], s72 offen offset:2048 sc1\n"
    "v_exp_f32 v120, v120\n"
    "v_cvt_pk_bf16_f32 v11, v118, v119\n"
    "v_exp_f32 v121, v121\n"
    "v_exp_f32 v122, v122\n"
    "v_cvt_pk_bf16_f32 v12, v120, v121\n"
    "v_exp_f32 v123, v123\n"
    "v_mfma_f32_32x32x16_bf16 v[48:63], v[184:187], v[8:11], v[48:63]\n"
    "buffer_load_dwordx4 a[184:187], v231, s[24:27], s72 offen offset:3072 sc1\n"
    "v_exp_f32 v124, v124\n"
    "v_cvt_pk_bf16_f32 v13, v122, v123\n"
    "v_exp_f32 v125, v125\n"
    "v_exp_f32 v126, v126\n"
    "v_cvt_pk_bf16_f32 v14, v124, v125\n"
    "v_exp_f32 v127, v127\n"
    "v_exp_f32 v64, v64\n"
    "v_cvt_pk_bf16_f32 v15, v126, v127\n"
    "v_exp_f32 v65, v65\n"
    "v_exp_f32 v66, v66\n"
    "v_cvt_pk_bf16_f32 v0, v64, v65\n"
    "v_mfma_f32_32x32x16_bf16 v[48:63], v[188:191], v[12:15], v[48:63]\n"
    "ds_read_b64_tr_b16 v[184:185], v230 offset:46720\n"
    "ds_read_b64_tr_b16 v[186:187], v230 offset:47104\n"
    "v_exp_f32 v67, v67\n"
    "v_exp_f32 v68, v68\n"
    "v_cvt_pk_bf16_f32 v1, v66, v67\n"
    "v_exp_f32 v69, v69\n"
    "v_mfma_f32_32x32x4_2b_bf16 v[96:127], v[136:137], v[220:221], 0 cbsz:1 abid:1\n"
    "ds_read_b64_tr_b16 v[188:189], v230 offset:47488\n"
    "ds_read_b64_tr_b16 v[190:191], v230 offset:47872\n"
    "v_exp_f32 v70, v70\n"
    "v_cvt_pk_bf16_f32 v2, v68, v69\n"
    "v_exp_f32 v71, v71\n"
    "v_exp_f32 v72, v72\n"
    "v_cvt_pk_bf16_f32 v3, v70, v71\n"
    "v_exp_f32 v73, v73\n"
    "v_exp_f32 v74, v74\n"
    "v_cvt_pk_bf16_f32 v4, v72, v73\n"
    "v_exp_f32 v75, v75\n"
    "s_waitcnt lgkmcnt(4)\n"
    "v_mfma_f32_32x32x16_bf16 v[32:47], v[140:143], v[0:3], v[32:47]\n"
    "ds_read_b128 v[128:131], v228 offset:43136\n"
    "ds_read_b64 v[138:139], v229 offset:51840\n"
    "v_exp_f32 v76, v76\n"
    "v_cvt_pk_bf16_f32 v5, v74, v75\n"
    "v_exp_f32 v77, v77\n"
    "v_exp_f32 v78, v78\n"
    "v_cvt_pk_bf16_f32 v6, v76, v77\n"
    "v_exp_f32 v79, v79\n"
    "v_mfma_f32_32x32x16_bf16 v[96:111], v[132:135], v[212:215], v[96:111]\n"
    "buffer_load_dwordx4 v242, s[32:35], s63 offen offset:2048 lds\n"
    "v_exp_f32 v80, v80\n"
    "v_cvt_pk_bf16_f32 v7, v78, v79\n"
    "v_exp_f32 v81, v81\n"
    "v_exp_f32 v82, v82\n"
    "v_cvt_pk_bf16_f32 v8, v80, v81\n"
    "v_exp_f32 v83, v83\n"
    "v_mfma_f32_32x32x16_bf16 v[32:47], v[144:147], v[4:7], v[32:47]\n"
    "v_exp_f32 v84, v84\n"
    "v_cvt_pk_bf16_f32 v9, v82, v83\n"
    "v_exp_f32 v85, v85\n"
    "v_exp_f32 v86, v86\n"
    "v_cvt_pk_bf16_f32 v10, v84, v85\n"
    "v_exp_f32 v87, v87\n"
    "v_mfma_f32_32x32x16_bf16 v[112:127], v[132:135], v[216:219], v[112:127]\n"
    "buffer_load_dwordx4 a[188:191], v232, s[24:27], s72 offen offset:3072 sc1\n"
    "v_exp_f32 v88, v88\n"
    "v_cvt_pk_bf16_f32 v11, v86, v87\n"
    "v_exp_f32 v89, v89\n"
    "v_exp_f32 v90, v90\n"
    "v_cvt_pk_bf16_f32 v12, v88, v89\n"
    "v_exp_f32 v91, v91\n"
    "v_mfma_f32_32x32x16_bf16 v[48:63], v[140:143], v[8:11], v[48:63]\n"
    "v_exp_f32 v92, v92\n"
    "v_cvt_pk_bf16_f32 v13, v90, v91\n"
    "v_exp_f32 v93, v93\n"
    "v_exp_f32 v94, v94\n"
    "v_cvt_pk_bf16_f32 v14, v92, v93\n"
    "v_exp_f32 v95, v95\n"
    "v_exp_f32 v96, v96\n"
    "v_cvt_pk_bf16_f32 v15, v94, v95\n"
    "v_exp_f32 v97, v97\n"
    "v_exp_f32 v98, v98\n"
    "v_cvt_pk_bf16_f32 v0, v96, v97\n"
    "v_mfma_f32_32x32x16_bf16 v[48:63], v[144:147], v[12:15], v[48:63]\n"
    "ds_read_b64_tr_b16 v[140:141], v230 offset:48256\n"
    "ds_read_b64_tr_b16 v[142:143], v230 offset:48640\n"
    "v_exp_f32 v99, v99\n"
    "v_exp_f32 v100, v100\n"
    "v_cvt_pk_bf16_f32 v1, v98, v99\n"
    "v_exp_f32 v101, v101\n"
    "s_waitcnt lgkmcnt(2)\n"
    "v_mfma_f32_32x32x4_2b_bf16 v[64:95], v[138:139], v[220:221], 0 cbsz:1 abid:0\n"
    "ds_read_b64_tr_b16 v[144:145], v230 offset:49024\n"
    "ds_read_b64_tr_b16 v[146:147], v230 offset:49408\n"
    "v_exp_f32 v102, v102\n"
    "v_cvt_pk_bf16_f32 v2, v100, v101\n"
    "v_exp_f32 v103, v103\n"
    "v_exp_f32 v104, v104\n"
    "v_cvt_pk_bf16_f32 v3, v102, v103\n"
    "v_exp_f32 v105, v105\n"
    "v_exp_f32 v106, v106\n"
    "v_cvt_pk_bf16_f32 v4, v104, v105\n"
    "v_exp_f32 v107, v107\n"
    "v_mfma_f32_32x32x16_bf16 v[32:47], v[184:187], v[0:3], v[32:47]\n"
    "ds_read_b128 v[132:135], v228 offset:44160\n"
    "v_exp_f32 v108, v108\n"
    "v_cvt_pk_bf16_f32 v5, v106, v107\n"
    "v_exp_f32 v109, v109\n"
    "v_exp_f32 v110, v110\n"
    "v_cvt_pk_bf16_f32 v6, v108, v109\n"
    "v_exp_f32 v111, v111\n"
    "v_mfma_f32_32x32x16_bf16 v[64:79], v[128:131], v[212:215], v[64:79]\n"
    "buffer_load_dwordx4 v243, s[32:35], s63 offen offset:3072 lds\n"
    "v_exp_f32 v112, v112\n"
    "v_cvt_pk_bf16_f32 v7, v110, v111\n"
    "v_exp_f32 v113, v113\n"
    "v_exp_f32 v114, v114\n"
    "v_cvt_pk_bf16_f32 v8, v112, v113\n"
    "v_exp_f32 v115, v115\n"
    "v_mfma_f32_32x32x16_bf16 v[32:47], v[188:191], v[4:7], v[32:47]\n"
    "s_add_u32 s72, s72, 0x1000\n"
    "v_exp_f32 v116, v116\n"
    "v_cvt_pk_bf16_f32 v9, v114, v115\n"
    "v_exp_f32 v117, v117\n"
    "v_exp_f32 v118, v118\n"
    "v_cvt_pk_bf16_f32 v10, v116, v117\n"
    "v_exp_f32 v119, v119\n"
    "v_mfma_f32_32x32x16_bf16 v[80:95], v[128:131], v[216:219], v[80:95]\n"
    "buffer_load_dwordx4 a[192:195], v231, s[24:27], s72 offen offset:0 sc1\n"
    "v_exp_f32 v120, v120\n"
    "v_cvt_pk_bf16_f32 v11, v118, v119\n"
    "v_exp_f32 v121, v121\n"
    "v_exp_f32 v122, v122\n"
    "v_cvt_pk_bf16_f32 v12, v120, v121\n"
    "v_exp_f32 v123, v123\n"
    "v_mfma_f32_32x32x16_bf16 v[48:63], v[184:187], v[8:11], v[48:63]\n"
    "buffer_load_dwordx4 a[196:199], v232, s[24:27], s72 offen offset:0 sc1\n"
    "v_exp_f32 v124, v124\n"
    "v_cvt_pk_bf16_f32 v13, v122, v123\n"
    "v_exp_f32 v125, v125\n"
    "v_exp_f32 v126, v126\n"
    "v_cvt_pk_bf16_f32 v14, v124, v125\n"
    "v_exp_f32 v127, v127\n"
    "v_exp_f32 v64, v64\n"
    "v_cvt_pk_bf16_f32 v15, v126, v127\n"
    "v_exp_f32 v65, v65\n"
    "v_exp_f32 v66, v66\n"
    "v_cvt_pk_bf16_f32 v0, v64, v65\n"
    "v_mfma_f32_32x32x16_bf16 v[48:63], v[188:191], v[12:15], v[48:63]\n"
    "ds_read_b64_tr_b16 v[184:185], v230 offset:49792\n"
    "ds_read_b64_tr_b16 v[186:187], v230 offset:50176\n"
    "v_exp_f32 v67, v67\n"
    "v_exp_f32 v68, v68\n"
    "v_cvt_pk_bf16_f32 v1, v66, v67\n"
    "v_exp_f32 v69, v69\n"
    "v_mfma_f32_32x32x4_2b_bf16 v[96:127], v[138:139], v[220:221], 0 cbsz:1 abid:1\n"
    "ds_read_b64_tr_b16 v[188:189], v230 offset:50560\n"
    "ds_read_b64_tr_b16 v[190:191], v230 offset:50944\n"
    "v_exp_f32 v70, v70\n"
    "v_cvt_pk_bf16_f32 v2, v68, v69\n"
    "v_exp_f32 v71, v71\n"
    "v_exp_f32 v72, v72\n"
    "v_cvt_pk_bf16_f32 v3, v70, v71\n"
    "v_exp_f32 v73, v73\n"
    "v_exp_f32 v74, v74\n"
    "v_cvt_pk_bf16_f32 v4, v72, v73\n"
    "v_exp_f32 v75, v75\n"
    "s_waitcnt lgkmcnt(4)\n"
    "v_mfma_f32_32x32x16_bf16 v[32:47], v[140:143], v[0:3], v[32:47]\n"
    "s_add_u32 m0, s62, 0x14900\n"
    "v_add_u32 v228, 0xfffe8000, v228\n"
    "ds_read_b128 v[128:131], v228 offset:0\n"
    "v_add_u32 v229, 0xfffe8800, v229\n"
    "ds_read_b64 v[136:137], v229 offset:8192\n"
    "v_exp_f32 v76, v76\n"
    "v_cvt_pk_bf16_f32 v5, v74, v75\n"
    "v_exp_f32 v77, v77\n"
    "v_exp_f32 v78, v78\n"
    "v_cvt_pk_bf16_f32 v6, v76, v77\n"
    "v_exp_f32 v79, v79\n"
    "v_mfma_f32_32x32x16_bf16 v[96:111], v[132:135], v[212:215], v[96:111]\n"
    "buffer_load_dwordx4 v244, s[32:35], s63 offen offset:0 lds\n"
    "v_exp_f32 v80, v80\n"
    "v_cvt_pk_bf16_f32 v7, v78, v79\n"
    "v_exp_f32 v81, v81\n"
    "v_exp_f32 v82, v82\n"
    "v_cvt_pk_bf16_f32 v8, v80, v81\n"
    "v_exp_f32 v83, v83\n"
    "v_mfma_f32_32x32x16_bf16 v[32:47], v[144:147], v[4:7], v[32:47]\n"
    "v_exp_f32 v84, v84\n"
    "v_cvt_pk_bf16_f32 v9, v82, v83\n"
    "v_exp_f32 v85, v85\n"
    "v_exp_f32 v86, v86\n"
    "v_cvt_pk_bf16_f32 v10, v84, v85\n"
    "v_exp_f32 v87, v87\n"
    "v_mfma_f32_32x32x16_bf16 v[112:127], v[132:135], v[216:219], v[112:127]\n"
    "buffer_load_dwordx4 a[200:203], v231, s[24:27], s72 offen offset:1024 sc1\n"
    "v_exp_f32 v88, v88\n"
    "v_cvt_pk_bf16_f32 v11, v86, v87\n"
    "v_exp_f32 v89, v89\n"
    "v_exp_f32 v90, v90\n"
    "v_cvt_pk_bf16_f32 v12, v88, v89\n"
    "v_exp_f32 v91, v91\n"
    "v_mfma_f32_32x32x16_bf16 v[48:63], v[140:143], v[8:11], v[48:63]\n"
    "v_exp_f32 v92, v92\n"
    "v_cvt_pk_bf16_f32 v13, v90, v91\n"
    "v_exp_f32 v93, v93\n"
    "v_exp_f32 v94, v94\n"
    "v_cvt_pk_bf16_f32 v14, v92, v93\n"
    "v_exp_f32 v95, v95\n"
    "v_exp_f32 v96, v96\n"
    "v_cvt_pk_bf16_f32 v15, v94, v95\n"
    "v_exp_f32 v97, v97\n"
    "v_exp_f32 v98, v98\n"
    "v_cvt_pk_bf16_f32 v0, v96, v97\n"
    "v_mfma_f32_32x32x16_bf16 v[48:63], v[144:147], v[12:15], v[48:63]\n"
    "v_add_u32 v230, 0xfffe8000, v230\n"
    "ds_read_b64_tr_b16 v[140:141], v230 offset:4096\n"
    "ds_read_b64_tr_b16 v[142:143], v230 offset:4480\n"
    "v_exp_f32 v99, v99\n"
    "v_exp_f32 v100, v100\n"
    "v_cvt_pk_bf16_f32 v1, v98, v99\n"
    "v_exp_f32 v101, v101\n"
    "s_waitcnt lgkmcnt(2)\n"
    "v_mfma_f32_32x32x4_2b_bf16 v[64:95], v[136:137], v[220:221], 0 cbsz:1 abid:0\n"
    "ds_read_b64_tr_b16 v[144:145], v230 offset:4864\n"
    "ds_read_b64_tr_b16 v[146:147], v230 offset:5248\n"
    "v_exp_f32 v102, v102\n"
    "v_cvt_pk_bf16_f32 v2, v100, v101\n"
    "v_exp_f32 v103, v103\n"
    "v_exp_f32 v104, v104\n"
    "v_cvt_pk_bf16_f32 v3, v102, v103\n"
    "v_exp_f32 v105, v105\n"
    "v_exp_f32 v106, v106\n"
    "v_cvt_pk_bf16_f32 v4, v104, v105\n"
    "v_exp_f32 v107, v107\n"
    "v_mfma_f32_32x32x16_bf16 v[32:47], v[184:187], v[0:3], v[32:47]\n"
    "ds_read_b128 v[132:135], v228 offset:1024\n"
    "v_exp_f32 v108, v108\n"
    "v_cvt_pk_bf16_f32 v5, v106, v107\n"
    "v_exp_f32 v109, v109\n"
    "v_exp_f32 v110, v110\n"
    "v_cvt_pk_bf16_f32 v6, v108, v109\n"
    "v_exp_f32 v111, v111\n"
    "v_mfma_f32_32x32x16_bf16 v[64:79], v[128:131], v[212:215], v[64:79]\n"
    "buffer_load_dwordx4 a[204:207], v232, s[24:27], s72 offen offset:1024 sc1\n"
    "v_exp_f32 v112, v112\n"
    "v_cvt_pk_bf16_f32 v7, v110, v111\n"
    "v_exp_f32 v113, v113\n"
    "v_exp_f32 v114, v114\n"
    "v_cvt_pk_bf16_f32 v8, v112, v113\n"
    "v_exp_f32 v115, v115\n"
    "v_mfma_f32_32x32x16_bf16 v[32:47], v[188:191], v[4:7], v[32:47]\n"
    "v_exp_f32 v116, v116\n"
    "v_cvt_pk_bf16_f32 v9, v114, v115\n"
    "v_exp_f32 v117, v117\n"
    "v_exp_f32 v118, v118\n"
    "v_cvt_pk_bf16_f32 v10, v116, v117\n"
    "v_exp_f32 v119, v119\n"
    "v_mfma_f32_32x32x16_bf16 v[80:95], v[128:131], v[216:219], v[80:95]\n"
    "buffer_load_dwordx4 a[208:211], v231, s[24:27], s72 offen offset:2048 sc1\n"
    "v_exp_f32 v120, v120\n"
    "v_cvt_pk_bf16_f32 v11, v118, v119\n"
    "v_exp_f32 v121, v121\n"
    "v_exp_f32 v122, v122\n"
    "v_cvt_pk_bf16_f32 v12, v120, v121\n"
    "v_exp_f32 v123, v123\n"
    "v_mfma_f32_32x32x16_bf16 v[48:63], v[184:187], v[8:11], v[48:63]\n"
    "v_exp_f32 v124, v124\n"
    "v_cvt_pk_bf16_f32 v13, v122, v123\n"
    "v_exp_f32 v125, v125\n"
    "v_exp_f32 v126, v126\n"
    "v_cvt_pk_bf16_f32 v14, v124, v125\n"
    "v_exp_f32 v127, v127\n"
    "v_exp_f32 v64, v64\n"
    "v_cvt_pk_bf16_f32 v15, v126, v127\n"
    "v_exp_f32 v65, v65\n"
    "v_exp_f32 v66, v66\n"
    "v_cvt_pk_bf16_f32 v0, v64, v65\n"
    "v_mfma_f32_32x32x16_bf16 v[48:63], v[188:191], v[12:15], v[48:63]\n"
    "ds_read_b64_tr_b16 v[184:185], v230 offset:5632\n"
    "ds_read_b64_tr_b16 v[186:187], v230 offset:6016\n"
    "v_exp_f32 v67, v67\n"
    "v_exp_f32 v68, v68\n"
    "v_cvt_pk_bf16_f32 v1, v66, v67\n"
    "v_exp_f32 v69, v69\n"
    "v_mfma_f32_32x32x4_2b_bf16 v[96:127], v[136:137], v[220:221], 0 cbsz:1 abid:1\n"
    "ds_read_b64_tr_b16 v[188:189], v230 offset:6400\n"
    "ds_read_b64_tr_b16 v[190:191], v230 offset:6784\n"
    "v_exp_f32 v70, v70\n"
    "v_cvt_pk_bf16_f32 v2, v68, v69\n"
    "v_exp_f32 v71, v71\n"
    "v_exp_f32 v72, v72\n"
    "v_cvt_pk_bf16_f32 v3, v70, v71\n"
    "v_exp_f32 v73, v73\n"
    "v_exp_f32 v74, v74\n"
    "v_cvt_pk_bf16_f32 v4, v72, v73\n"
    "v_exp_f32 v75, v75\n"
    "s_waitcnt lgkmcnt(4)\n"
    "v_mfma_f32_32x32x16_bf16 v[32:47], v[140:143], v[0:3], v[32:47]\n"
    "ds_read_b128 v[128:131], v228 offset:2048\n"
    "ds_read_b64 v[138:139], v229 offset:8704\n"
    "v_exp_f32 v76, v76\n"
    "v_cvt_pk_bf16_f32 v5, v74, v75\n"
    "v_exp_f32 v77, v77\n"
    "v_exp_f32 v78, v78\n"
    "v_cvt_pk_bf16_f32 v6, v76, v77\n"
    "v_exp_f32 v79, v79\n"
    "v_mfma_f32_32x32x16_bf16 v[96:111], v[132:135], v[212:215], v[96:111]\n"
    "buffer_load_dwordx4 a[212:215], v232, s[24:27], s72 offen offset:2048 sc1\n"
    "v_exp_f32 v80, v80\n"
    "v_cvt_pk_bf16_f32 v7, v78, v79\n"
    "v_exp_f32 v81, v81\n"
    "v_exp_f32 v82, v82\n"
    "v_cvt_pk_bf16_f32 v8, v80, v81\n"
    "v_exp_f32 v83, v83\n"
    "v_mfma_f32_32x32x16_bf16 v[32:47], v[144:147], v[4:7], v[32:47]\n"
    "v_exp_f32 v84, v84\n"
    "v_cvt_pk_bf16_f32 v9, v82, v83\n"
    "v_exp_f32 v85, v85\n"
    "v_exp_f32 v86, v86\n"
    "v_cvt_pk_bf16_f32 v10, v84, v85\n"
    "v_exp_f32 v87, v87\n"
    "v_mfma_f32_32x32x16_bf16 v[112:127], v[132:135], v[216:219], v[112:127]\n"
    "v_exp_f32 v88, v88\n"
    "v_cvt_pk_bf16_f32 v11, v86, v87\n"
    "v_exp_f32 v89, v89\n"
    "v_exp_f32 v90, v90\n"
    "v_cvt_pk_bf16_f32 v12, v88, v89\n"
    "v_exp_f32 v91, v91\n"
    "v_mfma_f32_32x32x16_bf16 v[48:63], v[140:143], v[8:11], v[48:63]\n"
    "v_exp_f32 v92, v92\n"
    "v_cvt_pk_bf16_f32 v13, v90, v91\n"
    "v_exp_f32 v93, v93\n"
    "v_exp_f32 v94, v94\n"
    "v_cvt_pk_bf16_f32 v14, v92, v93\n"
    "v_exp_f32 v95, v95\n"
    "v_exp_f32 v96, v96\n"
    "v_cvt_pk_bf16_f32 v15, v94, v95\n"
    "v_exp_f32 v97, v97\n"
    "v_exp_f32 v98, v98\n"
    "v_cvt_pk_bf16_f32 v0, v96, v97\n"
    "v_mfma_f32_32x32x16_bf16 v[48:63], v[144:147], v[12:15], v[48:63]\n"
    "ds_read_b64_tr_b16 v[140:141], v230 offset:7168\n"
    "ds_read_b64_tr_b16 v[142:143], v230 offset:7552\n"
    "v_exp_f32 v99, v99\n"
    "v_exp_f32 v100, v100\n"
    "v_cvt_pk_bf16_f32 v1, v98, v99\n"
    "v_exp_f32 v101, v101\n"
    "s_waitcnt lgkmcnt(2)\n"
    "v_mfma_f32_32x32x4_2b_bf16 v[64:95], v[138:139], v[220:221], 0 cbsz:1 abid:0\n"
    "ds_read_b64_tr_b16 v[144:145], v230 offset:7936\n"
    "ds_read_b64_tr_b16 v[146:147], v230 offset:8320\n"
    "v_exp_f32 v102, v102\n"
    "v_cvt_pk_bf16_f32 v2, v100, v101\n"
    "v_exp_f32 v103, v103\n"
    "v_exp_f32 v104, v104\n"
    "v_cvt_pk_bf16_f32 v3, v102, v103\n"
    "v_exp_f32 v105, v105\n"
    "v_exp_f32 v106, v106\n"
    "v_cvt_pk_bf16_f32 v4, v104, v105\n"
    "v_exp_f32 v107, v107\n"
    "v_mfma_f32_32x32x16_bf16 v[32:47], v[184:187], v[0:3], v[32:47]\n"
    "ds_read_b128 v[132:135], v228 offset:3072\n"
    "v_exp_f32 v108, v108\n"
    "v_cvt_pk_bf16_f32 v5, v106, v107\n"
    "v_exp_f32 v109, v109\n"
    "v_exp_f32 v110, v110\n"
    "v_cvt_pk_bf16_f32 v6, v108, v109\n"
    "v_exp_f32 v111, v111\n"
    "v_mfma_f32_32x32x16_bf16 v[64:79], v[128:131], v[212:215], v[64:79]\n"
    "buffer_load_dwordx4 a[216:219], v231, s[24:27], s72 offen offset:3072 sc1\n"
    "v_exp_f32 v112, v112\n"
    "v_cvt_pk_bf16_f32 v7, v110, v111\n"
    "v_exp_f32 v113, v113\n"
    "v_exp_f32 v114, v114\n"
    "v_cvt_pk_bf16_f32 v8, v112, v113\n"
    "v_exp_f32 v115, v115\n"
    "v_mfma_f32_32x32x16_bf16 v[32:47], v[188:191], v[4:7], v[32:47]\n"
    "v_exp_f32 v116, v116\n"
    "v_cvt_pk_bf16_f32 v9, v114, v115\n"
    "v_exp_f32 v117, v117\n"
    "v_exp_f32 v118, v118\n"
    "v_cvt_pk_bf16_f32 v10, v116, v117\n"
    "v_exp_f32 v119, v119\n"
    "v_mfma_f32_32x32x16_bf16 v[80:95], v[128:131], v[216:219], v[80:95]\n"
    "buffer_load_dwordx4 a[220:223], v232, s[24:27], s72 offen offset:3072 sc1\n"
    "v_exp_f32 v120, v120\n"
    "v_cvt_pk_bf16_f32 v11, v118, v119\n"
    "v_exp_f32 v121, v121\n"
    "v_exp_f32 v122, v122\n"
    "v_cvt_pk_bf16_f32 v12, v120, v121\n"
    "v_exp_f32 v123, v123\n"
    "v_mfma_f32_32x32x16_bf16 v[48:63], v[184:187], v[8:11], v[48:63]\n"
    "v_exp_f32 v124, v124\n"
    "v_cvt_pk_bf16_f32 v13, v122, v123\n"
    "v_exp_f32 v125, v125\n"
    "v_exp_f32 v126, v126\n"
    "v_cvt_pk_bf16_f32 v14, v124, v125\n"
    "v_exp_f32 v127, v127\n"
    "s_waitcnt vmcnt(36)\n"
    "s_barrier\n"
    "v_exp_f32 v64, v64\n"
    "v_cvt_pk_bf16_f32 v15, v126, v127\n"
    "v_exp_f32 v65, v65\n"
    "v_exp_f32 v66, v66\n"
    "v_cvt_pk_bf16_f32 v0, v64, v65\n"
    "v_mfma_f32_32x32x16_bf16 v[48:63], v[188:191], v[12:15], v[48:63]\n"
    "ds_read_b64_tr_b16 v[184:185], v230 offset:8704\n"
    "ds_read_b64_tr_b16 v[186:187], v230 offset:9088\n"
    "v_exp_f32 v67, v67\n"
    "v_exp_f32 v68, v68\n"
    "v_cvt_pk_bf16_f32 v1, v66, v67\n"
    "v_exp_f32 v69, v69\n"
    "v_mfma_f32_32x32x4_2b_bf16 v[96:127], v[138:139], v[220:221], 0 cbsz:1 abid:1\n"
    "ds_read_b64_tr_b16 v[188:189], v230 offset:9472\n"
    "ds_read_b64_tr_b16 v[190:191], v230 offset:9856\n"
    "v_exp_f32 v70, v70\n"
    "v_cvt_pk_bf16_f32 v2, v68, v69\n"
    "v_exp_f32 v71, v71\n"
    "v_exp_f32 v72, v72\n"
    "v_cvt_pk_bf16_f32 v3, v70, v71\n"
    "v_exp_f32 v73, v73\n"
    "v_exp_f32 v74, v74\n"
    "v_cvt_pk_bf16_f32 v4, v72, v73\n"
    "v_exp_f32 v75, v75\n"
    "s_waitcnt lgkmcnt(4)\n"
    "v_mfma_f32_32x32x16_bf16 v[32:47], v[140:143], v[0:3], v[32:47]\n"
    "s_add_u32 m0, s62, 0x1bd80\n"
    "s_add_u32 s63, s63, 0x24000\n"
    "ds_read_b128 v[128:131], v228 offset:12672\n"
    "ds_read_b64 v[136:137], v229 offset:20864\n"
    "v_exp_f32 v76, v76\n"
    "v_cvt_pk_bf16_f32 v5, v74, v75\n"
    "v_exp_f32 v77, v77\n"
    "v_exp_f32 v78, v78\n"
    "v_cvt_pk_bf16_f32 v6, v76, v77\n"
    "v_exp_f32 v79, v79\n"
    "v_mfma_f32_32x32x16_bf16 v[96:111], v[132:135], v[212:215], v[96:111]\n"
    "buffer_load_dwordx4 v236, s[32:35], s63 offen offset:0 lds\n"
    "v_exp_f32 v80, v80\n"
    "v_cvt_pk_bf16_f32 v7, v78, v79\n"
    "v_exp_f32 v81, v81\n"
    "v_exp_f32 v82, v82\n"
    "v_cvt_pk_bf16_f32 v8, v80, v81\n"
    "v_exp_f32 v83, v83\n"
    "v_mfma_f32_32x32x16_bf16 v[32:47], v[144:147], v[4:7], v[32:47]\n"
    "s_add_u32 s72, s72, 0x1000\n"
    "v_exp_f32 v84, v84\n"
    "v_cvt_pk_bf16_f32 v9, v82, v83\n"
    "v_exp_f32 v85, v85\n"
    "v_exp_f32 v86, v86\n"
    "v_cvt_pk_bf16_f32 v10, v84, v85\n"
    "v_exp_f32 v87, v87\n"
    "v_mfma_f32_32x32x16_bf16 v[112:127], v[132:135], v[216:219], v[112:127]\n"
    "buffer_load_dwordx4 a[224:227], v231, s[24:27], s72 offen offset:0 sc1\n"
    "v_exp_f32 v88, v88\n"
    "v_cvt_pk_bf16_f32 v11, v86, v87\n"
    "v_exp_f32 v89, v89\n"
    "v_exp_f32 v90, v90\n"
    "v_cvt_pk_bf16_f32 v12, v88, v89\n"
    "v_exp_f32 v91, v91\n"
    "v_mfma_f32_32x32x16_bf16 v[48:63], v[140:143], v[8:11], v[48:63]\n"
    "v_exp_f32 v92, v92\n"
    "v_cvt_pk_bf16_f32 v13, v90, v91\n"
    "v_exp_f32 v93, v93\n"
    "v_exp_f32 v94, v94\n"
    "v_cvt_pk_bf16_f32 v14, v92, v93\n"
    "v_exp_f32 v95, v95\n"
    "v_exp_f32 v96, v96\n"
    "v_cvt_pk_bf16_f32 v15, v94, v95\n"
    "v_exp_f32 v97, v97\n"
    "v_exp_f32 v98, v98\n"
    "v_cvt_pk_bf16_f32 v0, v96, v97\n"
    "v_mfma_f32_32x32x16_bf16 v[48:63], v[144:147], v[12:15], v[48:63]\n"
    "ds_read_b64_tr_b16 v[140:141], v230 offset:16768\n"
    "ds_read_b64_tr_b16 v[142:143], v230 offset:17152\n"
    "v_exp_f32 v99, v99\n"
    "v_exp_f32 v100, v100\n"
    "v_cvt_pk_bf16_f32 v1, v98, v99\n"
    "v_exp_f32 v101, v101\n"
    "s_waitcnt lgkmcnt(2)\n"
    "v_mfma_f32_32x32x4_2b_bf16 v[64:95], v[136:137], v[220:221], 0 cbsz:1 abid:0\n"
    "ds_read_b64_tr_b16 v[144:145], v230 offset:17536\n"
    "ds_read_b64_tr_b16 v[146:147], v230 offset:17920\n"
    "v_exp_f32 v102, v102\n"
    "v_cvt_pk_bf16_f32 v2, v100, v101\n"
    "v_exp_f32 v103, v103\n"
    "v_exp_f32 v104, v104\n"
    "v_cvt_pk_bf16_f32 v3, v102, v103\n"
    "v_exp_f32 v105, v105\n"
    "v_exp_f32 v106, v106\n"
    "v_cvt_pk_bf16_f32 v4, v104, v105\n"
    "v_exp_f32 v107, v107\n"
    "v_mfma_f32_32x32x16_bf16 v[32:47], v[184:187], v[0:3], v[32:47]\n"
    "ds_read_b128 v[132:135], v228 offset:13696\n"
    "v_exp_f32 v108, v108\n"
    "v_cvt_pk_bf16_f32 v5, v106, v107\n"
    "v_exp_f32 v109, v109\n"
    "v_exp_f32 v110, v110\n"
    "v_cvt_pk_bf16_f32 v6, v108, v109\n"
    "v_exp_f32 v111, v111\n"
    "v_mfma_f32_32x32x16_bf16 v[64:79], v[128:131], v[212:215], v[64:79]\n"
    "buffer_load_dwordx4 v237, s[32:35], s63 offen offset:1024 lds\n"
    "v_exp_f32 v112, v112\n"
    "v_cvt_pk_bf16_f32 v7, v110, v111\n"
    "v_exp_f32 v113, v113\n"
    "v_exp_f32 v114, v114\n"
    "v_cvt_pk_bf16_f32 v8, v112, v113\n"
    "v_exp_f32 v115, v115\n"
    "v_mfma_f32_32x32x16_bf16 v[32:47], v[188:191], v[4:7], v[32:47]\n"
    "v_exp_f32 v116, v116\n"
    "v_cvt_pk_bf16_f32 v9, v114, v115\n"
    "v_exp_f32 v117, v117\n"
    "v_exp_f32 v118, v118\n"
    "v_cvt_pk_bf16_f32 v10, v116, v117\n"
    "v_exp_f32 v119, v119\n"
    "v_mfma_f32_32x32x16_bf16 v[80:95], v[128:131], v[216:219], v[80:95]\n"
    "buffer_load_dwordx4 a[228:231], v232, s[24:27], s72 offen offset:0 sc1\n"
    "v_exp_f32 v120, v120\n"
    "v_cvt_pk_bf16_f32 v11, v118, v119\n"
    "v_exp_f32 v121, v121\n"
    "v_exp_f32 v122, v122\n"
    "v_cvt_pk_bf16_f32 v12, v120, v121\n"
    "v_exp_f32 v123, v123\n"
    "v_mfma_f32_32x32x16_bf16 v[48:63], v[184:187], v[8:11], v[48:63]\n"
    "buffer_load_dwordx4 a[232:235], v231, s[24:27], s72 offen offset:1024 sc1\n"
    "v_exp_f32 v124, v124\n"
    "v_cvt_pk_bf16_f32 v13, v122, v123\n"
    "v_exp_f32 v125, v125\n"
    "v_exp_f32 v126, v126\n"
    "v_cvt_pk_bf16_f32 v14, v124, v125\n"
    "v_exp_f32 v127, v127\n"
    "v_exp_f32 v64, v64\n"
    "v_cvt_pk_bf16_f32 v15, v126, v127\n"
    "v_exp_f32 v65, v65\n"
    "v_exp_f32 v66, v66\n"
    "v_cvt_pk_bf16_f32 v0, v64, v65\n"
    "v_mfma_f32_32x32x16_bf16 v[48:63], v[188:191], v[12:15], v[48:63]\n"
    "ds_read_b64_tr_b16 v[184:185], v230 offset:18304\n"
    "ds_read_b64_tr_b16 v[186:187], v230 offset:18688\n"
    "v_exp_f32 v67, v67\n"
    "v_exp_f32 v68, v68\n"
    "v_cvt_pk_bf16_f32 v1, v66, v67\n"
    "v_exp_f32 v69, v69\n"
    "v_mfma_f32_32x32x4_2b_bf16 v[96:127], v[136:137], v[220:221], 0 cbsz:1 abid:1\n"
    "ds_read_b64_tr_b16 v[188:189], v230 offset:19072\n"
    "ds_read_b64_tr_b16 v[190:191], v230 offset:19456\n"
    "v_exp_f32 v70, v70\n"
    "v_cvt_pk_bf16_f32 v2, v68, v69\n"
    "v_exp_f32 v71, v71\n"
    "v_exp_f32 v72, v72\n"
    "v_cvt_pk_bf16_f32 v3, v70, v71\n"
    "v_exp_f32 v73, v73\n"
    "v_exp_f32 v74, v74\n"
    "v_cvt_pk_bf16_f32 v4, v72, v73\n"
    "v_exp_f32 v75, v75\n"
    "s_waitcnt lgkmcnt(4)\n"
    "v_mfma_f32_32x32x16_bf16 v[32:47], v[140:143], v[0:3], v[32:47]\n"
    "ds_read_b128 v[128:131], v228 offset:14720\n"
    "ds_read_b64 v[138:139], v229 offset:21376\n"
    "v_exp_f32 v76, v76\n"
    "v_cvt_pk_bf16_f32 v5, v74, v75\n"
    "v_exp_f32 v77, v77\n"
    "v_exp_f32 v78, v78\n"
    "v_cvt_pk_bf16_f32 v6, v76, v77\n"
    "v_exp_f32 v79, v79\n"
    "v_mfma_f32_32x32x16_bf16 v[96:111], v[132:135], v[212:215], v[96:111]\n"
    "buffer_load_dwordx4 v238, s[32:35], s63 offen offset:2048 lds\n"
    "v_exp_f32 v80, v80\n"
    "v_cvt_pk_bf16_f32 v7, v78, v79\n"
    "v_exp_f32 v81, v81\n"
    "v_exp_f32 v82, v82\n"
    "v_cvt_pk_bf16_f32 v8, v80, v81\n"
    "v_exp_f32 v83, v83\n"
    "v_mfma_f32_32x32x16_bf16 v[32:47], v[144:147], v[4:7], v[32:47]\n"
    "v_exp_f32 v84, v84\n"
    "v_cvt_pk_bf16_f32 v9, v82, v83\n"
    "v_exp_f32 v85, v85\n"
    "v_exp_f32 v86, v86\n"
    "v_cvt_pk_bf16_f32 v10, v84, v85\n"
    "v_exp_f32 v87, v87\n"
    "v_mfma_f32_32x32x16_bf16 v[112:127], v[132:135], v[216:219], v[112:127]\n"
    "buffer_load_dwordx4 a[236:239], v232, s[24:27], s72 offen offset:1024 sc1\n"
    "v_exp_f32 v88, v88\n"
    "v_cvt_pk_bf16_f32 v11, v86, v87\n"
    "v_exp_f32 v89, v89\n"
    "v_exp_f32 v90, v90\n"
    "v_cvt_pk_bf16_f32 v12, v88, v89\n"
    "v_exp_f32 v91, v91\n"
    "v_mfma_f32_32x32x16_bf16 v[48:63], v[140:143], v[8:11], v[48:63]\n"
    "v_exp_f32 v92, v92\n"
    "v_cvt_pk_bf16_f32 v13, v90, v91\n"
    "v_exp_f32 v93, v93\n"
    "v_exp_f32 v94, v94\n"
    "v_cvt_pk_bf16_f32 v14, v92, v93\n"
    "v_exp_f32 v95, v95\n"
    "v_exp_f32 v96, v96\n"
    "v_cvt_pk_bf16_f32 v15, v94, v95\n"
    "v_exp_f32 v97, v97\n"
    "v_exp_f32 v98, v98\n"
    "v_cvt_pk_bf16_f32 v0, v96, v97\n"
    "v_mfma_f32_32x32x16_bf16 v[48:63], v[144:147], v[12:15], v[48:63]\n"
    "ds_read_b64_tr_b16 v[140:141], v230 offset:19840\n"
    "ds_read_b64_tr_b16 v[142:143], v230 offset:20224\n"
    "v_exp_f32 v99, v99\n"
    "v_exp_f32 v100, v100\n"
    "v_cvt_pk_bf16_f32 v1, v98, v99\n"
    "v_exp_f32 v101, v101\n"
    "s_waitcnt lgkmcnt(2)\n"
    "v_mfma_f32_32x32x4_2b_bf16 v[64:95], v[138:139], v[220:221], 0 cbsz:1 abid:0\n"
    "ds_read_b64_tr_b16 v[144:145], v230 offset:20608\n"
    "ds_read_b64_tr_b16 v[146:147], v230 offset:20992\n"
    "v_exp_f32 v102, v102\n"
    "v_cvt_pk_bf16_f32 v2, v100, v101\n"
    "v_exp_f32 v103, v103\n"
    "v_exp_f32 v104, v104\n"
    "v_cvt_pk_bf16_f32 v3, v102, v103\n"
    "v_exp_f32 v105, v105\n"
    "v_exp_f32 v106, v106\n"
    "v_cvt_pk_bf16_f32 v4, v104, v105\n"
    "v_exp_f32 v107, v107\n"
    "v_mfma_f32_32x32x16_bf16 v[32:47], v[184:187], v[0:3], v[32:47]\n"
    "ds_read_b128 v[132:135], v228 offset:15744\n"
    "v_exp_f32 v108, v108\n"
    "v_cvt_pk_bf16_f32 v5, v106, v107\n"
    "v_exp_f32 v109, v109\n"
    "v_exp_f32 v110, v110\n"
    "v_cvt_pk_bf16_f32 v6, v108, v109\n"
    "v_exp_f32 v111, v111\n"
    "v_mfma_f32_32x32x16_bf16 v[64:79], v[128:131], v[212:215], v[64:79]\n"
    "buffer_load_dwordx4 v239, s[32:35], s63 offen offset:3072 lds\n"
    "v_exp_f32 v112, v112\n"
    "v_cvt_pk_bf16_f32 v7, v110, v111\n"
    "v_exp_f32 v113, v113\n"
    "v_exp_f32 v114, v114\n"
    "v_cvt_pk_bf16_f32 v8, v112, v113\n"
    "v_exp_f32 v115, v115\n"
    "v_mfma_f32_32x32x16_bf16 v[32:47], v[188:191], v[4:7], v[32:47]\n"
    "v_exp_f32 v116, v116\n"
    "v_cvt_pk_bf16_f32 v9, v114, v115\n"
    "v_exp_f32 v117, v117\n"
    "v_exp_f32 v118, v118\n"
    "v_cvt_pk_bf16_f32 v10, v116, v117\n"
    "v_exp_f32 v119, v119\n"
    "v_mfma_f32_32x32x16_bf16 v[80:95], v[128:131], v[216:219], v[80:95]\n"
    "buffer_load_dwordx4 a[240:243], v231, s[24:27], s72 offen offset:2048 sc1\n"
    "v_exp_f32 v120, v120\n"
    "v_cvt_pk_bf16_f32 v11, v118, v119\n"
    "v_exp_f32 v121, v121\n"
    "v_exp_f32 v122, v122\n"
    "v_cvt_pk_bf16_f32 v12, v120, v121\n"
    "v_exp_f32 v123, v123\n"
    "v_mfma_f32_32x32x16_bf16 v[48:63], v[184:187], v[8:11], v[48:63]\n"
    "buffer_load_dwordx4 a[244:247], v232, s[24:27], s72 offen offset:2048 sc1\n"
    "v_exp_f32 v124, v124\n"
    "v_cvt_pk_bf16_f32 v13, v122, v123\n"
    "v_exp_f32 v125, v125\n"
    "v_exp_f32 v126, v126\n"
    "v_cvt_pk_bf16_f32 v14, v124, v125\n"
    "v_exp_f32 v127, v127\n"
    "v_exp_f32 v64, v64\n"
    "v_cvt_pk_bf16_f32 v15, v126, v127\n"
    "v_exp_f32 v65, v65\n"
    "v_exp_f32 v66, v66\n"
    "v_cvt_pk_bf16_f32 v0, v64, v65\n"
    "v_mfma_f32_32x32x16_bf16 v[48:63], v[188:191], v[12:15], v[48:63]\n"
    "ds_read_b64_tr_b16 v[184:185], v230 offset:21376\n"
    "ds_read_b64_tr_b16 v[186:187], v230 offset:21760\n"
    "v_exp_f32 v67, v67\n"
    "v_exp_f32 v68, v68\n"
    "v_cvt_pk_bf16_f32 v1, v66, v67\n"
    "v_exp_f32 v69, v69\n"
    "v_mfma_f32_32x32x4_2b_bf16 v[96:127], v[138:139], v[220:221], 0 cbsz:1 abid:1\n"
    "ds_read_b64_tr_b16 v[188:189], v230 offset:22144\n"
    "ds_read_b64_tr_b16 v[190:191], v230 offset:22528\n"
    "v_exp_f32 v70, v70\n"
    "v_cvt_pk_bf16_f32 v2, v68, v69\n"
    "v_exp_f32 v71, v71\n"
    "v_exp_f32 v72, v72\n"
    "v_cvt_pk_bf16_f32 v3, v70, v71\n"
    "v_exp_f32 v73, v73\n"
    "v_exp_f32 v74, v74\n"
    "v_cvt_pk_bf16_f32 v4, v72, v73\n"
    "v_exp_f32 v75, v75\n"
    "s_waitcnt lgkmcnt(4)\n"
    "v_mfma_f32_32x32x16_bf16 v[32:47], v[140:143], v[0:3], v[32:47]\n"
    "s_add_u32 m0, s62, 0x1cd80\n"
    "ds_read_b128 v[128:131], v228 offset:25344\n"
    "ds_read_b64 v[136:137], v229 offset:33536\n"
    "v_exp_f32 v76, v76\n"
    "v_cvt_pk_bf16_f32 v5, v74, v75\n"
    "v_exp_f32 v77, v77\n"
    "v_exp_f32 v78, v78\n"
    "v_cvt_pk_bf16_f32 v6, v76, v77\n"
    "v_exp_f32 v79, v79\n"
    "v_mfma_f32_32x32x16_bf16 v[96:111], v[132:135], v[212:215], v[96:111]\n"
    "buffer_load_dwordx4 v240, s[32:35], s63 offen offset:0 lds\n"
    "v_exp_f32 v80, v80\n"
    "v_cvt_pk_bf16_f32 v7, v78, v79\n"
    "v_exp_f32 v81, v81\n"
    "v_exp_f32 v82, v82\n"
    "v_cvt_pk_bf16_f32 v8, v80, v81\n"
    "v_exp_f32 v83, v83\n"
    "v_mfma_f32_32x32x16_bf16 v[32:47], v[144:147], v[4:7], v[32:47]\n"
    "v_exp_f32 v84, v84\n"
    "v_cvt_pk_bf16_f32 v9, v82, v83\n"
    "v_exp_f32 v85, v85\n"
    "v_exp_f32 v86, v86\n"
    "v_cvt_pk_bf16_f32 v10, v84, v85\n"
    "v_exp_f32 v87, v87\n"
    "v_mfma_f32_32x32x16_bf16 v[112:127], v[132:135], v[216:219], v[112:127]\n"
    "buffer_load_dwordx4 a[248:251], v231, s[24:27], s72 offen offset:3072 sc1\n"
    "v_exp_f32 v88, v88\n"
    "v_cvt_pk_bf16_f32 v11, v86, v87\n"
    "v_exp_f32 v89, v89\n"
    "v_exp_f32 v90, v90\n"
    "v_cvt_pk_bf16_f32 v12, v88, v89\n"
    "v_exp_f32 v91, v91\n"
    "v_mfma_f32_32x32x16_bf16 v[48:63], v[140:143], v[8:11], v[48:63]\n"
    "v_exp_f32 v92, v92\n"
    "v_cvt_pk_bf16_f32 v13, v90, v91\n"
    "v_exp_f32 v93, v93\n"
    "v_exp_f32 v94, v94\n"
    "v_cvt_pk_bf16_f32 v14, v92, v93\n"
    "v_exp_f32 v95, v95\n"
    "v_exp_f32 v96, v96\n"
    "v_cvt_pk_bf16_f32 v15, v94, v95\n"
    "v_exp_f32 v97, v97\n"
    "v_exp_f32 v98, v98\n"
    "v_cvt_pk_bf16_f32 v0, v96, v97\n"
    "v_mfma_f32_32x32x16_bf16 v[48:63], v[144:147], v[12:15], v[48:63]\n"
    "ds_read_b64_tr_b16 v[140:141], v230 offset:29440\n"
    "ds_read_b64_tr_b16 v[142:143], v230 offset:29824\n"
    "v_exp_f32 v99, v99\n"
    "v_exp_f32 v100, v100\n"
    "v_cvt_pk_bf16_f32 v1, v98, v99\n"
    "v_exp_f32 v101, v101\n"
    "s_waitcnt lgkmcnt(2)\n"
    "v_mfma_f32_32x32x4_2b_bf16 v[64:95], v[136:137], v[220:221], 0 cbsz:1 abid:0\n"
    "ds_read_b64_tr_b16 v[144:145], v230 offset:30208\n"
    "ds_read_b64_tr_b16 v[146:147], v230 offset:30592\n"
    "v_exp_f32 v102, v102\n"
    "v_cvt_pk_bf16_f32 v2, v100, v101\n"
    "v_exp_f32 v103, v103\n"
    "v_exp_f32 v104, v104\n"
    "v_cvt_pk_bf16_f32 v3, v102, v103\n"
    "v_exp_f32 v105, v105\n"
    "v_exp_f32 v106, v106\n"
    "v_cvt_pk_bf16_f32 v4, v104, v105\n"
    "v_exp_f32 v107, v107\n"
    "v_mfma_f32_32x32x16_bf16 v[32:47], v[184:187], v[0:3], v[32:47]\n"
    "ds_read_b128 v[132:135], v228 offset:26368\n"
    "v_exp_f32 v108, v108\n"
    "v_cvt_pk_bf16_f32 v5, v106, v107\n"
    "v_exp_f32 v109, v109\n"
    "v_exp_f32 v110, v110\n"
    "v_cvt_pk_bf16_f32 v6, v108, v109\n"
    "v_exp_f32 v111, v111\n"
    "v_mfma_f32_32x32x16_bf16 v[64:79], v[128:131], v[212:215], v[64:79]\n"
    "buffer_load_dwordx4 v241, s[32:35], s63 offen offset:1024 lds\n"
    "v_exp_f32 v112, v112\n"
    "v_cvt_pk_bf16_f32 v7, v110, v111\n"
    "v_exp_f32 v113, v113\n"
    "v_exp_f32 v114, v114\n"
    "v_cvt_pk_bf16_f32 v8, v112, v113\n"
    "v_exp_f32 v115, v115\n"
    "v_mfma_f32_32x32x16_bf16 v[32:47], v[188:191], v[4:7], v[32:47]\n"
    "v_exp_f32 v116, v116\n"
    "v_cvt_pk_bf16_f32 v9, v114, v115\n"
    "v_exp_f32 v117, v117\n"
    "v_exp_f32 v118, v118\n"
    "v_cvt_pk_bf16_f32 v10, v116, v117\n"
    "v_exp_f32 v119, v119\n"
    "v_mfma_f32_32x32x16_bf16 v[80:95], v[128:131], v[216:219], v[80:95]\n"
    "buffer_load_dwordx4 a[252:255], v232, s[24:27], s72 offen offset:3072 sc1\n"
    "s_add_u32 s72, s72, 0x1000\n"
    "v_exp_f32 v120, v120\n"
    "v_cvt_pk_bf16_f32 v11, v118, v119\n"
    "v_exp_f32 v121, v121\n"
    "v_exp_f32 v122, v122\n"
    "v_cvt_pk_bf16_f32 v12, v120, v121\n"
    "v_exp_f32 v123, v123\n"
    "v_mfma_f32_32x32x16_bf16 v[48:63], v[184:187], v[8:11], v[48:63]\n"
    "buffer_load_dwordx4 v[152:155], v231, s[24:27], s72 offen offset:0 sc1\n"
    "v_exp_f32 v124, v124\n"
    "v_cvt_pk_bf16_f32 v13, v122, v123\n"
    "v_exp_f32 v125, v125\n"
    "v_exp_f32 v126, v126\n"
    "v_cvt_pk_bf16_f32 v14, v124, v125\n"
    "v_exp_f32 v127, v127\n"
    "v_exp_f32 v64, v64\n"
    "v_cvt_pk_bf16_f32 v15, v126, v127\n"
    "v_exp_f32 v65, v65\n"
    "v_exp_f32 v66, v66\n"
    "v_cvt_pk_bf16_f32 v0, v64, v65\n"
    "v_mfma_f32_32x32x16_bf16 v[48:63], v[188:191], v[12:15], v[48:63]\n"
    "ds_read_b64_tr_b16 v[184:185], v230 offset:30976\n"
    "ds_read_b64_tr_b16 v[186:187], v230 offset:31360\n"
    "v_exp_f32 v67, v67\n"
    "v_exp_f32 v68, v68\n"
    "v_cvt_pk_bf16_f32 v1, v66, v67\n"
    "v_exp_f32 v69, v69\n"
    "v_mfma_f32_32x32x4_2b_bf16 v[96:127], v[136:137], v[220:221], 0 cbsz:1 abid:1\n"
    "ds_read_b64_tr_b16 v[188:189], v230 offset:31744\n"
    "ds_read_b64_tr_b16 v[190:191], v230 offset:32128\n"
    "v_exp_f32 v70, v70\n"
    "v_cvt_pk_bf16_f32 v2, v68, v69\n"
    "v_exp_f32 v71, v71\n"
    "v_exp_f32 v72, v72\n"
    "v_cvt_pk_bf16_f32 v3, v70, v71\n"
    "v_exp_f32 v73, v73\n"
    "v_exp_f32 v74, v74\n"
    "v_cvt_pk_bf16_f32 v4, v72, v73\n"
    "v_exp_f32 v75, v75\n"
    "s_waitcnt lgkmcnt(4)\n"
    "v_mfma_f32_32x32x16_bf16 v[32:47], v[140:143], v[0:3], v[32:47]\n"
    "ds_read_b128 v[128:131], v228 offset:27392\n"
    "ds_read_b64 v[138:139], v229 offset:34048\n"
    "v_exp_f32 v76, v76\n"
    "v_cvt_pk_bf16_f32 v5, v74, v75\n"
    "v_exp_f32 v77, v77\n"
    "v_exp_f32 v78, v78\n"
    "v_cvt_pk_bf16_f32 v6, v76, v77\n"
    "v_exp_f32 v79, v79\n"
    "v_mfma_f32_32x32x16_bf16 v[96:111], v[132:135], v[212:215], v[96:111]\n"
    "buffer_load_dwordx4 v242, s[32:35], s63 offen offset:2048 lds\n"
    "v_exp_f32 v80, v80\n"
    "v_cvt_pk_bf16_f32 v7, v78, v79\n"
    "v_exp_f32 v81, v81\n"
    "v_exp_f32 v82, v82\n"
    "v_cvt_pk_bf16_f32 v8, v80, v81\n"
    "v_exp_f32 v83, v83\n"
    "v_mfma_f32_32x32x16_bf16 v[32:47], v[144:147], v[4:7], v[32:47]\n"
    "v_exp_f32 v84, v84\n"
    "v_cvt_pk_bf16_f32 v9, v82, v83\n"
    "v_exp_f32 v85, v85\n"
    "v_exp_f32 v86, v86\n"
    "v_cvt_pk_bf16_f32 v10, v84, v85\n"
    "v_exp_f32 v87, v87\n"
    "v_mfma_f32_32x32x16_bf16 v[112:127], v[132:135], v[216:219], v[112:127]\n"
    "buffer_load_dwordx4 v243, s[32:35], s63 offen offset:3072 lds\n"
    "v_exp_f32 v88, v88\n"
    "v_cvt_pk_bf16_f32 v11, v86, v87\n"
    "v_exp_f32 v89, v89\n"
    "v_exp_f32 v90, v90\n"
    "v_cvt_pk_bf16_f32 v12, v88, v89\n"
    "v_exp_f32 v91, v91\n"
    "v_mfma_f32_32x32x16_bf16 v[48:63], v[140:143], v[8:11], v[48:63]\n"
    "buffer_load_dwordx4 v[156:159], v232, s[24:27], s72 offen offset:0 sc1\n"
    "v_exp_f32 v92, v92\n"
    "v_cvt_pk_bf16_f32 v13, v90, v91\n"
    "v_exp_f32 v93, v93\n"
    "v_exp_f32 v94, v94\n"
    "v_cvt_pk_bf16_f32 v14, v92, v93\n"
    "v_exp_f32 v95, v95\n"
    "v_exp_f32 v96, v96\n"
    "v_cvt_pk_bf16_f32 v15, v94, v95\n"
    "v_exp_f32 v97, v97\n"
    "v_exp_f32 v98, v98\n"
    "v_cvt_pk_bf16_f32 v0, v96, v97\n"
    "v_mfma_f32_32x32x16_bf16 v[48:63], v[144:147], v[12:15], v[48:63]\n"
    "ds_read_b64_tr_b16 v[140:141], v230 offset:32512\n"
    "ds_read_b64_tr_b16 v[142:143], v230 offset:32896\n"
    "v_exp_f32 v99, v99\n"
    "v_exp_f32 v100, v100\n"
    "v_cvt_pk_bf16_f32 v1, v98, v99\n"
    "v_exp_f32 v101, v101\n"
    "s_waitcnt lgkmcnt(2)\n"
    "v_mfma_f32_32x32x4_2b_bf16 v[64:95], v[138:139], v[220:221], 0 cbsz:1 abid:0\n"
    "ds_read_b64_tr_b16 v[144:145], v230 offset:33280\n"
    "ds_read_b64_tr_b16 v[146:147], v230 offset:33664\n"
    "v_exp_f32 v102, v102\n"
    "v_cvt_pk_bf16_f32 v2, v100, v101\n"
    "v_exp_f32 v103, v103\n"
    "v_exp_f32 v104, v104\n"
    "v_cvt_pk_bf16_f32 v3, v102, v103\n"
    "v_exp_f32 v105, v105\n"
    "v_exp_f32 v106, v106\n"
    "v_cvt_pk_bf16_f32 v4, v104, v105\n"
    "v_exp_f32 v107, v107\n"
    "v_mfma_f32_32x32x16_bf16 v[32:47], v[184:187], v[0:3], v[32:47]\n"
    "s_add_u32 m0, s62, 0x1dd80\n"
    "ds_read_b128 v[132:135], v228 offset:28416\n"
    "v_exp_f32 v108, v108\n"
    "v_cvt_pk_bf16_f32 v5, v106, v107\n"
    "v_exp_f32 v109, v109\n"
    "v_exp_f32 v110, v110\n"
    "v_cvt_pk_bf16_f32 v6, v108, v109\n"
    "v_exp_f32 v111, v111\n"
    "v_mfma_f32_32x32x16_bf16 v[64:79], v[128:131], v[212:215], v[64:79]\n"
    "buffer_load_dwordx4 v244, s[32:35], s63 offen offset:0 lds\n"
    "v_exp_f32 v112, v112\n"
    "v_cvt_pk_bf16_f32 v7, v110, v111\n"
    "v_exp_f32 v113, v113\n"
    "v_exp_f32 v114, v114\n"
    "v_cvt_pk_bf16_f32 v8, v112, v113\n"
    "v_exp_f32 v115, v115\n"
    "v_mfma_f32_32x32x16_bf16 v[32:47], v[188:191], v[4:7], v[32:47]\n"
    "v_exp_f32 v116, v116\n"
    "v_cvt_pk_bf16_f32 v9, v114, v115\n"
    "v_exp_f32 v117, v117\n"
    "v_exp_f32 v118, v118\n"
    "v_cvt_pk_bf16_f32 v10, v116, v117\n"
    "v_exp_f32 v119, v119\n"
    "v_mfma_f32_32x32x16_bf16 v[80:95], v[128:131], v[216:219], v[80:95]\n"
    "buffer_load_dwordx4 v[160:163], v231, s[24:27], s72 offen offset:1024 sc1\n"
    "v_exp_f32 v120, v120\n"
    "v_cvt_pk_bf16_f32 v11, v118, v119\n"
    "v_exp_f32 v121, v121\n"
    "v_exp_f32 v122, v122\n"
    "v_cvt_pk_bf16_f32 v12, v120, v121\n"
    "v_exp_f32 v123, v123\n"
    "v_mfma_f32_32x32x16_bf16 v[48:63], v[184:187], v[8:11], v[48:63]\n"
    "buffer_load_dwordx4 v[164:167], v232, s[24:27], s72 offen offset:1024 sc1\n"
    "v_exp_f32 v124, v124\n"
    "v_cvt_pk_bf16_f32 v13, v122, v123\n"
    "v_exp_f32 v125, v125\n"
    "v_exp_f32 v126, v126\n"
    "v_cvt_pk_bf16_f32 v14, v124, v125\n"
    "v_exp_f32 v127, v127\n"
    "v_exp_f32 v64, v64\n"
    "v_cvt_pk_bf16_f32 v15, v126, v127\n"
    "v_exp_f32 v65, v65\n"
    "v_exp_f32 v66, v66\n"
    "v_cvt_pk_bf16_f32 v0, v64, v65\n"
    "v_mfma_f32_32x32x16_bf16 v[48:63], v[188:191], v[12:15], v[48:63]\n"
    "ds_read_b64_tr_b16 v[184:185], v230 offset:34048\n"
    "ds_read_b64_tr_b16 v[186:187], v230 offset:34432\n"
    "v_exp_f32 v67, v67\n"
    "v_exp_f32 v68, v68\n"
    "v_cvt_pk_bf16_f32 v1, v66, v67\n"
    "v_exp_f32 v69, v69\n"
    "v_mfma_f32_32x32x4_2b_bf16 v[96:127], v[138:139], v[220:221], 0 cbsz:1 abid:1\n"
    "ds_read_b64_tr_b16 v[188:189], v230 offset:34816\n"
    "ds_read_b64_tr_b16 v[190:191], v230 offset:35200\n"
    "v_exp_f32 v70, v70\n"
    "v_cvt_pk_bf16_f32 v2, v68, v69\n"
    "v_exp_f32 v71, v71\n"
    "v_exp_f32 v72, v72\n"
    "v_cvt_pk_bf16_f32 v3, v70, v71\n"
    "v_exp_f32 v73, v73\n"
    "v_exp_f32 v74, v74\n"
    "v_cvt_pk_bf16_f32 v4, v72, v73\n"
    "v_exp_f32 v75, v75\n"
    "s_waitcnt lgkmcnt(4)\n"
    "v_mfma_f32_32x32x16_bf16 v[32:47], v[140:143], v[0:3], v[32:47]\n"
    "ds_read_b128 v[128:131], v228 offset:38016\n"
    "ds_read_b64 v[136:137], v229 offset:46208\n"
    "v_exp_f32 v76, v76\n"
    "v_cvt_pk_bf16_f32 v5, v74, v75\n"
    "v_exp_f32 v77, v77\n"
    "v_exp_f32 v78, v78\n"
    "v_cvt_pk_bf16_f32 v6, v76, v77\n"
    "v_exp_f32 v79, v79\n"
    "v_mfma_f32_32x32x16_bf16 v[96:111], v[132:135], v[212:215], v[96:111]\n"
    "buffer_load_dwordx4 v[168:171], v231, s[24:27], s72 offen offset:2048 sc1\n"
    "v_exp_f32 v80, v80\n"
    "v_cvt_pk_bf16_f32 v7, v78, v79\n"
    "v_exp_f32 v81, v81\n"
    "v_exp_f32 v82, v82\n"
    "v_cvt_pk_bf16_f32 v8, v80, v81\n"
    "v_exp_f32 v83, v83\n"
    "v_mfma_f32_32x32x16_bf16 v[32:47], v[144:147], v[4:7], v[32:47]\n"
    "v_exp_f32 v84, v84\n"
    "v_cvt_pk_bf16_f32 v9, v82, v83\n"
    "v_exp_f32 v85, v85\n"
    "v_exp_f32 v86, v86\n"
    "v_cvt_pk_bf16_f32 v10, v84, v85\n"
    "v_exp_f32 v87, v87\n"
    "v_mfma_f32_32x32x16_bf16 v[112:127], v[132:135], v[216:219], v[112:127]\n"
    "v_exp_f32 v88, v88\n"
    "v_cvt_pk_bf16_f32 v11, v86, v87\n"
    "v_exp_f32 v89, v89\n"
    "v_exp_f32 v90, v90\n"
    "v_cvt_pk_bf16_f32 v12, v88, v89\n"
    "v_exp_f32 v91, v91\n"
    "v_mfma_f32_32x32x16_bf16 v[48:63], v[140:143], v[8:11], v[48:63]\n"
    "v_exp_f32 v92, v92\n"
    "v_cvt_pk_bf16_f32 v13, v90, v91\n"
    "v_exp_f32 v93, v93\n"
    "v_exp_f32 v94, v94\n"
    "v_cvt_pk_bf16_f32 v14, v92, v93\n"
    "v_exp_f32 v95, v95\n"
    "v_exp_f32 v96, v96\n"
    "v_cvt_pk_bf16_f32 v15, v94, v95\n"
    "v_exp_f32 v97, v97\n"
    "v_exp_f32 v98, v98\n"
    "v_cvt_pk_bf16_f32 v0, v96, v97\n"
    "v_mfma_f32_32x32x16_bf16 v[48:63], v[144:147], v[12:15], v[48:63]\n"
    "ds_read_b64_tr_b16 v[140:141], v230 offset:42112\n"
    "ds_read_b64_tr_b16 v[142:143], v230 offset:42496\n"
    "v_exp_f32 v99, v99\n"
    "v_exp_f32 v100, v100\n"
    "v_cvt_pk_bf16_f32 v1, v98, v99\n"
    "v_exp_f32 v101, v101\n"
    "s_waitcnt lgkmcnt(2)\n"
    "v_mfma_f32_32x32x4_2b_bf16 v[64:95], v[136:137], v[220:221], 0 cbsz:1 abid:0\n"
    "ds_read_b64_tr_b16 v[144:145], v230 offset:42880\n"
    "ds_read_b64_tr_b16 v[146:147], v230 offset:43264\n"
    "v_exp_f32 v102, v102\n"
    "v_cvt_pk_bf16_f32 v2, v100, v101\n"
    "v_exp_f32 v103, v103\n"
    "v_exp_f32 v104, v104\n"
    "v_cvt_pk_bf16_f32 v3, v102, v103\n"
    "v_exp_f32 v105, v105\n"
    "v_exp_f32 v106, v106\n"
    "v_cvt_pk_bf16_f32 v4, v104, v105\n"
    "v_exp_f32 v107, v107\n"
    "v_mfma_f32_32x32x16_bf16 v[32:47], v[184:187], v[0:3], v[32:47]\n"
    "ds_read_b128 v[132:135], v228 offset:39040\n"
    "v_exp_f32 v108, v108\n"
    "v_cvt_pk_bf16_f32 v5, v106, v107\n"
    "v_exp_f32 v109, v109\n"
    "v_exp_f32 v110, v110\n"
    "v_cvt_pk_bf16_f32 v6, v108, v109\n"
    "v_exp_f32 v111, v111\n"
    "v_mfma_f32_32x32x16_bf16 v[64:79], v[128:131], v[212:215], v[64:79]\n"
    "buffer_load_dwordx4 v[172:175], v232, s[24:27], s72 offen offset:2048 sc1\n"
    "v_exp_f32 v112, v112\n"
    "v_cvt_pk_bf16_f32 v7, v110, v111\n"
    "v_exp_f32 v113, v113\n"
    "v_exp_f32 v114, v114\n"
    "v_cvt_pk_bf16_f32 v8, v112, v113\n"
    "v_exp_f32 v115, v115\n"
    "v_mfma_f32_32x32x16_bf16 v[32:47], v[188:191], v[4:7], v[32:47]\n"
    "v_exp_f32 v116, v116\n"
    "v_cvt_pk_bf16_f32 v9, v114, v115\n"
    "v_exp_f32 v117, v117\n"
    "v_exp_f32 v118, v118\n"
    "v_cvt_pk_bf16_f32 v10, v116, v117\n"
    "v_exp_f32 v119, v119\n"
    "v_mfma_f32_32x32x16_bf16 v[80:95], v[128:131], v[216:219], v[80:95]\n"
    "buffer_load_dwordx4 v[176:179], v231, s[24:27], s72 offen offset:3072 sc1\n"
    "v_exp_f32 v120, v120\n"
    "v_cvt_pk_bf16_f32 v11, v118, v119\n"
    "v_exp_f32 v121, v121\n"
    "v_exp_f32 v122, v122\n"
    "v_cvt_pk_bf16_f32 v12, v120, v121\n"
    "v_exp_f32 v123, v123\n"
    "v_mfma_f32_32x32x16_bf16 v[48:63], v[184:187], v[8:11], v[48:63]\n"
    "v_exp_f32 v124, v124\n"
    "v_cvt_pk_bf16_f32 v13, v122, v123\n"
    "v_exp_f32 v125, v125\n"
    "v_exp_f32 v126, v126\n"
    "v_cvt_pk_bf16_f32 v14, v124, v125\n"
    "v_exp_f32 v127, v127\n"
    "v_exp_f32 v64, v64\n"
    "v_cvt_pk_bf16_f32 v15, v126, v127\n"
    "v_exp_f32 v65, v65\n"
    "v_exp_f32 v66, v66\n"
    "v_cvt_pk_bf16_f32 v0, v64, v65\n"
    "v_mfma_f32_32x32x16_bf16 v[48:63], v[188:191], v[12:15], v[48:63]\n"
    "ds_read_b64_tr_b16 v[184:185], v230 offset:43648\n"
    "ds_read_b64_tr_b16 v[186:187], v230 offset:44032\n"
    "v_exp_f32 v67, v67\n"
    "v_exp_f32 v68, v68\n"
    "v_cvt_pk_bf16_f32 v1, v66, v67\n"
    "v_exp_f32 v69, v69\n"
    "v_mfma_f32_32x32x4_2b_bf16 v[96:127], v[136:137], v[220:221], 0 cbsz:1 abid:1\n"
    "ds_read_b64_tr_b16 v[188:189], v230 offset:44416\n"
    "ds_read_b64_tr_b16 v[190:191], v230 offset:44800\n"
    "v_exp_f32 v70, v70\n"
    "v_cvt_pk_bf16_f32 v2, v68, v69\n"
    "v_exp_f32 v71, v71\n"
    "v_exp_f32 v72, v72\n"
    "v_cvt_pk_bf16_f32 v3, v70, v71\n"
    "v_exp_f32 v73, v73\n"
    "v_exp_f32 v74, v74\n"
    "v_cvt_pk_bf16_f32 v4, v72, v73\n"
    "v_exp_f32 v75, v75\n"
    "s_waitcnt lgkmcnt(4)\n"
    "v_mfma_f32_32x32x16_bf16 v[32:47], v[140:143], v[0:3], v[32:47]\n"
    "ds_read_b128 v[128:131], v228 offset:40064\n"
    "ds_read_b64 v[138:139], v229 offset:46720\n"
    "v_exp_f32 v76, v76\n"
    "v_cvt_pk_bf16_f32 v5, v74, v75\n"
    "v_exp_f32 v77, v77\n"
    "v_exp_f32 v78, v78\n"
    "v_cvt_pk_bf16_f32 v6, v76, v77\n"
    "v_exp_f32 v79, v79\n"
    "v_mfma_f32_32x32x16_bf16 v[96:111], v[132:135], v[212:215], v[96:111]\n"
    "buffer_load_dwordx4 v[180:183], v232, s[24:27], s72 offen offset:3072 sc1\n"
    "v_exp_f32 v80, v80\n"
    "v_cvt_pk_bf16_f32 v7, v78, v79\n"
    "v_exp_f32 v81, v81\n"
    "v_exp_f32 v82, v82\n"
    "v_cvt_pk_bf16_f32 v8, v80, v81\n"
    "v_exp_f32 v83, v83\n"
    "v_mfma_f32_32x32x16_bf16 v[32:47], v[144:147], v[4:7], v[32:47]\n"
    "v_exp_f32 v84, v84\n"
    "v_cvt_pk_bf16_f32 v9, v82, v83\n"
    "v_exp_f32 v85, v85\n"
    "v_exp_f32 v86, v86\n"
    "v_cvt_pk_bf16_f32 v10, v84, v85\n"
    "v_exp_f32 v87, v87\n"
    "v_mfma_f32_32x32x16_bf16 v[112:127], v[132:135], v[216:219], v[112:127]\n"
    "v_exp_f32 v88, v88\n"
    "v_cvt_pk_bf16_f32 v11, v86, v87\n"
    "v_exp_f32 v89, v89\n"
    "v_exp_f32 v90, v90\n"
    "v_cvt_pk_bf16_f32 v12, v88, v89\n"
    "v_exp_f32 v91, v91\n"
    "v_mfma_f32_32x32x16_bf16 v[48:63], v[140:143], v[8:11], v[48:63]\n"
    "v_exp_f32 v92, v92\n"
    "v_cvt_pk_bf16_f32 v13, v90, v91\n"
    "v_exp_f32 v93, v93\n"
    "v_exp_f32 v94, v94\n"
    "v_cvt_pk_bf16_f32 v14, v92, v93\n"
    "v_exp_f32 v95, v95\n"
    "v_exp_f32 v96, v96\n"
    "v_cvt_pk_bf16_f32 v15, v94, v95\n"
    "v_exp_f32 v97, v97\n"
    "v_exp_f32 v98, v98\n"
    "v_cvt_pk_bf16_f32 v0, v96, v97\n"
    "v_mfma_f32_32x32x16_bf16 v[48:63], v[144:147], v[12:15], v[48:63]\n"
    "ds_read_b64_tr_b16 v[140:141], v230 offset:45184\n"
    "ds_read_b64_tr_b16 v[142:143], v230 offset:45568\n"
    "v_exp_f32 v99, v99\n"
    "v_exp_f32 v100, v100\n"
    "v_cvt_pk_bf16_f32 v1, v98, v99\n"
    "v_exp_f32 v101, v101\n"
    "s_waitcnt lgkmcnt(2)\n"
    "v_mfma_f32_32x32x4_2b_bf16 v[64:95], v[138:139], v[220:221], 0 cbsz:1 abid:0\n"
    "ds_read_b64_tr_b16 v[144:145], v230 offset:45952\n"
    "ds_read_b64_tr_b16 v[146:147], v230 offset:46336\n"
    "v_exp_f32 v102, v102\n"
    "v_cvt_pk_bf16_f32 v2, v100, v101\n"
    "v_exp_f32 v103, v103\n"
    "v_exp_f32 v104, v104\n"
    "v_cvt_pk_bf16_f32 v3, v102, v103\n"
    "v_exp_f32 v105, v105\n"
    "v_exp_f32 v106, v106\n"
    "v_cvt_pk_bf16_f32 v4, v104, v105\n"
    "v_exp_f32 v107, v107\n"
    "v_mfma_f32_32x32x16_bf16 v[32:47], v[184:187], v[0:3], v[32:47]\n"
    "ds_read_b128 v[132:135], v228 offset:41088\n"
    "v_exp_f32 v108, v108\n"
    "v_cvt_pk_bf16_f32 v5, v106, v107\n"
    "v_exp_f32 v109, v109\n"
    "v_exp_f32 v110, v110\n"
    "v_cvt_pk_bf16_f32 v6, v108, v109\n"
    "v_exp_f32 v111, v111\n"
    "v_mfma_f32_32x32x16_bf16 v[64:79], v[128:131], v[212:215], v[64:79]\n"
    "v_exp_f32 v112, v112\n"
    "v_cvt_pk_bf16_f32 v7, v110, v111\n"
    "v_exp_f32 v113, v113\n"
    "v_exp_f32 v114, v114\n"
    "v_cvt_pk_bf16_f32 v8, v112, v113\n"
    "v_exp_f32 v115, v115\n"
    "v_mfma_f32_32x32x16_bf16 v[32:47], v[188:191], v[4:7], v[32:47]\n"
    "v_exp_f32 v116, v116\n"
    "v_cvt_pk_bf16_f32 v9, v114, v115\n"
    "v_exp_f32 v117, v117\n"
    "v_exp_f32 v118, v118\n"
    "v_cvt_pk_bf16_f32 v10, v116, v117\n"
    "v_exp_f32 v119, v119\n"
    "v_mfma_f32_32x32x16_bf16 v[80:95], v[128:131], v[216:219], v[80:95]\n"
    "v_exp_f32 v120, v120\n"
    "v_cvt_pk_bf16_f32 v11, v118, v119\n"
    "v_exp_f32 v121, v121\n"
    "v_exp_f32 v122, v122\n"
    "v_cvt_pk_bf16_f32 v12, v120, v121\n"
    "v_exp_f32 v123, v123\n"
    "v_mfma_f32_32x32x16_bf16 v[48:63], v[184:187], v[8:11], v[48:63]\n"
    "v_exp_f32 v124, v124\n"
    "v_cvt_pk_bf16_f32 v13, v122, v123\n"
    "v_exp_f32 v125, v125\n"
    "v_exp_f32 v126, v126\n"
    "v_cvt_pk_bf16_f32 v14, v124, v125\n"
    "v_exp_f32 v127, v127\n"
    "v_exp_f32 v64, v64\n"
    "v_cvt_pk_bf16_f32 v15, v126, v127\n"
    "v_exp_f32 v65, v65\n"
    "v_exp_f32 v66, v66\n"
    "v_cvt_pk_bf16_f32 v0, v64, v65\n"
    "v_mfma_f32_32x32x16_bf16 v[48:63], v[188:191], v[12:15], v[48:63]\n"
    "ds_read_b64_tr_b16 v[184:185], v230 offset:46720\n"
    "ds_read_b64_tr_b16 v[186:187], v230 offset:47104\n"
    "v_exp_f32 v67, v67\n"
    "v_exp_f32 v68, v68\n"
    "v_cvt_pk_bf16_f32 v1, v66, v67\n"
    "v_exp_f32 v69, v69\n"
    "v_mfma_f32_32x32x4_2b_bf16 v[96:127], v[138:139], v[220:221], 0 cbsz:1 abid:1\n"
    "ds_read_b64_tr_b16 v[188:189], v230 offset:47488\n"
    "ds_read_b64_tr_b16 v[190:191], v230 offset:47872\n"
    "v_exp_f32 v70, v70\n"
    "v_cvt_pk_bf16_f32 v2, v68, v69\n"
    "v_exp_f32 v71, v71\n"
    "v_exp_f32 v72, v72\n"
    "v_cvt_pk_bf16_f32 v3, v70, v71\n"
    "v_exp_f32 v73, v73\n"
    "v_exp_f32 v74, v74\n"
    "v_cvt_pk_bf16_f32 v4, v72, v73\n"
    "v_exp_f32 v75, v75\n"
    "s_waitcnt lgkmcnt(4)\n"
    "v_mfma_f32_32x32x16_bf16 v[32:47], v[140:143], v[0:3], v[32:47]\n"
    "v_exp_f32 v76, v76\n"
    "v_cvt_pk_bf16_f32 v5, v74, v75\n"
    "v_exp_f32 v77, v77\n"
    "v_exp_f32 v78, v78\n"
    "v_cvt_pk_bf16_f32 v6, v76, v77\n"
    "v_exp_f32 v79, v79\n"
    "v_mfma_f32_32x32x16_bf16 v[96:111], v[132:135], v[212:215], v[96:111]\n"
    "v_exp_f32 v80, v80\n"
    "v_cvt_pk_bf16_f32 v7, v78, v79\n"
    "v_exp_f32 v81, v81\n"
    "v_exp_f32 v82, v82\n"
    "v_cvt_pk_bf16_f32 v8, v80, v81\n"
    "v_exp_f32 v83, v83\n"
    "v_mfma_f32_32x32x16_bf16 v[32:47], v[144:147], v[4:7], v[32:47]\n"
    "v_exp_f32 v84, v84\n"
    "v_cvt_pk_bf16_f32 v9, v82, v83\n"
    "v_exp_f32 v85, v85\n"
    "v_exp_f32 v86, v86\n"
    "v_cvt_pk_bf16_f32 v10, v84, v85\n"
    "v_exp_f32 v87, v87\n"
    "v_mfma_f32_32x32x16_bf16 v[112:127], v[132:135], v[216:219], v[112:127]\n"
    "v_exp_f32 v88, v88\n"
    "v_cvt_pk_bf16_f32 v11, v86, v87\n"
    "v_exp_f32 v89, v89\n"
    "v_exp_f32 v90, v90\n"
    "v_cvt_pk_bf16_f32 v12, v88, v89\n"
    "v_exp_f32 v91, v91\n"
    "v_mfma_f32_32x32x16_bf16 v[48:63], v[140:143], v[8:11], v[48:63]\n"
    "v_exp_f32 v92, v92\n"
    "v_cvt_pk_bf16_f32 v13, v90, v91\n"
    "v_exp_f32 v93, v93\n"
    "v_exp_f32 v94, v94\n"
    "v_cvt_pk_bf16_f32 v14, v92, v93\n"
    "v_exp_f32 v95, v95\n"
    "v_exp_f32 v96, v96\n"
    "v_cvt_pk_bf16_f32 v15, v94, v95\n"
    "v_exp_f32 v97, v97\n"
    "v_exp_f32 v98, v98\n"
    "v_cvt_pk_bf16_f32 v0, v96, v97\n"
    "v_mfma_f32_32x32x16_bf16 v[48:63], v[144:147], v[12:15], v[48:63]\n"
    "v_exp_f32 v99, v99\n"
    "v_exp_f32 v100, v100\n"
    "v_cvt_pk_bf16_f32 v1, v98, v99\n"
    "v_exp_f32 v101, v101\n"
    "v_exp_f32 v102, v102\n"
    "v_cvt_pk_bf16_f32 v2, v100, v101\n"
    "v_exp_f32 v103, v103\n"
    "v_exp_f32 v104, v104\n"
    "v_cvt_pk_bf16_f32 v3, v102, v103\n"
    "v_exp_f32 v105, v105\n"
    "v_exp_f32 v106, v106\n"
    "v_cvt_pk_bf16_f32 v4, v104, v105\n"
    "v_exp_f32 v107, v107\n"
    "s_waitcnt lgkmcnt(0)\n"
    "v_mfma_f32_32x32x16_bf16 v[32:47], v[184:187], v[0:3], v[32:47]\n"
    "v_exp_f32 v108, v108\n"
    "v_cvt_pk_bf16_f32 v5, v106, v107\n"
    "v_exp_f32 v109, v109\n"
    "v_exp_f32 v110, v110\n"
    "v_cvt_pk_bf16_f32 v6, v108, v109\n"
    "v_exp_f32 v111, v111\n"
    "v_exp_f32 v112, v112\n"
    "v_cvt_pk_bf16_f32 v7, v110, v111\n"
    "v_exp_f32 v113, v113\n"
    "v_exp_f32 v114, v114\n"
    "v_cvt_pk_bf16_f32 v8, v112, v113\n"
    "v_exp_f32 v115, v115\n"
    "v_mfma_f32_32x32x16_bf16 v[32:47], v[188:191], v[4:7], v[32:47]\n"
    "v_exp_f32 v116, v116\n"
    "v_cvt_pk_bf16_f32 v9, v114, v115\n"
    "v_exp_f32 v117, v117\n"
    "v_exp_f32 v118, v118\n"
    "v_cvt_pk_bf16_f32 v10, v116, v117\n"
    "v_exp_f32 v119, v119\n"
    "v_exp_f32 v120, v120\n"
    "v_cvt_pk_bf16_f32 v11, v118, v119\n"
    "v_exp_f32 v121, v121\n"
    "v_exp_f32 v122, v122\n"
    "v_cvt_pk_bf16_f32 v12, v120, v121\n"
    "v_exp_f32 v123, v123\n"
    "v_mfma_f32_32x32x16_bf16 v[48:63], v[184:187], v[8:11], v[48:63]\n"
    "v_exp_f32 v124, v124\n"
    "v_cvt_pk_bf16_f32 v13, v122, v123\n"
    "v_exp_f32 v125, v125\n"
    "v_exp_f32 v126, v126\n"
    "v_cvt_pk_bf16_f32 v14, v124, v125\n"
    "v_exp_f32 v127, v127\n"
    "s_nop 0\n"
    "v_cvt_pk_bf16_f32 v15, v126, v127\n"
    "s_nop 1\n"
    "v_mfma_f32_32x32x16_bf16 v[48:63], v[188:191], v[12:15], v[48:63]\n"
    "s_mov_b64 s[90:91], -1\n"
    ".Lfbr:\n"
    "s_lshl_b32 s86, s12, 10\n"
    "s_add_u32 s86, s86, 36864\n"
    "v_rcp_f32 v184, v42\n"
    "s_nop 7\n"
    "v_rcp_f32 v185, v58\n"
    "v_mov_b32 v186, 0\n"
    "v_mul_f32 v184, v222, v184\n"
    "v_mul_f32 v0, v32, v184\n"
    "v_mul_f32 v1, v33, v184\n"
    "v_mul_f32 v2, v34, v184\n"
    "v_mul_f32 v3, v35, v184\n"
    "v_mul_f32 v4, v36, v184\n"
    "v_mul_f32 v5, v37, v184\n"
    "v_mul_f32 v6, v38, v184\n"
    "v_mul_f32 v7, v39, v184\n"
    "v_mul_f32 v8, v40, v184\n"
    "v_mul_f32 v9, v41, v184\n"
    "v_cvt_pk_bf16_f32 v20, v0, v1\n"
    "v_cvt_pk_bf16_f32 v21, v2, v3\n"
    "v_cvt_pk_bf16_f32 v22, v4, v5\n"
    "v_cvt_pk_bf16_f32 v23, v6, v7\n"
    "v_cvt_pk_bf16_f32 v24, v8, v9\n"
    "v_mul_f32 v185, v223, v185\n"
    "v_mul_f32 v10, v48, v185\n"
    "v_mul_f32 v11, v49, v185\n"
    "v_mul_f32 v12, v50, v185\n"
    "v_mul_f32 v13, v51, v185\n"
    "v_mul_f32 v14, v52, v185\n"
    "v_mul_f32 v15, v53, v185\n"
    "v_mul_f32 v16, v54, v185\n"
    "v_mul_f32 v17, v55, v185\n"
    "v_mul_f32 v18, v56, v185\n"
    "v_mul_f32 v19, v57, v185\n"
    "v_cvt_pk_bf16_f32 v26, v10, v11\n"
    "v_cvt_pk_bf16_f32 v27, v12, v13\n"
    "v_cvt_pk_bf16_f32 v28, v14, v15\n"
    "v_cvt_pk_bf16_f32 v29, v16, v17\n"
    "v_cvt_pk_bf16_f32 v30, v18, v19\n"
    "v_dot2_f32_bf16 v186, v20, 0, v186\n"
    "v_dot2_f32_bf16 v186, v21, 0, v186\n"
    "v_dot2_f32_bf16 v186, v22, 0, v186\n"
    "v_dot2_f32_bf16 v186, v23, 0, v186\n"
    "v_dot2_f32_bf16 v186, v24, 0, v186\n"
    "v_dot2_f32_bf16 v186, v26, 0, v186\n"
    "v_dot2_f32_bf16 v186, v27, 0, v186\n"
    "v_dot2_f32_bf16 v186, v28, 0, v186\n"
    "v_dot2_f32_bf16 v186, v29, 0, v186\n"
    "v_dot2_f32_bf16 v186, v30, 0, v186\n"
    "v_min_f32 v184, v42, v58\n"
    "v_max_f32 v185, v42, v58\n"
    "v_cmp_nle_f32 vcc, 0x30800000, v184\n"
    "v_mov_b32 v184, 0x6c800000\n"
    "v_cmp_nle_f32 s[54:55], v185, v184\n"
    "s_nop 1\n"
    "s_or_b64 s[54:55], vcc, s[54:55]\n"
    "v_cmp_u_f32 vcc, v186, v186\n"
    "s_nop 1\n"
    "s_or_b64 s[54:55], vcc, s[54:55]\n"
    "s_and_b64 s[54:55], s[54:55], s[90:91]\n"
    "s_cbranch_scc1 .Lfb\n"
    "buffer_store_dwordx4 v[20:23], v210, s[24:27], s86 offen sc1\n"
    "s_waitcnt vmcnt(30)\n"
    "buffer_store_dword v24, v245, s[24:27], 0 offen sc1\n"
    "buffer_store_dwordx4 v[26:29], v211, s[24:27], s86 offen sc1\n"
    "v_add_u32 v245, 83968, v245\n"
    "buffer_store_dword v30, v245, s[24:27], 0 offen sc1\n"
    "s_barrier\n"
    "ds_read_b64_tr_b16 v[0:1], v234 offset:10496\n"
    "ds_read_b64_tr_b16 v[2:3], v234 offset:15104\n"
    "ds_read_b64_tr_b16 v[4:5], v234 offset:10528\n"
    "ds_read_b64_tr_b16 v[6:7], v234 offset:15136\n"
    "ds_read_b64_tr_b16 v[8:9], v234 offset:10560\n"
    "ds_read_b64_tr_b16 v[10:11], v234 offset:15168\n"
    "ds_read_b64_tr_b16 v[12:13], v234 offset:10592\n"
    "ds_read_b64_tr_b16 v[14:15], v234 offset:15200\n"
    "ds_read_b64_tr_b16 v[16:17], v234 offset:10624\n"
    "ds_read_b64_tr_b16 v[18:19], v234 offset:15232\n"
    "ds_read_b64_tr_b16 v[20:21], v234 offset:10656\n"
    "ds_read_b64_tr_b16 v[22:23], v234 offset:15264\n"
    "s_waitcnt lgkmcnt(8)\n"
    "v_mfma_f32_16x16x32_bf16 v[64:67], v[0:3], a[0:3], 0\n"
    "ds_read_b64_tr_b16 v[24:25], v234 offset:10688\n"
    "v_mul_u32_u24 v184, 3641, v192\n"
    "v_mfma_f32_16x16x32_bf16 v[96:99], v[0:3], a[4:7], 0\n"
    "ds_read_b64_tr_b16 v[26:27], v234 offset:15296\n"
    "v_mfma_f32_16x16x32_bf16 v[68:71], v[4:7], a[0:3], 0\n"
    "s_add_u32 m0, s62, 0x0\n"
    "s_add_u32 s63, s63, 0x24000\n"
    "ds_read_b64_tr_b16 v[28:29], v234 offset:10720\n"
    "ds_read_b64_tr_b16 v[30:31], v234 offset:15328\n"
    "v_mfma_f32_16x16x32_bf16 v[100:103], v[4:7], a[4:7], 0\n"
    "buffer_load_dwordx4 v236, s[32:35], s63 offen offset:0 lds\n"
    "s_waitcnt lgkmcnt(8)\n"
    "v_mfma_f32_16x16x32_bf16 v[72:75], v[8:11], a[0:3], 0\n"
    "ds_read_b64_tr_b16 v[0:1], v234 offset:10752\n"
    "v_add_u32 v185, 0, v184\n"
    "v_mfma_f32_16x16x32_bf16 v[104:107], v[8:11], a[4:7], 0\n"
    "ds_read_b64_tr_b16 v[2:3], v234 offset:15360\n"
    "v_mfma_f32_16x16x32_bf16 v[76:79], v[12:15], a[0:3], 0\n"
    "ds_read_b64_tr_b16 v[4:5], v234 offset:19712\n"
    "v_bfe_u32 v186, v185, 16, 3\n"
    "v_mfma_f32_16x16x32_bf16 v[108:111], v[12:15], a[4:7], 0\n"
    "ds_read_b64_tr_b16 v[6:7], v234 offset:24320\n"
    "s_waitcnt lgkmcnt(8)\n"
    "v_mfma_f32_16x16x32_bf16 v[80:83], v[16:19], a[0:3], 0\n"
    "ds_read_b64_tr_b16 v[8:9], v234 offset:19744\n"
    "ds_read_b64_tr_b16 v[10:11], v234 offset:24352\n"
    "v_mfma_f32_16x16x32_bf16 v[112:115], v[16:19], a[4:7], 0\n"
    "buffer_load_dwordx4 v237, s[32:35], s63 offen offset:1024 lds\n"
    "v_mfma_f32_16x16x32_bf16 v[84:87], v[20:23], a[0:3], 0\n"
    "ds_read_b64_tr_b16 v[12:13], v234 offset:19776\n"
    "v_mul_u32_u24 v186, 864, v186\n"
    "v_mfma_f32_16x16x32_bf16 v[116:119], v[20:23], a[4:7], 0\n"
    "ds_read_b64_tr_b16 v[14:15], v234 offset:24384\n"
    "s_waitcnt lgkmcnt(8)\n"
    "v_mfma_f32_16x16x32_bf16 v[88:91], v[24:27], a[0:3], 0\n"
    "ds_read_b64_tr_b16 v[16:17], v234 offset:19808\n"
    "v_bfe_u32 v188, v185, 19, 1\n"
    "v_mfma_f32_16x16x32_bf16 v[120:123], v[24:27], a[4:7], 0\n"
    "ds_read_b64_tr_b16 v[18:19], v234 offset:24416\n"
    "v_mfma_f32_16x16x32_bf16 v[92:95], v[28:31], a[0:3], 0\n"
    "ds_read_b64_tr_b16 v[20:21], v234 offset:19840\n"
    "ds_read_b64_tr_b16 v[22:23], v234 offset:24448\n"
    "v_mfma_f32_16x16x32_bf16 v[124:127], v[28:31], a[4:7], 0\n"
    "buffer_load_dwordx4 v238, s[32:35], s63 offen offset:2048 lds\n"
    "s_waitcnt lgkmcnt(8)\n"
    "v_mfma_f32_16x16x32_bf16 v[128:131], v[0:3], a[0:3], 0\n"
    "ds_read_b64_tr_b16 v[24:25], v234 offset:19872\n"
    "v_mul_u32_u24 v188, 18432, v188\n"
    "v_mfma_f32_16x16x32_bf16 v[132:135], v[0:3], a[4:7], 0\n"
    "ds_read_b64_tr_b16 v[26:27], v234 offset:24480\n"
    "v_mfma_f32_16x16x32_bf16 v[64:67], v[4:7], a[8:11], v[64:67]\n"
    "ds_read_b64_tr_b16 v[28:29], v234 offset:19904\n"
    "v_add_u32 v187, 0, v246\n"
    "v_mfma_f32_16x16x32_bf16 v[96:99], v[4:7], a[12:15], v[96:99]\n"
    "ds_read_b64_tr_b16 v[30:31], v234 offset:24512\n"
    "s_waitcnt lgkmcnt(8)\n"
    "v_mfma_f32_16x16x32_bf16 v[68:71], v[8:11], a[8:11], v[68:71]\n"
    "ds_read_b64_tr_b16 v[0:1], v234 offset:19936\n"
    "ds_read_b64_tr_b16 v[2:3], v234 offset:24544\n"
    "v_mfma_f32_16x16x32_bf16 v[100:103], v[8:11], a[12:15], v[100:103]\n"
    "buffer_load_dwordx4 v239, s[32:35], s63 offen offset:3072 lds\n"
    "v_mfma_f32_16x16x32_bf16 v[72:75], v[12:15], a[8:11], v[72:75]\n"
    "ds_read_b64_tr_b16 v[4:5], v234 offset:19968\n"
    "v_add3_u32 v186, v186, v188, v187\n"
    "v_mfma_f32_16x16x32_bf16 v[104:107], v[12:15], a[12:15], v[104:107]\n"
    "ds_read_b64_tr_b16 v[6:7], v234 offset:24576\n"
    "s_waitcnt lgkmcnt(8)\n"
    "v_mfma_f32_16x16x32_bf16 v[76:79], v[16:19], a[8:11], v[76:79]\n"
    "ds_read_b64_tr_b16 v[8:9], v234 offset:28928\n"
    "v_lshrrev_b32 v185, 20, v185\n"
    "v_mfma_f32_16x16x32_bf16 v[108:111], v[16:19], a[12:15], v[108:111]\n"
    "ds_read_b64_tr_b16 v[10:11], v234 offset:33536\n"
    "v_mfma_f32_16x16x32_bf16 v[80:83], v[20:23], a[8:11], v[80:83]\n"
    "s_add_u32 m0, s62, 0x1000\n"
    "ds_read_b64_tr_b16 v[12:13], v234 offset:28960\n"
    "ds_read_b64_tr_b16 v[14:15], v234 offset:33568\n"
    "v_mfma_f32_16x16x32_bf16 v[112:115], v[20:23], a[12:15], v[112:115]\n"
    "buffer_load_dwordx4 v240, s[32:35], s63 offen offset:0 lds\n"
    "s_waitcnt lgkmcnt(8)\n"
    "v_mfma_f32_16x16x32_bf16 v[84:87], v[24:27], a[8:11], v[84:87]\n"
    "ds_read_b64_tr_b16 v[16:17], v234 offset:28992\n"
    "v_mul_u32_u24 v185, 4608, v185\n"
    "v_mfma_f32_16x16x32_bf16 v[116:119], v[24:27], a[12:15], v[116:119]\n"
    "ds_read_b64_tr_b16 v[18:19], v234 offset:33600\n"
    "v_mfma_f32_16x16x32_bf16 v[88:91], v[28:31], a[8:11], v[88:91]\n"
    "ds_read_b64_tr_b16 v[20:21], v234 offset:29024\n"
    "v_add_u32 v193, v185, v186\n"
    "v_mfma_f32_16x16x32_bf16 v[120:123], v[28:31], a[12:15], v[120:123]\n"
    "ds_read_b64_tr_b16 v[22:23], v234 offset:33632\n"
    "s_waitcnt lgkmcnt(8)\n"
    "v_mfma_f32_16x16x32_bf16 v[92:95], v[0:3], a[8:11], v[92:95]\n"
    "ds_read_b64_tr_b16 v[24:25], v234 offset:29056\n"
    "ds_read_b64_tr_b16 v[26:27], v234 offset:33664\n"
    "v_mfma_f32_16x16x32_bf16 v[124:127], v[0:3], a[12:15], v[124:127]\n"
    "buffer_load_dwordx4 v241, s[32:35], s63 offen offset:1024 lds\n"
    "v_mfma_f32_16x16x32_bf16 v[128:131], v[4:7], a[8:11], v[128:131]\n"
    "ds_read_b64_tr_b16 v[28:29], v234 offset:29088\n"
    "v_add_u32 v185, 233024, v184\n"
    "v_mfma_f32_16x16x32_bf16 v[132:135], v[4:7], a[12:15], v[132:135]\n"
    "ds_read_b64_tr_b16 v[30:31], v234 offset:33696\n"
    "s_waitcnt lgkmcnt(8)\n"
    "v_mfma_f32_16x16x32_bf16 v[64:67], v[8:11], a[16:19], v[64:67]\n"
    "ds_read_b64_tr_b16 v[0:1], v234 offset:29120\n"
    "v_bfe_u32 v186, v185, 16, 3\n"
    "v_mfma_f32_16x16x32_bf16 v[96:99], v[8:11], a[20:23], v[96:99]\n"
    "ds_read_b64_tr_b16 v[2:3], v234 offset:33728\n"
    "v_mfma_f32_16x16x32_bf16 v[68:71], v[12:15], a[16:19], v[68:71]\n"
    "ds_read_b64_tr_b16 v[4:5], v234 offset:29152\n"
    "ds_read_b64_tr_b16 v[6:7], v234 offset:33760\n"
    "v_mfma_f32_16x16x32_bf16 v[100:103], v[12:15], a[20:23], v[100:103]\n"
    "buffer_load_dwordx4 v242, s[32:35], s63 offen offset:2048 lds\n"
    "s_waitcnt lgkmcnt(8)\n"
    "v_mfma_f32_16x16x32_bf16 v[72:75], v[16:19], a[16:19], v[72:75]\n"
    "ds_read_b64_tr_b16 v[8:9], v234 offset:29184\n"
    "v_mul_u32_u24 v186, 864, v186\n"
    "v_mfma_f32_16x16x32_bf16 v[104:107], v[16:19], a[20:23], v[104:107]\n"
    "ds_read_b64_tr_b16 v[10:11], v234 offset:33792\n"
    "v_mfma_f32_16x16x32_bf16 v[76:79], v[20:23], a[16:19], v[76:79]\n"
    "ds_read_b64_tr_b16 v[12:13], v234 offset:38144\n"
    "v_bfe_u32 v188, v185, 19, 1\n"
    "v_mfma_f32_16x16x32_bf16 v[108:111], v[20:23], a[20:23], v[108:111]\n"
    "ds_read_b64_tr_b16 v[14:15], v234 offset:42752\n"
    "s_waitcnt lgkmcnt(8)\n"
    "v_mfma_f32_16x16x32_bf16 v[80:83], v[24:27], a[16:19], v[80:83]\n"
    "ds_read_b64_tr_b16 v[16:17], v234 offset:38176\n"
    "ds_read_b64_tr_b16 v[18:19], v234 offset:42784\n"
    "v_mfma_f32_16x16x32_bf16 v[112:115], v[24:27], a[20:23], v[112:115]\n"
    "buffer_load_dwordx4 v243, s[32:35], s63 offen offset:3072 lds\n"
    "v_mfma_f32_16x16x32_bf16 v[84:87], v[28:31], a[16:19], v[84:87]\n"
    "ds_read_b64_tr_b16 v[20:21], v234 offset:38208\n"
    "v_mul_u32_u24 v188, 18432, v188\n"
    "v_mfma_f32_16x16x32_bf16 v[116:119], v[28:31], a[20:23], v[116:119]\n"
    "ds_read_b64_tr_b16 v[22:23], v234 offset:42816\n"
    "s_waitcnt lgkmcnt(8)\n"
    "v_mfma_f32_16x16x32_bf16 v[88:91], v[0:3], a[16:19], v[88:91]\n"
    "ds_read_b64_tr_b16 v[24:25], v234 offset:38240\n"
    "v_add3_u32 v186, v186, v188, v187\n"
    "v_mfma_f32_16x16x32_bf16 v[120:123], v[0:3], a[20:23], v[120:123]\n"
    "ds_read_b64_tr_b16 v[26:27], v234 offset:42848\n"
    "v_mfma_f32_16x16x32_bf16 v[92:95], v[4:7], a[16:19], v[92:95]\n"
    "s_add_u32 m0, s62, 0x2000\n"
    "ds_read_b64_tr_b16 v[28:29], v234 offset:38272\n"
    "ds_read_b64_tr_b16 v[30:31], v234 offset:42880\n"
    "v_mfma_f32_16x16x32_bf16 v[124:127], v[4:7], a[20:23], v[124:127]\n"
    "buffer_load_dwordx4 v244, s[32:35], s63 offen offset:0 lds\n"
    "s_waitcnt lgkmcnt(8)\n"
    "v_mfma_f32_16x16x32_bf16 v[128:131], v[8:11], a[16:19], v[128:131]\n"
    "ds_read_b64_tr_b16 v[0:1], v234 offset:38304\n"
    "v_lshrrev_b32 v185, 20, v185\n"
    "v_mfma_f32_16x16x32_bf16 v[132:135], v[8:11], a[20:23], v[132:135]\n"
    "ds_read_b64_tr_b16 v[2:3], v234 offset:42912\n"
    "v_mfma_f32_16x16x32_bf16 v[64:67], v[12:15], a[24:27], v[64:67]\n"
    "ds_read_b64_tr_b16 v[4:5], v234 offset:38336\n"
    "v_mul_u32_u24 v185, 4608, v185\n"
    "v_mfma_f32_16x16x32_bf16 v[96:99], v[12:15], a[28:31], v[96:99]\n"
    "ds_read_b64_tr_b16 v[6:7], v234 offset:42944\n"
    "s_waitcnt lgkmcnt(8)\n"
    "v_mfma_f32_16x16x32_bf16 v[68:71], v[16:19], a[24:27], v[68:71]\n"
    "s_add_u32 m0, s62, 0x9480\n"
    "s_add_u32 s63, s63, 0x24000\n"
    "ds_read_b64_tr_b16 v[8:9], v234 offset:38368\n"
    "ds_read_b64_tr_b16 v[10:11], v234 offset:42976\n"
    "v_mfma_f32_16x16x32_bf16 v[100:103], v[16:19], a[28:31], v[100:103]\n"
    "buffer_load_dwordx4 v236, s[32:35], s63 offen offset:0 lds\n"
    "v_mfma_f32_16x16x32_bf16 v[72:75], v[20:23], a[24:27], v[72:75]\n"
    "ds_read_b64_tr_b16 v[12:13], v234 offset:38400\n"
    "v_add_u32 v194, v185, v186\n"
    "v_mfma_f32_16x16x32_bf16 v[104:107], v[20:23], a[28:31], v[104:107]\n"
    "ds_read_b64_tr_b16 v[14:15], v234 offset:43008\n"
    "s_waitcnt vmcnt(20)\n"
    "s_barrier\n"
    "s_waitcnt lgkmcnt(8)\n"
    "v_mfma_f32_16x16x32_bf16 v[76:79], v[24:27], a[24:27], v[76:79]\n"
    "ds_read_b64_tr_b16 v[16:17], v234 offset:48512\n"
    "v_add_u32 v185, 466048, v184\n"
    "v_mfma_f32_16x16x32_bf16 v[108:111], v[24:27], a[28:31], v[108:111]\n"
    "ds_read_b64_tr_b16 v[18:19], v234 offset:53120\n"
    "v_mfma_f32_16x16x32_bf16 v[80:83], v[28:31], a[24:27], v[80:83]\n"
    "ds_read_b64_tr_b16 v[20:21], v234 offset:48544\n"
    "ds_read_b64_tr_b16 v[22:23], v234 offset:53152\n"
    "v_mfma_f32_16x16x32_bf16 v[112:115], v[28:31], a[28:31], v[112:115]\n"
    "buffer_load_dwordx4 v237, s[32:35], s63 offen offset:1024 lds\n"
    "s_waitcnt lgkmcnt(8)\n"
    "v_mfma_f32_16x16x32_bf16 v[84:87], v[0:3], a[24:27], v[84:87]\n"
    "ds_read_b64_tr_b16 v[24:25], v234 offset:48576\n"
    "v_bfe_u32 v186, v185, 16, 3\n"
    "v_mfma_f32_16x16x32_bf16 v[116:119], v[0:3], a[28:31], v[116:119]\n"
    "ds_read_b64_tr_b16 v[26:27], v234 offset:53184\n"
    "v_mfma_f32_16x16x32_bf16 v[88:91], v[4:7], a[24:27], v[88:91]\n"
    "ds_read_b64_tr_b16 v[28:29], v234 offset:48608\n"
    "v_mul_u32_u24 v186, 864, v186\n"
    "v_mfma_f32_16x16x32_bf16 v[120:123], v[4:7], a[28:31], v[120:123]\n"
    "ds_read_b64_tr_b16 v[30:31], v234 offset:53216\n"
    "s_waitcnt lgkmcnt(8)\n"
    "v_mfma_f32_16x16x32_bf16 v[92:95], v[8:11], a[24:27], v[92:95]\n"
    "ds_read_b64_tr_b16 v[0:1], v234 offset:48640\n"
    "ds_read_b64_tr_b16 v[2:3], v234 offset:53248\n"
    "v_mfma_f32_16x16x32_bf16 v[124:127], v[8:11], a[28:31], v[124:127]\n"
    "buffer_load_dwordx4 v238, s[32:35], s63 offen offset:2048 lds\n"
    "v_mfma_f32_16x16x32_bf16 v[128:131], v[12:15], a[24:27], v[128:131]\n"
    "ds_read_b64_tr_b16 v[4:5], v234 offset:48672\n"
    "v_bfe_u32 v188, v185, 19, 1\n"
    "v_mfma_f32_16x16x32_bf16 v[132:135], v[12:15], a[28:31], v[132:135]\n"
    "ds_read_b64_tr_b16 v[6:7], v234 offset:53280\n"
    "s_waitcnt lgkmcnt(8)\n"
    "v_mfma_f32_16x16x32_bf16 v[64:67], v[16:19], a[32:35], v[64:67]\n"
    "ds_read_b64_tr_b16 v[8:9], v234 offset:48704\n"
    "v_mul_u32_u24 v188, 18432, v188\n"
    "v_mfma_f32_16x16x32_bf16 v[96:99], v[16:19], a[36:39], v[96:99]\n"
    "ds_read_b64_tr_b16 v[10:11], v234 offset:53312\n"
    "v_mfma_f32_16x16x32_bf16 v[68:71], v[20:23], a[32:35], v[68:71]\n"
    "ds_read_b64_tr_b16 v[12:13], v234 offset:48736\n"
    "ds_read_b64_tr_b16 v[14:15], v234 offset:53344\n"
    "v_mfma_f32_16x16x32_bf16 v[100:103], v[20:23], a[36:39], v[100:103]\n"
    "buffer_load_dwordx4 v239, s[32:35], s63 offen offset:3072 lds\n"
    "s_waitcnt lgkmcnt(8)\n"
    "v_mfma_f32_16x16x32_bf16 v[72:75], v[24:27], a[32:35], v[72:75]\n"
    "ds_read_b64_tr_b16 v[16:17], v234 offset:48768\n"
    "v_add3_u32 v186, v186, v188, v187\n"
    "v_mfma_f32_16x16x32_bf16 v[104:107], v[24:27], a[36:39], v[104:107]\n"
    "ds_read_b64_tr_b16 v[18:19], v234 offset:53376\n"
    "v_mfma_f32_16x16x32_bf16 v[76:79], v[28:31], a[32:35], v[76:79]\n"
    "ds_read_b64_tr_b16 v[20:21], v234 offset:57728\n"
    "v_lshrrev_b32 v185, 20, v185\n"
    "v_mfma_f32_16x16x32_bf16 v[108:111], v[28:31], a[36:39], v[108:111]\n"
    "ds_read_b64_tr_b16 v[22:23], v234 offset:62336\n"
    "s_waitcnt lgkmcnt(8)\n"
    "v_mfma_f32_16x16x32_bf16 v[80:83], v[0:3], a[32:35], v[80:83]\n"
    "s_add_u32 m0, s62, 0xa480\n"
    "ds_read_b64_tr_b16 v[24:25], v234 offset:57760\n"
    "ds_read_b64_tr_b16 v[26:27], v234 offset:62368\n"
    "v_mfma_f32_16x16x32_bf16 v[112:115], v[0:3], a[36:39], v[112:115]\n"
    "buffer_load_dwordx4 v240, s[32:35], s63 offen offset:0 lds\n"
    "v_mfma_f32_16x16x32_bf16 v[84:87], v[4:7], a[32:35], v[84:87]\n"
    "ds_read_b64_tr_b16 v[28:29], v234 offset:57792\n"
    "v_mul_u32_u24 v185, 4608, v185\n"
    "v_mfma_f32_16x16x32_bf16 v[116:119], v[4:7], a[36:39], v[116:119]\n"
    "ds_read_b64_tr_b16 v[30:31], v234 offset:62400\n"
    "s_waitcnt lgkmcnt(8)\n"
    "v_mfma_f32_16x16x32_bf16 v[88:91], v[8:11], a[32:35], v[88:91]\n"
    "ds_read_b64_tr_b16 v[0:1], v234 offset:57824\n"
    "v_add_u32 v195, v185, v186\n"
    "v_mfma_f32_16x16x32_bf16 v[120:123], v[8:11], a[36:39], v[120:123]\n"
    "ds_read_b64_tr_b16 v[2:3], v234 offset:62432\n"
    "v_mfma_f32_16x16x32_bf16 v[92:95], v[12:15], a[32:35], v[92:95]\n"
    "ds_read_b64_tr_b16 v[4:5], v234 offset:57856\n"
    "ds_read_b64_tr_b16 v[6:7], v234 offset:62464\n"
    "v_mfma_f32_16x16x32_bf16 v[124:127], v[12:15], a[36:39], v[124:127]\n"
    "buffer_load_dwordx4 v241, s[32:35], s63 offen offset:1024 lds\n"
    "s_waitcnt lgkmcnt(8)\n"
    "v_mfma_f32_16x16x32_bf16 v[128:131], v[16:19], a[32:35], v[128:131]\n"
    "ds_read_b64_tr_b16 v[8:9], v234 offset:57888\n"
    "v_add_u32 v185, 699072, v184\n"
    "v_mfma_f32_16x16x32_bf16 v[132:135], v[16:19], a[36:39], v[132:135]\n"
    "ds_read_b64_tr_b16 v[10:11], v234 offset:62496\n"
    "v_mfma_f32_16x16x32_bf16 v[64:67], v[20:23], a[40:43], v[64:67]\n"
    "ds_read_b64_tr_b16 v[12:13], v234 offset:57920\n"
    "v_bfe_u32 v186, v185, 16, 3\n"
    "v_mfma_f32_16x16x32_bf16 v[96:99], v[20:23], a[44:47], v[96:99]\n"
    "ds_read_b64_tr_b16 v[14:15], v234 offset:62528\n"
    "s_waitcnt lgkmcnt(8)\n"
    "v_mfma_f32_16x16x32_bf16 v[68:71], v[24:27], a[40:43], v[68:71]\n"
    "ds_read_b64_tr_b16 v[16:17], v234 offset:57952\n"
    "ds_read_b64_tr_b16 v[18:19], v234 offset:62560\n"
    "v_mfma_f32_16x16x32_bf16 v[100:103], v[24:27], a[44:47], v[100:103]\n"
    "buffer_load_dwordx4 v242, s[32:35], s63 offen offset:2048 lds\n"
    "v_mfma_f32_16x16x32_bf16 v[72:75], v[28:31], a[40:43], v[72:75]\n"
    "ds_read_b64_tr_b16 v[20:21], v234 offset:57984\n"
    "v_mul_u32_u24 v186, 864, v186\n"
    "v_mfma_f32_16x16x32_bf16 v[104:107], v[28:31], a[44:47], v[104:107]\n"
    "ds_read_b64_tr_b16 v[22:23], v234 offset:62592\n"
    "s_waitcnt lgkmcnt(8)\n"
    "v_mfma_f32_16x16x32_bf16 v[76:79], v[0:3], a[40:43], v[76:79]\n"
    "ds_read_b64_tr_b16 v[24:25], v235 offset:1408\n"
    "v_bfe_u32 v188, v185, 19, 1\n"
    "v_mfma_f32_16x16x32_bf16 v[108:111], v[0:3], a[44:47], v[108:111]\n"
    "ds_read_b64_tr_b16 v[26:27], v235 offset:6016\n"
    "v_mfma_f32_16x16x32_bf16 v[80:83], v[4:7], a[40:43], v[80:83]\n"
    "ds_read_b64_tr_b16 v[28:29], v235 offset:1440\n"
    "ds_read_b64_tr_b16 v[30:31], v235 offset:6048\n"
    "v_mfma_f32_16x16x32_bf16 v[112:115], v[4:7], a[44:47], v[112:115]\n"
    "buffer_load_dwordx4 v243, s[32:35], s63 offen offset:3072 lds\n"
    "s_waitcnt lgkmcnt(8)\n"
    "v_mfma_f32_16x16x32_bf16 v[84:87], v[8:11], a[40:43], v[84:87]\n"
    "ds_read_b64_tr_b16 v[0:1], v235 offset:1472\n"
    "v_mul_u32_u24 v188, 18432, v188\n"
    "v_mfma_f32_16x16x32_bf16 v[116:119], v[8:11], a[44:47], v[116:119]\n"
    "ds_read_b64_tr_b16 v[2:3], v235 offset:6080\n"
    "v_mfma_f32_16x16x32_bf16 v[88:91], v[12:15], a[40:43], v[88:91]\n"
    "ds_read_b64_tr_b16 v[4:5], v235 offset:1504\n"
    "v_add3_u32 v186, v186, v188, v187\n"
    "v_mfma_f32_16x16x32_bf16 v[120:123], v[12:15], a[44:47], v[120:123]\n"
    "ds_read_b64_tr_b16 v[6:7], v235 offset:6112\n"
    "s_waitcnt lgkmcnt(8)\n"
    "v_mfma_f32_16x16x32_bf16 v[92:95], v[16:19], a[40:43], v[92:95]\n"
    "s_add_u32 m0, s62, 0xb480\n"
    "ds_read_b64_tr_b16 v[8:9], v235 offset:1536\n"
    "ds_read_b64_tr_b16 v[10:11], v235 offset:6144\n"
    "v_mfma_f32_16x16x32_bf16 v[124:127], v[16:19], a[44:47], v[124:127]\n"
    "buffer_load_dwordx4 v244, s[32:35], s63 offen offset:0 lds\n"
    "v_mfma_f32_16x16x32_bf16 v[128:131], v[20:23], a[40:43], v[128:131]\n"
    "ds_read_b64_tr_b16 v[12:13], v235 offset:1568\n"
    "v_lshrrev_b32 v185, 20, v185\n"
    "v_mfma_f32_16x16x32_bf16 v[132:135], v[20:23], a[44:47], v[132:135]\n"
    "ds_read_b64_tr_b16 v[14:15], v235 offset:6176\n"
    "s_waitcnt lgkmcnt(8)\n"
    "v_mfma_f32_16x16x32_bf16 v[64:67], v[24:27], a[48:51], v[64:67]\n"
    "ds_read_b64_tr_b16 v[16:17], v235 offset:1600\n"
    "v_mul_u32_u24 v185, 4608, v185\n"
    "v_mfma_f32_16x16x32_bf16 v[96:99], v[24:27], a[52:55], v[96:99]\n"
    "ds_read_b64_tr_b16 v[18:19], v235 offset:6208\n"
    "v_mfma_f32_16x16x32_bf16 v[68:71], v[28:31], a[48:51], v[68:71]\n"
    "ds_read_b64_tr_b16 v[20:21], v235 offset:1632\n"
    "v_add_u32 v196, v185, v186\n"
    "v_mfma_f32_16x16x32_bf16 v[100:103], v[28:31], a[52:55], v[100:103]\n"
    "ds_read_b64_tr_b16 v[22:23], v235 offset:6240\n"
    "s_waitcnt lgkmcnt(8)\n"
    "v_mfma_f32_16x16x32_bf16 v[72:75], v[0:3], a[48:51], v[72:75]\n"
    "ds_read_b64_tr_b16 v[24:25], v235 offset:1664\n"
    "v_add_u32 v185, 932096, v184\n"
    "v_mfma_f32_16x16x32_bf16 v[104:107], v[0:3], a[52:55], v[104:107]\n"
    "ds_read_b64_tr_b16 v[26:27], v235 offset:6272\n"
    "v_mfma_f32_16x16x32_bf16 v[76:79], v[4:7], a[48:51], v[76:79]\n"
    "ds_read_b64_tr_b16 v[28:29], v235 offset:10624\n"
    "v_bfe_u32 v186, v185, 16, 3\n"
    "v_mfma_f32_16x16x32_bf16 v[108:111], v[4:7], a[52:55], v[108:111]\n"
    "ds_read_b64_tr_b16 v[30:31], v235 offset:15232\n"
    "s_waitcnt lgkmcnt(8)\n"
    "v_mfma_f32_16x16x32_bf16 v[80:83], v[8:11], a[48:51], v[80:83]\n"
    "ds_read_b64_tr_b16 v[0:1], v235 offset:10656\n"
    "v_mul_u32_u24 v186, 864, v186\n"
    "v_mfma_f32_16x16x32_bf16 v[112:115], v[8:11], a[52:55], v[112:115]\n"
    "ds_read_b64_tr_b16 v[2:3], v235 offset:15264\n"
    "v_mfma_f32_16x16x32_bf16 v[84:87], v[12:15], a[48:51], v[84:87]\n"
    "ds_read_b64_tr_b16 v[4:5], v235 offset:10688\n"
    "v_bfe_u32 v188, v185, 19, 1\n"
    "v_mfma_f32_16x16x32_bf16 v[116:119], v[12:15], a[52:55], v[116:119]\n"
    "ds_read_b64_tr_b16 v[6:7], v235 offset:15296\n"
    "s_waitcnt lgkmcnt(8)\n"
    "v_mfma_f32_16x16x32_bf16 v[88:91], v[16:19], a[48:51], v[88:91]\n"
    "ds_read_b64_tr_b16 v[8:9], v235 offset:10720\n"
    "s_lshl_b32 s15, s64, 2\n"
    "s_add_u32 s15, s15, s9\n"
    "s_lshl_b32 s15, s15, 3\n"
    "s_add_u32 s15, s15, 5120\n"
    "v_lshl_add_u32 v225, v192, 24, s15\n"
    "v_mov_b32 v226, s48\n"
    "v_mov_b32 v227, s49\n"
    "s_waitcnt vmcnt(18)\n"
    "v_mfma_f32_16x16x32_bf16 v[120:123], v[16:19], a[52:55], v[120:123]\n"
    "buffer_store_dwordx2 v[226:227], v225, s[68:71], 0 offen sc1\n"
    "ds_read_b64_tr_b16 v[10:11], v235 offset:15328\n"
    "v_mfma_f32_16x16x32_bf16 v[92:95], v[20:23], a[48:51], v[92:95]\n"
    "ds_read_b64_tr_b16 v[12:13], v235 offset:10752\n"
    "v_mul_u32_u24 v188, 18432, v188\n"
    "v_mfma_f32_16x16x32_bf16 v[124:127], v[20:23], a[52:55], v[124:127]\n"
    "ds_read_b64_tr_b16 v[14:15], v235 offset:15360\n"
    "s_waitcnt lgkmcnt(8)\n"
    "v_mfma_f32_16x16x32_bf16 v[128:131], v[24:27], a[48:51], v[128:131]\n"
    "ds_read_b64_tr_b16 v[16:17], v235 offset:10784\n"
    "v_add_u32 v187, 4096, v246\n"
    "v_mfma_f32_16x16x32_bf16 v[132:135], v[24:27], a[52:55], v[132:135]\n"
    "ds_read_b64_tr_b16 v[18:19], v235 offset:15392\n"
    "v_mfma_f32_16x16x32_bf16 v[64:67], v[28:31], a[56:59], v[64:67]\n"
    "ds_read_b64_tr_b16 v[20:21], v235 offset:10816\n"
    "v_add3_u32 v186, v186, v188, v187\n"
    "v_mfma_f32_16x16x32_bf16 v[96:99], v[28:31], a[60:63], v[96:99]\n"
    "ds_read_b64_tr_b16 v[22:23], v235 offset:15424\n"
    "s_waitcnt lgkmcnt(8)\n"
    "v_mfma_f32_16x16x32_bf16 v[68:71], v[0:3], a[56:59], v[68:71]\n"
    "ds_read_b64_tr_b16 v[24:25], v235 offset:10848\n"
    "v_lshrrev_b32 v185, 20, v185\n"
    "v_mfma_f32_16x16x32_bf16 v[100:103], v[0:3], a[60:63], v[100:103]\n"
    "ds_read_b64_tr_b16 v[26:27], v235 offset:15456\n"
    "v_mfma_f32_16x16x32_bf16 v[72:75], v[4:7], a[56:59], v[72:75]\n"
    "ds_read_b64_tr_b16 v[28:29], v235 offset:10880\n"
    "v_mul_u32_u24 v185, 4608, v185\n"
    "v_mfma_f32_16x16x32_bf16 v[104:107], v[4:7], a[60:63], v[104:107]\n"
    "ds_read_b64_tr_b16 v[30:31], v235 offset:15488\n"
    "s_waitcnt vmcnt(10)\n"
    "s_barrier\n"
    "s_waitcnt lgkmcnt(8)\n"
    "v_mfma_f32_16x16x32_bf16 v[76:79], v[8:11], a[56:59], v[76:79]\n"
    "ds_read_b64_tr_b16 v[0:1], v233 offset:0\n"
    "v_add_u32 v197, v185, v186\n"
    "v_mfma_f32_16x16x32_bf16 v[108:111], v[8:11], a[60:63], v[108:111]\n"
    "ds_read_b64_tr_b16 v[2:3], v233 offset:4608\n"
    "v_mfma_f32_16x16x32_bf16 v[80:83], v[12:15], a[56:59], v[80:83]\n"
    "s_add_u32 m0, s62, 0x12900\n"
    "s_add_u32 s63, s63, 0x24000\n"
    "ds_read_b64_tr_b16 v[4:5], v233 offset:32\n"
    "ds_read_b64_tr_b16 v[6:7], v233 offset:4640\n"
    "v_mfma_f32_16x16x32_bf16 v[112:115], v[12:15], a[60:63], v[112:115]\n"
    "buffer_load_dwordx4 v236, s[32:35], s63 offen offset:0 lds\n"
    "s_waitcnt lgkmcnt(8)\n"
    "v_mfma_f32_16x16x32_bf16 v[84:87], v[16:19], a[56:59], v[84:87]\n"
    "ds_read_b64_tr_b16 v[8:9], v233 offset:64\n"
    "v_add_u32 v185, 1165120, v184\n"
    "v_mfma_f32_16x16x32_bf16 v[116:119], v[16:19], a[60:63], v[116:119]\n"
    "ds_read_b64_tr_b16 v[10:11], v233 offset:4672\n"
    "v_mfma_f32_16x16x32_bf16 v[88:91], v[20:23], a[56:59], v[88:91]\n"
    "ds_read_b64_tr_b16 v[12:13], v233 offset:96\n"
    "v_bfe_u32 v186, v185, 16, 3\n"
    "v_mfma_f32_16x16x32_bf16 v[120:123], v[20:23], a[60:63], v[120:123]\n"
    "ds_read_b64_tr_b16 v[14:15], v233 offset:4704\n"
    "s_waitcnt lgkmcnt(8)\n"
    "v_mfma_f32_16x16x32_bf16 v[92:95], v[24:27], a[56:59], v[92:95]\n"
    "ds_read_b64_tr_b16 v[16:17], v233 offset:128\n"
    "ds_read_b64_tr_b16 v[18:19], v233 offset:4736\n"
    "v_mfma_f32_16x16x32_bf16 v[124:127], v[24:27], a[60:63], v[124:127]\n"
    "buffer_load_dwordx4 v237, s[32:35], s63 offen offset:1024 lds\n"
    "v_mfma_f32_16x16x32_bf16 v[128:131], v[28:31], a[56:59], v[128:131]\n"
    "ds_read_b64_tr_b16 v[20:21], v233 offset:160\n"
    "v_mul_u32_u24 v186, 864, v186\n"
    "v_mfma_f32_16x16x32_bf16 v[132:135], v[28:31], a[60:63], v[132:135]\n"
    "ds_read_b64_tr_b16 v[22:23], v233 offset:4768\n"
    "s_waitcnt lgkmcnt(8)\n"
    "v_mfma_f32_16x16x32_bf16 v[64:67], v[0:3], a[64:67], v[64:67]\n"
    "ds_read_b64_tr_b16 v[24:25], v233 offset:192\n"
    "v_bfe_u32 v188, v185, 19, 1\n"
    "v_mfma_f32_16x16x32_bf16 v[96:99], v[0:3], a[68:71], v[96:99]\n"
    "ds_read_b64_tr_b16 v[26:27], v233 offset:4800\n"
    "v_mfma_f32_16x16x32_bf16 v[68:71], v[4:7], a[64:67], v[68:71]\n"
    "ds_read_b64_tr_b16 v[28:29], v233 offset:224\n"
    "ds_read_b64_tr_b16 v[30:31], v233 offset:4832\n"
    "v_mfma_f32_16x16x32_bf16 v[100:103], v[4:7], a[68:71], v[100:103]\n"
    "buffer_load_dwordx4 v238, s[32:35], s63 offen offset:2048 lds\n"
    "s_waitcnt lgkmcnt(8)\n"
    "v_mfma_f32_16x16x32_bf16 v[72:75], v[8:11], a[64:67], v[72:75]\n"
    "ds_read_b64_tr_b16 v[0:1], v233 offset:256\n"
    "v_mul_u32_u24 v188, 18432, v188\n"
    "v_mfma_f32_16x16x32_bf16 v[104:107], v[8:11], a[68:71], v[104:107]\n"
    "ds_read_b64_tr_b16 v[2:3], v233 offset:4864\n"
    "v_mfma_f32_16x16x32_bf16 v[76:79], v[12:15], a[64:67], v[76:79]\n"
    "ds_read_b64_tr_b16 v[4:5], v233 offset:9216\n"
    "v_add3_u32 v186, v186, v188, v187\n"
    "v_mfma_f32_16x16x32_bf16 v[108:111], v[12:15], a[68:71], v[108:111]\n"
    "ds_read_b64_tr_b16 v[6:7], v233 offset:13824\n"
    "s_waitcnt lgkmcnt(8)\n"
    "v_mfma_f32_16x16x32_bf16 v[80:83], v[16:19], a[64:67], v[80:83]\n"
    "ds_read_b64_tr_b16 v[8:9], v233 offset:9248\n"
    "ds_read_b64_tr_b16 v[10:11], v233 offset:13856\n"
    "v_mfma_f32_16x16x32_bf16 v[112:115], v[16:19], a[68:71], v[112:115]\n"
    "buffer_load_dwordx4 v239, s[32:35], s63 offen offset:3072 lds\n"
    "v_mfma_f32_16x16x32_bf16 v[84:87], v[20:23], a[64:67], v[84:87]\n"
    "ds_read_b64_tr_b16 v[12:13], v233 offset:9280\n"
    "v_lshrrev_b32 v185, 20, v185\n"
    "v_mfma_f32_16x16x32_bf16 v[116:119], v[20:23], a[68:71], v[116:119]\n"
    "ds_read_b64_tr_b16 v[14:15], v233 offset:13888\n"
    "s_waitcnt lgkmcnt(8)\n"
    "v_mfma_f32_16x16x32_bf16 v[88:91], v[24:27], a[64:67], v[88:91]\n"
    "ds_read_b64_tr_b16 v[16:17], v233 offset:9312\n"
    "v_mul_u32_u24 v185, 4608, v185\n"
    "v_mfma_f32_16x16x32_bf16 v[120:123], v[24:27], a[68:71], v[120:123]\n"
    "ds_read_b64_tr_b16 v[18:19], v233 offset:13920\n"
    "v_mfma_f32_16x16x32_bf16 v[92:95], v[28:31], a[64:67], v[92:95]\n"
    "s_add_u32 m0, s62, 0x13900\n"
    "ds_read_b64_tr_b16 v[20:21], v233 offset:9344\n"
    "ds_read_b64_tr_b16 v[22:23], v233 offset:13952\n"
    "v_mfma_f32_16x16x32_bf16 v[124:127], v[28:31], a[68:71], v[124:127]\n"
    "buffer_load_dwordx4 v240, s[32:35], s63 offen offset:0 lds\n"
    "s_waitcnt lgkmcnt(8)\n"
    "v_mfma_f32_16x16x32_bf16 v[128:131], v[0:3], a[64:67], v[128:131]\n"
    "ds_read_b64_tr_b16 v[24:25], v233 offset:9376\n"
    "v_add_u32 v198, v185, v186\n"
    "v_mfma_f32_16x16x32_bf16 v[132:135], v[0:3], a[68:71], v[132:135]\n"
    "ds_read_b64_tr_b16 v[26:27], v233 offset:13984\n"
    "v_mfma_f32_16x16x32_bf16 v[64:67], v[4:7], a[72:75], v[64:67]\n"
    "ds_read_b64_tr_b16 v[28:29], v233 offset:9408\n"
    "v_add_u32 v185, 1398144, v184\n"
    "v_mfma_f32_16x16x32_bf16 v[96:99], v[4:7], a[76:79], v[96:99]\n"
    "ds_read_b64_tr_b16 v[30:31], v233 offset:14016\n"
    "s_waitcnt lgkmcnt(8)\n"
    "v_mfma_f32_16x16x32_bf16 v[68:71], v[8:11], a[72:75], v[68:71]\n"
    "ds_read_b64_tr_b16 v[0:1], v233 offset:9440\n"
    "ds_read_b64_tr_b16 v[2:3], v233 offset:14048\n"
    "v_mfma_f32_16x16x32_bf16 v[100:103], v[8:11], a[76:79], v[100:103]\n"
    "buffer_load_dwordx4 v241, s[32:35], s63 offen offset:1024 lds\n"
    "v_mfma_f32_16x16x32_bf16 v[72:75], v[12:15], a[72:75], v[72:75]\n"
    "ds_read_b64_tr_b16 v[4:5], v233 offset:9472\n"
    "v_bfe_u32 v186, v185, 16, 3\n"
    "v_mfma_f32_16x16x32_bf16 v[104:107], v[12:15], a[76:79], v[104:107]\n"
    "ds_read_b64_tr_b16 v[6:7], v233 offset:14080\n"
    "s_waitcnt lgkmcnt(8)\n"
    "v_mfma_f32_16x16x32_bf16 v[76:79], v[16:19], a[72:75], v[76:79]\n"
    "ds_read_b64_tr_b16 v[8:9], v233 offset:18432\n"
    "v_mul_u32_u24 v186, 864, v186\n"
    "v_mfma_f32_16x16x32_bf16 v[108:111], v[16:19], a[76:79], v[108:111]\n"
    "ds_read_b64_tr_b16 v[10:11], v233 offset:23040\n"
    "v_mfma_f32_16x16x32_bf16 v[80:83], v[20:23], a[72:75], v[80:83]\n"
    "ds_read_b64_tr_b16 v[12:13], v233 offset:18464\n"
    "ds_read_b64_tr_b16 v[14:15], v233 offset:23072\n"
    "v_mfma_f32_16x16x32_bf16 v[112:115], v[20:23], a[76:79], v[112:115]\n"
    "buffer_load_dwordx4 v242, s[32:35], s63 offen offset:2048 lds\n"
    "s_waitcnt lgkmcnt(8)\n"
    "v_mfma_f32_16x16x32_bf16 v[84:87], v[24:27], a[72:75], v[84:87]\n"
    "ds_read_b64_tr_b16 v[16:17], v233 offset:18496\n"
    "v_bfe_u32 v188, v185, 19, 1\n"
    "v_mfma_f32_16x16x32_bf16 v[116:119], v[24:27], a[76:79], v[116:119]\n"
    "ds_read_b64_tr_b16 v[18:19], v233 offset:23104\n"
    "v_mfma_f32_16x16x32_bf16 v[88:91], v[28:31], a[72:75], v[88:91]\n"
    "ds_read_b64_tr_b16 v[20:21], v233 offset:18528\n"
    "v_mul_u32_u24 v188, 18432, v188\n"
    "v_mfma_f32_16x16x32_bf16 v[120:123], v[28:31], a[76:79], v[120:123]\n"
    "ds_read_b64_tr_b16 v[22:23], v233 offset:23136\n"
    "s_waitcnt lgkmcnt(8)\n"
    "v_mfma_f32_16x16x32_bf16 v[92:95], v[0:3], a[72:75], v[92:95]\n"
    "ds_read_b64_tr_b16 v[24:25], v233 offset:18560\n"
    "ds_read_b64_tr_b16 v[26:27], v233 offset:23168\n"
    "v_mfma_f32_16x16x32_bf16 v[124:127], v[0:3], a[76:79], v[124:127]\n"
    "buffer_load_dwordx4 v243, s[32:35], s63 offen offset:3072 lds\n"
    "v_mfma_f32_16x16x32_bf16 v[128:131], v[4:7], a[72:75], v[128:131]\n"
    "ds_read_b64_tr_b16 v[28:29], v233 offset:18592\n"
    "v_add3_u32 v186, v186, v188, v187\n"
    "v_mfma_f32_16x16x32_bf16 v[132:135], v[4:7], a[76:79], v[132:135]\n"
    "ds_read_b64_tr_b16 v[30:31], v233 offset:23200\n"
    "s_waitcnt lgkmcnt(8)\n"
    "v_mfma_f32_16x16x32_bf16 v[64:67], v[8:11], a[80:83], v[64:67]\n"
    "ds_read_b64_tr_b16 v[0:1], v233 offset:18624\n"
    "v_lshrrev_b32 v185, 20, v185\n"
    "v_mfma_f32_16x16x32_bf16 v[96:99], v[8:11], a[84:87], v[96:99]\n"
    "ds_read_b64_tr_b16 v[2:3], v233 offset:23232\n"
    "v_mfma_f32_16x16x32_bf16 v[68:71], v[12:15], a[80:83], v[68:71]\n"
    "s_add_u32 m0, s62, 0x14900\n"
    "ds_read_b64_tr_b16 v[4:5], v233 offset:18656\n"
    "ds_read_b64_tr_b16 v[6:7], v233 offset:23264\n"
    "v_mfma_f32_16x16x32_bf16 v[100:103], v[12:15], a[84:87], v[100:103]\n"
    "buffer_load_dwordx4 v244, s[32:35], s63 offen offset:0 lds\n"
    "s_waitcnt lgkmcnt(8)\n"
    "v_mfma_f32_16x16x32_bf16 v[72:75], v[16:19], a[80:83], v[72:75]\n"
    "ds_read_b64_tr_b16 v[8:9], v233 offset:18688\n"
    "v_mul_u32_u24 v185, 4608, v185\n"
    "v_mfma_f32_16x16x32_bf16 v[104:107], v[16:19], a[84:87], v[104:107]\n"
    "ds_read_b64_tr_b16 v[10:11], v233 offset:23296\n"
    "v_mfma_f32_16x16x32_bf16 v[76:79], v[20:23], a[80:83], v[76:79]\n"
    "ds_read_b64_tr_b16 v[12:13], v233 offset:27648\n"
    "v_add_u32 v199, v185, v186\n"
    "v_mfma_f32_16x16x32_bf16 v[108:111], v[20:23], a[84:87], v[108:111]\n"
    "ds_read_b64_tr_b16 v[14:15], v233 offset:32256\n"
    "s_waitcnt lgkmcnt(8)\n"
    "v_mfma_f32_16x16x32_bf16 v[80:83], v[24:27], a[80:83], v[80:83]\n"
    "ds_read_b64_tr_b16 v[16:17], v233 offset:27680\n"
    "v_add_u32 v185, 1631168, v184\n"
    "v_mfma_f32_16x16x32_bf16 v[112:115], v[24:27], a[84:87], v[112:115]\n"
    "ds_read_b64_tr_b16 v[18:19], v233 offset:32288\n"
    "v_mfma_f32_16x16x32_bf16 v[84:87], v[28:31], a[80:83], v[84:87]\n"
    "ds_read_b64_tr_b16 v[20:21], v233 offset:27712\n"
    "v_bfe_u32 v186, v185, 16, 3\n"
    "v_mfma_f32_16x16x32_bf16 v[116:119], v[28:31], a[84:87], v[116:119]\n"
    "ds_read_b64_tr_b16 v[22:23], v233 offset:32320\n"
    "s_waitcnt lgkmcnt(8)\n"
    "v_mfma_f32_16x16x32_bf16 v[88:91], v[0:3], a[80:83], v[88:91]\n"
    "ds_read_b64_tr_b16 v[24:25], v233 offset:27744\n"
    "v_mul_u32_u24 v186, 864, v186\n"
    "v_mfma_f32_16x16x32_bf16 v[120:123], v[0:3], a[84:87], v[120:123]\n"
    "ds_read_b64_tr_b16 v[26:27], v233 offset:32352\n"
    "v_mfma_f32_16x16x32_bf16 v[92:95], v[4:7], a[80:83], v[92:95]\n"
    "ds_read_b64_tr_b16 v[28:29], v233 offset:27776\n"
    "v_bfe_u32 v188, v185, 19, 1\n"
    "v_mfma_f32_16x16x32_bf16 v[124:127], v[4:7], a[84:87], v[124:127]\n"
    "ds_read_b64_tr_b16 v[30:31], v233 offset:32384\n"
    "s_waitcnt lgkmcnt(8)\n"
    "v_mfma_f32_16x16x32_bf16 v[128:131], v[8:11], a[80:83], v[128:131]\n"
    "ds_read_b64_tr_b16 v[0:1], v233 offset:27808\n"
    "v_mul_u32_u24 v188, 18432, v188\n"
    "v_mfma_f32_16x16x32_bf16 v[132:135], v[8:11], a[84:87], v[132:135]\n"
    "ds_read_b64_tr_b16 v[2:3], v233 offset:32416\n"
    "v_mfma_f32_16x16x32_bf16 v[64:67], v[12:15], a[88:91], v[64:67]\n"
    "ds_read_b64_tr_b16 v[4:5], v233 offset:27840\n"
    "v_add3_u32 v186, v186, v188, v187\n"
    "v_mfma_f32_16x16x32_bf16 v[96:99], v[12:15], a[92:95], v[96:99]\n"
    "ds_read_b64_tr_b16 v[6:7], v233 offset:32448\n"
    "s_waitcnt lgkmcnt(8)\n"
    "v_mfma_f32_16x16x32_bf16 v[68:71], v[16:19], a[88:91], v[68:71]\n"
    "ds_read_b64_tr_b16 v[8:9], v233 offset:27872\n"
    "v_lshrrev_b32 v185, 20, v185\n"
    "v_mfma_f32_16x16x32_bf16 v[100:103], v[16:19], a[92:95], v[100:103]\n"
    "ds_read_b64_tr_b16 v[10:11], v233 offset:32480\n"
    "v_mfma_f32_16x16x32_bf16 v[72:75], v[20:23], a[88:91], v[72:75]\n"
    "ds_read_b64_tr_b16 v[12:13], v233 offset:27904\n"
    "v_mul_u32_u24 v185, 4608, v185\n"
    "v_mfma_f32_16x16x32_bf16 v[104:107], v[20:23], a[92:95], v[104:107]\n"
    "ds_read_b64_tr_b16 v[14:15], v233 offset:32512\n"
    "s_waitcnt vmcnt(10)\n"
    "s_barrier\n"
    "s_waitcnt lgkmcnt(8)\n"
    "v_mfma_f32_16x16x32_bf16 v[76:79], v[24:27], a[88:91], v[76:79]\n"
    "ds_read_b64_tr_b16 v[16:17], v233 offset:38016\n"
    "v_add_u32 v200, v185, v186\n"
    "v_mfma_f32_16x16x32_bf16 v[108:111], v[24:27], a[92:95], v[108:111]\n"
    "ds_read_b64_tr_b16 v[18:19], v233 offset:42624\n"
    "v_mfma_f32_16x16x32_bf16 v[80:83], v[28:31], a[88:91], v[80:83]\n"
    "s_add_u32 m0, s62, 0x1bd80\n"
    "s_add_u32 s63, s63, 0x24000\n"
    "ds_read_b64_tr_b16 v[20:21], v233 offset:38048\n"
    "ds_read_b64_tr_b16 v[22:23], v233 offset:42656\n"
    "v_mfma_f32_16x16x32_bf16 v[112:115], v[28:31], a[92:95], v[112:115]\n"
    "buffer_load_dwordx4 v236, s[32:35], s63 offen offset:0 lds\n"
    "s_waitcnt lgkmcnt(8)\n"
    "v_mfma_f32_16x16x32_bf16 v[84:87], v[0:3], a[88:91], v[84:87]\n"
    "ds_read_b64_tr_b16 v[24:25], v233 offset:38080\n"
    "v_add_u32 v185, 1864192, v184\n"
    "v_mfma_f32_16x16x32_bf16 v[116:119], v[0:3], a[92:95], v[116:119]\n"
    "ds_read_b64_tr_b16 v[26:27], v233 offset:42688\n"
    "v_mfma_f32_16x16x32_bf16 v[88:91], v[4:7], a[88:91], v[88:91]\n"
    "ds_read_b64_tr_b16 v[28:29], v233 offset:38112\n"
    "v_bfe_u32 v186, v185, 16, 3\n"
    "v_mfma_f32_16x16x32_bf16 v[120:123], v[4:7], a[92:95], v[120:123]\n"
    "ds_read_b64_tr_b16 v[30:31], v233 offset:42720\n"
    "s_waitcnt lgkmcnt(8)\n"
    "v_mfma_f32_16x16x32_bf16 v[92:95], v[8:11], a[88:91], v[92:95]\n"
    "ds_read_b64_tr_b16 v[0:1], v233 offset:38144\n"
    "ds_read_b64_tr_b16 v[2:3], v233 offset:42752\n"
    "v_mfma_f32_16x16x32_bf16 v[124:127], v[8:11], a[92:95], v[124:127]\n"
    "buffer_load_dwordx4 v237, s[32:35], s63 offen offset:1024 lds\n"
    "v_mfma_f32_16x16x32_bf16 v[128:131], v[12:15], a[88:91], v[128:131]\n"
    "ds_read_b64_tr_b16 v[4:5], v233 offset:38176\n"
    "v_mul_u32_u24 v186, 864, v186\n"
    "v_mfma_f32_16x16x32_bf16 v[132:135], v[12:15], a[92:95], v[132:135]\n"
    "ds_read_b64_tr_b16 v[6:7], v233 offset:42784\n"
    "s_waitcnt lgkmcnt(8)\n"
    "v_mfma_f32_16x16x32_bf16 v[64:67], v[16:19], a[96:99], v[64:67]\n"
    "ds_read_b64_tr_b16 v[8:9], v233 offset:38208\n"
    "v_bfe_u32 v188, v185, 19, 1\n"
    "v_mfma_f32_16x16x32_bf16 v[96:99], v[16:19], a[100:103], v[96:99]\n"
    "ds_read_b64_tr_b16 v[10:11], v233 offset:42816\n"
    "v_mfma_f32_16x16x32_bf16 v[68:71], v[20:23], a[96:99], v[68:71]\n"
    "ds_read_b64_tr_b16 v[12:13], v233 offset:38240\n"
    "ds_read_b64_tr_b16 v[14:15], v233 offset:42848\n"
    "v_mfma_f32_16x16x32_bf16 v[100:103], v[20:23], a[100:103], v[100:103]\n"
    "buffer_load_dwordx4 v238, s[32:35], s63 offen offset:2048 lds\n"
    "s_waitcnt lgkmcnt(8)\n"
    "v_mfma_f32_16x16x32_bf16 v[72:75], v[24:27], a[96:99], v[72:75]\n"
    "ds_read_b64_tr_b16 v[16:17], v233 offset:38272\n"
    "v_mul_u32_u24 v188, 18432, v188\n"
    "v_mfma_f32_16x16x32_bf16 v[104:107], v[24:27], a[100:103], v[104:107]\n"
    "ds_read_b64_tr_b16 v[18:19], v233 offset:42880\n"
    "v_mfma_f32_16x16x32_bf16 v[76:79], v[28:31], a[96:99], v[76:79]\n"
    "ds_read_b64_tr_b16 v[20:21], v233 offset:47232\n"
    "v_add_u32 v187, 8192, v246\n"
    "v_mfma_f32_16x16x32_bf16 v[108:111], v[28:31], a[100:103], v[108:111]\n"
    "ds_read_b64_tr_b16 v[22:23], v233 offset:51840\n"
    "s_waitcnt lgkmcnt(8)\n"
    "v_mfma_f32_16x16x32_bf16 v[80:83], v[0:3], a[96:99], v[80:83]\n"
    "ds_read_b64_tr_b16 v[24:25], v233 offset:47264\n"
    "ds_read_b64_tr_b16 v[26:27], v233 offset:51872\n"
    "v_mfma_f32_16x16x32_bf16 v[112:115], v[0:3], a[100:103], v[112:115]\n"
    "buffer_load_dwordx4 v239, s[32:35], s63 offen offset:3072 lds\n"
    "v_mfma_f32_16x16x32_bf16 v[84:87], v[4:7], a[96:99], v[84:87]\n"
    "ds_read_b64_tr_b16 v[28:29], v233 offset:47296\n"
    "v_add3_u32 v186, v186, v188, v187\n"
    "v_mfma_f32_16x16x32_bf16 v[116:119], v[4:7], a[100:103], v[116:119]\n"
    "ds_read_b64_tr_b16 v[30:31], v233 offset:51904\n"
    "s_waitcnt lgkmcnt(8)\n"
    "v_mfma_f32_16x16x32_bf16 v[88:91], v[8:11], a[96:99], v[88:91]\n"
    "ds_read_b64_tr_b16 v[0:1], v233 offset:47328\n"
    "v_lshrrev_b32 v185, 20, v185\n"
    "v_mfma_f32_16x16x32_bf16 v[120:123], v[8:11], a[100:103], v[120:123]\n"
    "ds_read_b64_tr_b16 v[2:3], v233 offset:51936\n"
    "v_mfma_f32_16x16x32_bf16 v[92:95], v[12:15], a[96:99], v[92:95]\n"
    "s_add_u32 m0, s62, 0x1cd80\n"
    "ds_read_b64_tr_b16 v[4:5], v233 offset:47360\n"
    "ds_read_b64_tr_b16 v[6:7], v233 offset:51968\n"
    "v_mfma_f32_16x16x32_bf16 v[124:127], v[12:15], a[100:103], v[124:127]\n"
    "buffer_load_dwordx4 v240, s[32:35], s63 offen offset:0 lds\n"
    "s_waitcnt lgkmcnt(8)\n"
    "v_mfma_f32_16x16x32_bf16 v[128:131], v[16:19], a[96:99], v[128:131]\n"
    "ds_read_b64_tr_b16 v[8:9], v233 offset:47392\n"
    "v_mul_u32_u24 v185, 4608, v185\n"
    "v_mfma_f32_16x16x32_bf16 v[132:135], v[16:19], a[100:103], v[132:135]\n"
    "ds_read_b64_tr_b16 v[10:11], v233 offset:52000\n"
    "v_mfma_f32_16x16x32_bf16 v[64:67], v[20:23], a[104:107], v[64:67]\n"
    "ds_read_b64_tr_b16 v[12:13], v233 offset:47424\n"
    "v_add_u32 v201, v185, v186\n"
    "v_mfma_f32_16x16x32_bf16 v[96:99], v[20:23], a[108:111], v[96:99]\n"
    "ds_read_b64_tr_b16 v[14:15], v233 offset:52032\n"
    "s_waitcnt lgkmcnt(8)\n"
    "v_mfma_f32_16x16x32_bf16 v[68:71], v[24:27], a[104:107], v[68:71]\n"
    "ds_read_b64_tr_b16 v[16:17], v233 offset:47456\n"
    "ds_read_b64_tr_b16 v[18:19], v233 offset:52064\n"
    "v_mfma_f32_16x16x32_bf16 v[100:103], v[24:27], a[108:111], v[100:103]\n"
    "buffer_load_dwordx4 v241, s[32:35], s63 offen offset:1024 lds\n"
    "v_mfma_f32_16x16x32_bf16 v[72:75], v[28:31], a[104:107], v[72:75]\n"
    "ds_read_b64_tr_b16 v[20:21], v233 offset:47488\n"
    "s_and_b32 s15, s64, 0xfc\n"
    "s_lshl_b32 s15, s15, 5\n"
    "s_add_u32 s15, s15, 5120\n"
    "v_and_b32 v225, 15, v192\n"
    "v_lshl_add_u32 v225, v225, 3, s15\n"
    "v_mfma_f32_16x16x32_bf16 v[104:107], v[28:31], a[108:111], v[104:107]\n"
    "ds_read_b64_tr_b16 v[22:23], v233 offset:52096\n"
    "v_lshrrev_b32 v248, 1, v246\n"
    "s_mul_i32 s15, s9, 83968\n"
    "s_add_u32 s15, s15, 40960\n"
    "v_add_u32 v248, s15, v248\n"
    "v_add_u32 v249, 41984, v248\n"
    "s_waitcnt lgkmcnt(8)\n"
    "v_mfma_f32_16x16x32_bf16 v[76:79], v[0:3], a[104:107], v[76:79]\n"
    "ds_read_b64_tr_b16 v[24:25], v233 offset:56448\n"
    "ds_read_b64_tr_b16 v[26:27], v233 offset:61056\n"
    "v_mfma_f32_16x16x32_bf16 v[108:111], v[0:3], a[108:111], v[108:111]\n"
    "global_load_dwordx2 v[226:227], v225, s[50:51] sc1\n"
    "v_mfma_f32_16x16x32_bf16 v[80:83], v[4:7], a[104:107], v[80:83]\n"
    "ds_read_b64_tr_b16 v[28:29], v233 offset:56480\n"
    "ds_read_b64_tr_b16 v[30:31], v233 offset:61088\n"
    "v_mfma_f32_16x16x32_bf16 v[112:115], v[4:7], a[108:111], v[112:115]\n"
    "buffer_load_dwordx4 v242, s[32:35], s63 offen offset:2048 lds\n"
    "s_waitcnt lgkmcnt(8)\n"
    "v_mfma_f32_16x16x32_bf16 v[84:87], v[8:11], a[104:107], v[84:87]\n"
    "ds_read_b64_tr_b16 v[0:1], v233 offset:56512\n"
    "v_add_u32 v184, 0, v192\n"
    "v_mfma_f32_16x16x32_bf16 v[116:119], v[8:11], a[108:111], v[116:119]\n"
    "ds_read_b64_tr_b16 v[2:3], v233 offset:61120\n"
    "v_mfma_f32_16x16x32_bf16 v[88:91], v[12:15], a[104:107], v[88:91]\n"
    "ds_read_b64_tr_b16 v[4:5], v233 offset:56544\n"
    "v_mul_u32_u24 v185, 3641, v184\n"
    "v_mfma_f32_16x16x32_bf16 v[120:123], v[12:15], a[108:111], v[120:123]\n"
    "ds_read_b64_tr_b16 v[6:7], v233 offset:61152\n"
    "s_waitcnt lgkmcnt(8)\n"
    "v_mfma_f32_16x16x32_bf16 v[92:95], v[16:19], a[104:107], v[92:95]\n"
    "ds_read_b64_tr_b16 v[8:9], v233 offset:56576\n"
    "ds_read_b64_tr_b16 v[10:11], v233 offset:61184\n"
    "v_mfma_f32_16x16x32_bf16 v[124:127], v[16:19], a[108:111], v[124:127]\n"
    "buffer_load_dwordx4 v243, s[32:35], s63 offen offset:3072 lds\n"
    "v_mfma_f32_16x16x32_bf16 v[128:131], v[20:23], a[104:107], v[128:131]\n"
    "ds_read_b64_tr_b16 v[12:13], v233 offset:56608\n"
    "v_lshrrev_b32 v185, 16, v185\n"
    "v_mfma_f32_16x16x32_bf16 v[132:135], v[20:23], a[108:111], v[132:135]\n"
    "ds_read_b64_tr_b16 v[14:15], v233 offset:61216\n"
    "s_waitcnt lgkmcnt(8)\n"
    "v_mfma_f32_16x16x32_bf16 v[64:67], v[24:27], a[112:115], v[64:67]\n"
    "ds_read_b64_tr_b16 v[16:17], v233 offset:56640\n"
    "v_mul_u32_u24 v186, 18, v185\n"
    "v_mfma_f32_16x16x32_bf16 v[96:99], v[24:27], a[116:119], v[96:99]\n"
    "ds_read_b64_tr_b16 v[18:19], v233 offset:61248\n"
    "v_mfma_f32_16x16x32_bf16 v[68:71], v[28:31], a[112:115], v[68:71]\n"
    "s_add_u32 m0, s62, 0x1dd80\n"
    "ds_read_b64_tr_b16 v[20:21], v233 offset:56672\n"
    "ds_read_b64_tr_b16 v[22:23], v233 offset:61280\n"
    "v_mfma_f32_16x16x32_bf16 v[100:103], v[28:31], a[116:119], v[100:103]\n"
    "buffer_load_dwordx4 v244, s[32:35], s63 offen offset:0 lds\n"
    "s_waitcnt lgkmcnt(8)\n"
    "v_mfma_f32_16x16x32_bf16 v[72:75], v[0:3], a[112:115], v[72:75]\n"
    "ds_read_b64_tr_b16 v[24:25], v233 offset:56704\n"
    "v_sub_u32 v184, v184, v186\n"
    "v_mfma_f32_16x16x32_bf16 v[104:107], v[0:3], a[116:119], v[104:107]\n"
    "ds_read_b64_tr_b16 v[26:27], v233 offset:61312\n"
    "v_mfma_f32_16x16x32_bf16 v[76:79], v[4:7], a[112:115], v[76:79]\n"
    "ds_read_b64_tr_b16 v[28:29], v234 offset:128\n"
    "v_lshrrev_b32 v186, 1, v185\n"
    "v_mfma_f32_16x16x32_bf16 v[108:111], v[4:7], a[116:119], v[108:111]\n"
    "ds_read_b64_tr_b16 v[30:31], v234 offset:4736\n"
    "s_waitcnt lgkmcnt(8)\n"
    "v_mfma_f32_16x16x32_bf16 v[80:83], v[8:11], a[112:115], v[80:83]\n"
    "ds_read_b64_tr_b16 v[0:1], v234 offset:160\n"
    "v_mul_u32_u24 v186, 18, v186\n"
    "v_mfma_f32_16x16x32_bf16 v[112:115], v[8:11], a[116:119], v[112:115]\n"
    "ds_read_b64_tr_b16 v[2:3], v234 offset:4768\n"
    "v_mfma_f32_16x16x32_bf16 v[84:87], v[12:15], a[112:115], v[84:87]\n"
    "ds_read_b64_tr_b16 v[4:5], v234 offset:192\n"
    "v_and_b32 v187, 1, v185\n"
    "v_mfma_f32_16x16x32_bf16 v[116:119], v[12:15], a[116:119], v[116:119]\n"
    "ds_read_b64_tr_b16 v[6:7], v234 offset:4800\n"
    "s_waitcnt lgkmcnt(8)\n"
    "v_mfma_f32_16x16x32_bf16 v[88:91], v[16:19], a[112:115], v[88:91]\n"
    "ds_read_b64_tr_b16 v[8:9], v234 offset:224\n"
    "s_mul_i32 s2, s9, 36\n"
    "v_mfma_f32_16x16x32_bf16 v[120:123], v[16:19], a[116:119], v[120:123]\n"
    "ds_read_b64_tr_b16 v[10:11], v234 offset:4832\n"
    "v_mfma_f32_16x16x32_bf16 v[92:95], v[20:23], a[112:115], v[92:95]\n"
    "ds_read_b64_tr_b16 v[12:13], v234 offset:256\n"
    "s_add_u32 s2, s2, 16\n"
    "v_mfma_f32_16x16x32_bf16 v[124:127], v[20:23], a[116:119], v[124:127]\n"
    "ds_read_b64_tr_b16 v[14:15], v234 offset:4864\n"
    "s_waitcnt lgkmcnt(8)\n"
    "v_mfma_f32_16x16x32_bf16 v[128:131], v[24:27], a[112:115], v[128:131]\n"
    "ds_read_b64_tr_b16 v[16:17], v234 offset:288\n"
    "v_add3_u32 v186, v186, v187, s2\n"
    "v_mfma_f32_16x16x32_bf16 v[132:135], v[24:27], a[116:119], v[132:135]\n"
    "ds_read_b64_tr_b16 v[18:19], v234 offset:4896\n"
    "v_mfma_f32_16x16x32_bf16 v[64:67], v[28:31], a[120:123], v[64:67]\n"
    "ds_read_b64_tr_b16 v[20:21], v234 offset:320\n"
    "v_mul_u32_u24 v186, 1152, v186\n"
    "v_mfma_f32_16x16x32_bf16 v[96:99], v[28:31], a[124:127], v[96:99]\n"
    "ds_read_b64_tr_b16 v[22:23], v234 offset:4928\n"
    "s_waitcnt lgkmcnt(8)\n"
    "v_mfma_f32_16x16x32_bf16 v[68:71], v[0:3], a[120:123], v[68:71]\n"
    "ds_read_b64_tr_b16 v[24:25], v234 offset:352\n"
    "v_lshl_add_u32 v186, v184, 4, v186\n"
    "v_mfma_f32_16x16x32_bf16 v[100:103], v[0:3], a[124:127], v[100:103]\n"
    "ds_read_b64_tr_b16 v[26:27], v234 offset:4960\n"
    "v_mfma_f32_16x16x32_bf16 v[72:75], v[4:7], a[120:123], v[72:75]\n"
    "ds_read_b64_tr_b16 v[28:29], v234 offset:384\n"
    "v_subrev_u32 v186, 0, v186\n"
    "v_mfma_f32_16x16x32_bf16 v[104:107], v[4:7], a[124:127], v[104:107]\n"
    "ds_read_b64_tr_b16 v[30:31], v234 offset:4992\n"
    "s_waitcnt vmcnt(10)\n"
    "s_barrier\n"
    "s_waitcnt lgkmcnt(8)\n"
    "v_mfma_f32_16x16x32_bf16 v[76:79], v[8:11], a[120:123], v[76:79]\n"
    "ds_read_b64_tr_b16 v[0:1], v234 offset:10496\n"
    "v_cmp_le_u32 vcc, 4, v185\n"
    "v_mfma_f32_16x16x32_bf16 v[108:111], v[8:11], a[124:127], v[108:111]\n"
    "ds_read_b64_tr_b16 v[2:3], v234 offset:15104\n"
    "v_mfma_f32_16x16x32_bf16 v[80:83], v[12:15], a[120:123], v[80:83]\n"
    "s_add_u32 m0, s62, 0x0\n"
    "s_add_u32 s63, s63, 0x24000\n"
    "ds_read_b64_tr_b16 v[4:5], v234 offset:10528\n"
    "ds_read_b64_tr_b16 v[6:7], v234 offset:15136\n"
    "v_mfma_f32_16x16x32_bf16 v[112:115], v[12:15], a[124:127], v[112:115]\n"
    "buffer_load_dwordx4 v236, s[32:35], s63 offen offset:0 lds\n"
    "s_waitcnt lgkmcnt(8)\n"
    "v_mfma_f32_16x16x32_bf16 v[84:87], v[16:19], a[120:123], v[84:87]\n"
    "ds_read_b64_tr_b16 v[8:9], v234 offset:10560\n"
    "v_mov_b32 v187, 0x80000000\n"
    "v_mfma_f32_16x16x32_bf16 v[116:119], v[16:19], a[124:127], v[116:119]\n"
    "ds_read_b64_tr_b16 v[10:11], v234 offset:15168\n"
    "v_mfma_f32_16x16x32_bf16 v[88:91], v[20:23], a[120:123], v[88:91]\n"
    "ds_read_b64_tr_b16 v[12:13], v234 offset:10592\n"
    "v_cndmask_b32 v202, v186, v187, vcc\n"
    "v_mfma_f32_16x16x32_bf16 v[120:123], v[20:23], a[124:127], v[120:123]\n"
    "ds_read_b64_tr_b16 v[14:15], v234 offset:15200\n"
    "s_waitcnt lgkmcnt(8)\n"
    "v_mfma_f32_16x16x32_bf16 v[92:95], v[24:27], a[120:123], v[92:95]\n"
    "ds_read_b64_tr_b16 v[16:17], v234 offset:10624\n"
    "ds_read_b64_tr_b16 v[18:19], v234 offset:15232\n"
    "v_mfma_f32_16x16x32_bf16 v[124:127], v[24:27], a[124:127], v[124:127]\n"
    "buffer_load_dwordx4 v237, s[32:35], s63 offen offset:1024 lds\n"
    "v_mfma_f32_16x16x32_bf16 v[128:131], v[28:31], a[120:123], v[128:131]\n"
    "ds_read_b64_tr_b16 v[20:21], v234 offset:10656\n"
    "v_add_u32 v184, 64, v192\n"
    "v_mfma_f32_16x16x32_bf16 v[132:135], v[28:31], a[124:127], v[132:135]\n"
    "ds_read_b64_tr_b16 v[22:23], v234 offset:15264\n"
    "s_waitcnt lgkmcnt(8)\n"
    "v_mfma_f32_16x16x32_bf16 v[64:67], v[0:3], a[128:131], v[64:67]\n"
    "ds_read_b64_tr_b16 v[24:25], v234 offset:10688\n"
    "v_mul_u32_u24 v185, 3641, v184\n"
    "v_mfma_f32_16x16x32_bf16 v[96:99], v[0:3], a[132:135], v[96:99]\n"
    "ds_read_b64_tr_b16 v[26:27], v234 offset:15296\n"
    "v_mfma_f32_16x16x32_bf16 v[68:71], v[4:7], a[128:131], v[68:71]\n"
    "ds_read_b64_tr_b16 v[28:29], v234 offset:10720\n"
    "ds_read_b64_tr_b16 v[30:31], v234 offset:15328\n"
    "v_mfma_f32_16x16x32_bf16 v[100:103], v[4:7], a[132:135], v[100:103]\n"
    "buffer_load_dwordx4 v238, s[32:35], s63 offen offset:2048 lds\n"
    "s_waitcnt lgkmcnt(8)\n"
    "v_mfma_f32_16x16x32_bf16 v[72:75], v[8:11], a[128:131], v[72:75]\n"
    "ds_read_b64_tr_b16 v[0:1], v234 offset:10752\n"
    "v_lshrrev_b32 v185, 16, v185\n"
    "v_mfma_f32_16x16x32_bf16 v[104:107], v[8:11], a[132:135], v[104:107]\n"
    "ds_read_b64_tr_b16 v[2:3], v234 offset:15360\n"
    "v_mfma_f32_16x16x32_bf16 v[76:79], v[12:15], a[128:131], v[76:79]\n"
    "ds_read_b64_tr_b16 v[4:5], v234 offset:19712\n"
    "v_mul_u32_u24 v186, 18, v185\n"
    "v_mfma_f32_16x16x32_bf16 v[108:111], v[12:15], a[132:135], v[108:111]\n"
    "ds_read_b64_tr_b16 v[6:7], v234 offset:24320\n"
    "s_waitcnt lgkmcnt(8)\n"
    "v_mfma_f32_16x16x32_bf16 v[80:83], v[16:19], a[128:131], v[80:83]\n"
    "ds_read_b64_tr_b16 v[8:9], v234 offset:19744\n"
    "ds_read_b64_tr_b16 v[10:11], v234 offset:24352\n"
    "v_mfma_f32_16x16x32_bf16 v[112:115], v[16:19], a[132:135], v[112:115]\n"
    "buffer_load_dwordx4 v239, s[32:35], s63 offen offset:3072 lds\n"
    "v_mfma_f32_16x16x32_bf16 v[84:87], v[20:23], a[128:131], v[84:87]\n"
    "ds_read_b64_tr_b16 v[12:13], v234 offset:19776\n"
    "v_sub_u32 v184, v184, v186\n"
    "v_mfma_f32_16x16x32_bf16 v[116:119], v[20:23], a[132:135], v[116:119]\n"
    "ds_read_b64_tr_b16 v[14:15], v234 offset:24384\n"
    "s_waitcnt lgkmcnt(8)\n"
    "v_mfma_f32_16x16x32_bf16 v[88:91], v[24:27], a[128:131], v[88:91]\n"
    "ds_read_b64_tr_b16 v[16:17], v234 offset:19808\n"
    "v_lshrrev_b32 v186, 1, v185\n"
    "v_mfma_f32_16x16x32_bf16 v[120:123], v[24:27], a[132:135], v[120:123]\n"
    "ds_read_b64_tr_b16 v[18:19], v234 offset:24416\n"
    "v_mfma_f32_16x16x32_bf16 v[92:95], v[28:31], a[128:131], v[92:95]\n"
    "s_add_u32 m0, s62, 0x1000\n"
    "ds_read_b64_tr_b16 v[20:21], v234 offset:19840\n"
    "ds_read_b64_tr_b16 v[22:23], v234 offset:24448\n"
    "v_mfma_f32_16x16x32_bf16 v[124:127], v[28:31], a[132:135], v[124:127]\n"
    "buffer_load_dwordx4 v240, s[32:35], s63 offen offset:0 lds\n"
    "s_waitcnt lgkmcnt(8)\n"
    "v_mfma_f32_16x16x32_bf16 v[128:131], v[0:3], a[128:131], v[128:131]\n"
    "ds_read_b64_tr_b16 v[24:25], v234 offset:19872\n"
    "v_mul_u32_u24 v186, 18, v186\n"
    "v_mfma_f32_16x16x32_bf16 v[132:135], v[0:3], a[132:135], v[132:135]\n"
    "ds_read_b64_tr_b16 v[26:27], v234 offset:24480\n"
    "v_mfma_f32_16x16x32_bf16 v[64:67], v[4:7], a[136:139], v[64:67]\n"
    "ds_read_b64_tr_b16 v[28:29], v234 offset:19904\n"
    "v_and_b32 v187, 1, v185\n"
    "v_mfma_f32_16x16x32_bf16 v[96:99], v[4:7], a[140:143], v[96:99]\n"
    "ds_read_b64_tr_b16 v[30:31], v234 offset:24512\n"
    "s_waitcnt lgkmcnt(8)\n"
    "v_mfma_f32_16x16x32_bf16 v[68:71], v[8:11], a[136:139], v[68:71]\n"
    "ds_read_b64_tr_b16 v[0:1], v234 offset:19936\n"
    "ds_read_b64_tr_b16 v[2:3], v234 offset:24544\n"
    "v_mfma_f32_16x16x32_bf16 v[100:103], v[8:11], a[140:143], v[100:103]\n"
    "buffer_load_dwordx4 v241, s[32:35], s63 offen offset:1024 lds\n"
    "v_mfma_f32_16x16x32_bf16 v[72:75], v[12:15], a[136:139], v[72:75]\n"
    "ds_read_b64_tr_b16 v[4:5], v234 offset:19968\n"
    "s_mul_i32 s2, s9, 36\n"
    "v_mfma_f32_16x16x32_bf16 v[104:107], v[12:15], a[140:143], v[104:107]\n"
    "ds_read_b64_tr_b16 v[6:7], v234 offset:24576\n"
    "s_waitcnt lgkmcnt(8)\n"
    "v_mfma_f32_16x16x32_bf16 v[76:79], v[16:19], a[136:139], v[76:79]\n"
    "ds_read_b64_tr_b16 v[8:9], v234 offset:28928\n"
    "s_add_u32 s2, s2, 16\n"
    "v_mfma_f32_16x16x32_bf16 v[108:111], v[16:19], a[140:143], v[108:111]\n"
    "ds_read_b64_tr_b16 v[10:11], v234 offset:33536\n"
    "v_mfma_f32_16x16x32_bf16 v[80:83], v[20:23], a[136:139], v[80:83]\n"
    "ds_read_b64_tr_b16 v[12:13], v234 offset:28960\n"
    "ds_read_b64_tr_b16 v[14:15], v234 offset:33568\n"
    "v_mfma_f32_16x16x32_bf16 v[112:115], v[20:23], a[140:143], v[112:115]\n"
    "buffer_load_dwordx4 v242, s[32:35], s63 offen offset:2048 lds\n"
    "s_waitcnt lgkmcnt(8)\n"
    "v_mfma_f32_16x16x32_bf16 v[84:87], v[24:27], a[136:139], v[84:87]\n"
    "ds_read_b64_tr_b16 v[16:17], v234 offset:28992\n"
    "v_add3_u32 v186, v186, v187, s2\n"
    "v_mfma_f32_16x16x32_bf16 v[116:119], v[24:27], a[140:143], v[116:119]\n"
    "ds_read_b64_tr_b16 v[18:19], v234 offset:33600\n"
    "v_mfma_f32_16x16x32_bf16 v[88:91], v[28:31], a[136:139], v[88:91]\n"
    "ds_read_b64_tr_b16 v[20:21], v234 offset:29024\n"
    "v_mul_u32_u24 v186, 1152, v186\n"
    "v_mfma_f32_16x16x32_bf16 v[120:123], v[28:31], a[140:143], v[120:123]\n"
    "ds_read_b64_tr_b16 v[22:23], v234 offset:33632\n"
    "s_waitcnt lgkmcnt(8)\n"
    "v_mfma_f32_16x16x32_bf16 v[92:95], v[0:3], a[136:139], v[92:95]\n"
    "ds_read_b64_tr_b16 v[24:25], v234 offset:29056\n"
    "ds_read_b64_tr_b16 v[26:27], v234 offset:33664\n"
    "v_mfma_f32_16x16x32_bf16 v[124:127], v[0:3], a[140:143], v[124:127]\n"
    "buffer_load_dwordx4 v243, s[32:35], s63 offen offset:3072 lds\n"
    "v_mfma_f32_16x16x32_bf16 v[128:131], v[4:7], a[136:139], v[128:131]\n"
    "ds_read_b64_tr_b16 v[28:29], v234 offset:29088\n"
    "v_lshl_add_u32 v186, v184, 4, v186\n"
    "v_mfma_f32_16x16x32_bf16 v[132:135], v[4:7], a[140:143], v[132:135]\n"
    "ds_read_b64_tr_b16 v[30:31], v234 offset:33696\n"
    "s_waitcnt lgkmcnt(8)\n"
    "v_mfma_f32_16x16x32_bf16 v[64:67], v[8:11], a[144:147], v[64:67]\n"
    "ds_read_b64_tr_b16 v[0:1], v234 offset:29120\n"
    "v_subrev_u32 v186, 1024, v186\n"
    "v_mfma_f32_16x16x32_bf16 v[96:99], v[8:11], a[148:151], v[96:99]\n"
    "ds_read_b64_tr_b16 v[2:3], v234 offset:33728\n"
    "v_mfma_f32_16x16x32_bf16 v[68:71], v[12:15], a[144:147], v[68:71]\n"
    "s_add_u32 m0, s62, 0x2000\n"
    "ds_read_b64_tr_b16 v[4:5], v234 offset:29152\n"
    "ds_read_b64_tr_b16 v[6:7], v234 offset:33760\n"
    "v_mfma_f32_16x16x32_bf16 v[100:103], v[12:15], a[148:151], v[100:103]\n"
    "buffer_load_dwordx4 v244, s[32:35], s63 offen offset:0 lds\n"
    "s_waitcnt lgkmcnt(8)\n"
    "v_mfma_f32_16x16x32_bf16 v[72:75], v[16:19], a[144:147], v[72:75]\n"
    "ds_read_b64_tr_b16 v[8:9], v234 offset:29184\n"
    "v_cmp_le_u32 vcc, 4, v185\n"
    "v_mfma_f32_16x16x32_bf16 v[104:107], v[16:19], a[148:151], v[104:107]\n"
    "ds_read_b64_tr_b16 v[10:11], v234 offset:33792\n"
    "v_mfma_f32_16x16x32_bf16 v[76:79], v[20:23], a[144:147], v[76:79]\n"
    "ds_read_b64_tr_b16 v[12:13], v234 offset:38144\n"
    "v_mov_b32 v187, 0x80000000\n"
    "v_mfma_f32_16x16x32_bf16 v[108:111], v[20:23], a[148:151], v[108:111]\n"
    "ds_read_b64_tr_b16 v[14:15], v234 offset:42752\n"
    "s_waitcnt lgkmcnt(8)\n"
    "v_mfma_f32_16x16x32_bf16 v[80:83], v[24:27], a[144:147], v[80:83]\n"
    "ds_read_b64_tr_b16 v[16:17], v234 offset:38176\n"
    "v_cndmask_b32 v203, v186, v187, vcc\n"
    "v_mfma_f32_16x16x32_bf16 v[112:115], v[24:27], a[148:151], v[112:115]\n"
    "ds_read_b64_tr_b16 v[18:19], v234 offset:42784\n"
    "v_mfma_f32_16x16x32_bf16 v[84:87], v[28:31], a[144:147], v[84:87]\n"
    "ds_read_b64_tr_b16 v[20:21], v234 offset:38208\n"
    "v_lshrrev_b32 v184, 4, v192\n"
    "v_mfma_f32_16x16x32_bf16 v[116:119], v[28:31], a[148:151], v[116:119]\n"
    "ds_read_b64_tr_b16 v[22:23], v234 offset:42816\n"
    "s_waitcnt lgkmcnt(8)\n"
    "v_mfma_f32_16x16x32_bf16 v[88:91], v[0:3], a[144:147], v[88:91]\n"
    "ds_read_b64_tr_b16 v[24:25], v234 offset:38240\n"
    "v_mul_u32_u24 v184, 2176, v184\n"
    "v_mfma_f32_16x16x32_bf16 v[120:123], v[0:3], a[148:151], v[120:123]\n"
    "ds_read_b64_tr_b16 v[26:27], v234 offset:42848\n"
    "v_mfma_f32_16x16x32_bf16 v[92:95], v[4:7], a[144:147], v[92:95]\n"
    "ds_read_b64_tr_b16 v[28:29], v234 offset:38272\n"
    "v_bfe_u32 v185, v192, 2, 2\n"
    "v_mfma_f32_16x16x32_bf16 v[124:127], v[4:7], a[148:151], v[124:127]\n"
    "ds_read_b64_tr_b16 v[30:31], v234 offset:42880\n"
    "s_waitcnt lgkmcnt(8)\n"
    "v_mfma_f32_16x16x32_bf16 v[128:131], v[8:11], a[144:147], v[128:131]\n"
    "ds_read_b64_tr_b16 v[0:1], v234 offset:38304\n"
    "v_mul_u32_u24 v185, 288, v185\n"
    "v_mfma_f32_16x16x32_bf16 v[132:135], v[8:11], a[148:151], v[132:135]\n"
    "ds_read_b64_tr_b16 v[2:3], v234 offset:42912\n"
    "v_mfma_f32_16x16x32_bf16 v[64:67], v[12:15], a[152:155], v[64:67]\n"
    "ds_read_b64_tr_b16 v[4:5], v234 offset:38336\n"
    "v_and_b32 v186, 3, v192\n"
    "v_mfma_f32_16x16x32_bf16 v[96:99], v[12:15], a[156:159], v[96:99]\n"
    "ds_read_b64_tr_b16 v[6:7], v234 offset:42944\n"
    "s_waitcnt lgkmcnt(8)\n"
    "v_mfma_f32_16x16x32_bf16 v[68:71], v[16:19], a[152:155], v[68:71]\n"
    "ds_read_b64_tr_b16 v[8:9], v234 offset:38368\n"
    "v_lshl_add_u32 v184, v186, 3, v184\n"
    "v_mfma_f32_16x16x32_bf16 v[100:103], v[16:19], a[156:159], v[100:103]\n"
    "ds_read_b64_tr_b16 v[10:11], v234 offset:42976\n"
    "v_mfma_f32_16x16x32_bf16 v[72:75], v[20:23], a[152:155], v[72:75]\n"
    "ds_read_b64_tr_b16 v[12:13], v234 offset:38400\n"
    "v_add_u32 v206, v184, v185\n"
    "v_mfma_f32_16x16x32_bf16 v[104:107], v[20:23], a[156:159], v[104:107]\n"
    "ds_read_b64_tr_b16 v[14:15], v234 offset:43008\n"
    "s_waitcnt vmcnt(9)\n"
    "s_barrier\n"
    "s_waitcnt lgkmcnt(8)\n"
    "v_mfma_f32_16x16x32_bf16 v[76:79], v[24:27], a[152:155], v[76:79]\n"
    "ds_read_b64_tr_b16 v[16:17], v234 offset:48512\n"
    "v_and_b32 v184, 15, v192\n"
    "v_mfma_f32_16x16x32_bf16 v[108:111], v[24:27], a[156:159], v[108:111]\n"
    "ds_read_b64_tr_b16 v[18:19], v234 offset:53120\n"
    "v_mfma_f32_16x16x32_bf16 v[80:83], v[28:31], a[152:155], v[80:83]\n"
    "s_add_u32 m0, s62, 0x9480\n"
    "s_add_u32 s63, s63, 0x24000\n"
    "ds_read_b64_tr_b16 v[20:21], v234 offset:48544\n"
    "ds_read_b64_tr_b16 v[22:23], v234 offset:53152\n"
    "v_mfma_f32_16x16x32_bf16 v[112:115], v[28:31], a[156:159], v[112:115]\n"
    "buffer_load_dwordx4 v236, s[32:35], s63 offen offset:0 lds\n"
    "s_waitcnt lgkmcnt(8)\n"
    "v_mfma_f32_16x16x32_bf16 v[84:87], v[0:3], a[152:155], v[84:87]\n"
    "ds_read_b64_tr_b16 v[24:25], v234 offset:48576\n"
    "s_lshl_b32 s2, s9, 7\n"
    "v_mfma_f32_16x16x32_bf16 v[116:119], v[0:3], a[156:159], v[116:119]\n"
    "ds_read_b64_tr_b16 v[26:27], v234 offset:53184\n"
    "v_mfma_f32_16x16x32_bf16 v[88:91], v[4:7], a[152:155], v[88:91]\n"
    "ds_read_b64_tr_b16 v[28:29], v234 offset:48608\n"
    "s_add_u32 s2, s2, 162560\n"
    "v_mfma_f32_16x16x32_bf16 v[120:123], v[4:7], a[156:159], v[120:123]\n"
    "ds_read_b64_tr_b16 v[30:31], v234 offset:53216\n"
    "s_waitcnt lgkmcnt(8)\n"
    "v_mfma_f32_16x16x32_bf16 v[92:95], v[8:11], a[152:155], v[92:95]\n"
    "ds_read_b64_tr_b16 v[0:1], v234 offset:48640\n"
    "ds_read_b64_tr_b16 v[2:3], v234 offset:53248\n"
    "v_mfma_f32_16x16x32_bf16 v[124:127], v[8:11], a[156:159], v[124:127]\n"
    "buffer_load_dwordx4 v237, s[32:35], s63 offen offset:1024 lds\n"
    "v_mfma_f32_16x16x32_bf16 v[128:131], v[12:15], a[152:155], v[128:131]\n"
    "ds_read_b64_tr_b16 v[4:5], v234 offset:48672\n"
    "v_lshl_add_u32 v205, v184, 2, s2\n"
    "v_mfma_f32_16x16x32_bf16 v[132:135], v[12:15], a[156:159], v[132:135]\n"
    "ds_read_b64_tr_b16 v[6:7], v234 offset:53280\n"
    "s_waitcnt lgkmcnt(8)\n"
    "v_mfma_f32_16x16x32_bf16 v[64:67], v[16:19], a[160:163], v[64:67]\n"
    "ds_read_b64_tr_b16 v[8:9], v234 offset:48704\n"
    "v_mfma_f32_16x16x32_bf16 v[96:99], v[16:19], a[164:167], v[96:99]\n"
    "ds_read_b64_tr_b16 v[10:11], v234 offset:53312\n"
    "v_mfma_f32_16x16x32_bf16 v[68:71], v[20:23], a[160:163], v[68:71]\n"
    "ds_read_b64_tr_b16 v[12:13], v234 offset:48736\n"
    "ds_read_b64_tr_b16 v[14:15], v234 offset:53344\n"
    "v_mfma_f32_16x16x32_bf16 v[100:103], v[20:23], a[164:167], v[100:103]\n"
    "buffer_load_dwordx4 v238, s[32:35], s63 offen offset:2048 lds\n"
    "s_waitcnt lgkmcnt(8)\n"
    "v_mfma_f32_16x16x32_bf16 v[72:75], v[24:27], a[160:163], v[72:75]\n"
    "ds_read_b64_tr_b16 v[16:17], v234 offset:48768\n"
    "v_mfma_f32_16x16x32_bf16 v[104:107], v[24:27], a[164:167], v[104:107]\n"
    "ds_read_b64_tr_b16 v[18:19], v234 offset:53376\n"
    "v_mfma_f32_16x16x32_bf16 v[76:79], v[28:31], a[160:163], v[76:79]\n"
    "ds_read_b64_tr_b16 v[20:21], v234 offset:57728\n"
    "v_mfma_f32_16x16x32_bf16 v[108:111], v[28:31], a[164:167], v[108:111]\n"
    "ds_read_b64_tr_b16 v[22:23], v234 offset:62336\n"
    "s_waitcnt lgkmcnt(8)\n"
    "v_mfma_f32_16x16x32_bf16 v[80:83], v[0:3], a[160:163], v[80:83]\n"
    "ds_read_b64_tr_b16 v[24:25], v234 offset:57760\n"
    "ds_read_b64_tr_b16 v[26:27], v234 offset:62368\n"
    "v_mfma_f32_16x16x32_bf16 v[112:115], v[0:3], a[164:167], v[112:115]\n"
    "buffer_load_dwordx4 v239, s[32:35], s63 offen offset:3072 lds\n"
    "v_mfma_f32_16x16x32_bf16 v[84:87], v[4:7], a[160:163], v[84:87]\n"
    "ds_read_b64_tr_b16 v[28:29], v234 offset:57792\n"
    "v_mfma_f32_16x16x32_bf16 v[116:119], v[4:7], a[164:167], v[116:119]\n"
    "ds_read_b64_tr_b16 v[30:31], v234 offset:62400\n"
    "s_waitcnt lgkmcnt(8)\n"
    "v_mfma_f32_16x16x32_bf16 v[88:91], v[8:11], a[160:163], v[88:91]\n"
    "ds_read_b64_tr_b16 v[0:1], v234 offset:57824\n"
    "v_mfma_f32_16x16x32_bf16 v[120:123], v[8:11], a[164:167], v[120:123]\n"
    "ds_read_b64_tr_b16 v[2:3], v234 offset:62432\n"
    "v_mfma_f32_16x16x32_bf16 v[92:95], v[12:15], a[160:163], v[92:95]\n"
    "s_add_u32 m0, s62, 0xa480\n"
    "ds_read_b64_tr_b16 v[4:5], v234 offset:57856\n"
    "ds_read_b64_tr_b16 v[6:7], v234 offset:62464\n"
    "v_mfma_f32_16x16x32_bf16 v[124:127], v[12:15], a[164:167], v[124:127]\n"
    "buffer_load_dwordx4 v240, s[32:35], s63 offen offset:0 lds\n"
    "s_waitcnt lgkmcnt(8)\n"
    "v_mfma_f32_16x16x32_bf16 v[128:131], v[16:19], a[160:163], v[128:131]\n"
    "ds_read_b64_tr_b16 v[8:9], v234 offset:57888\n"
    "v_mfma_f32_16x16x32_bf16 v[132:135], v[16:19], a[164:167], v[132:135]\n"
    "ds_read_b64_tr_b16 v[10:11], v234 offset:62496\n"
    "v_mfma_f32_16x16x32_bf16 v[64:67], v[20:23], a[168:171], v[64:67]\n"
    "ds_read_b64_tr_b16 v[12:13], v234 offset:57920\n"
    "v_mfma_f32_16x16x32_bf16 v[96:99], v[20:23], a[172:175], v[96:99]\n"
    "ds_read_b64_tr_b16 v[14:15], v234 offset:62528\n"
    "s_waitcnt lgkmcnt(8)\n"
    "v_mfma_f32_16x16x32_bf16 v[68:71], v[24:27], a[168:171], v[68:71]\n"
    "ds_read_b64_tr_b16 v[16:17], v234 offset:57952\n"
    "ds_read_b64_tr_b16 v[18:19], v234 offset:62560\n"
    "v_mfma_f32_16x16x32_bf16 v[100:103], v[24:27], a[172:175], v[100:103]\n"
    "buffer_load_dwordx4 v241, s[32:35], s63 offen offset:1024 lds\n"
    "v_mfma_f32_16x16x32_bf16 v[72:75], v[28:31], a[168:171], v[72:75]\n"
    "ds_read_b64_tr_b16 v[20:21], v234 offset:57984\n"
    "v_mfma_f32_16x16x32_bf16 v[104:107], v[28:31], a[172:175], v[104:107]\n"
    "ds_read_b64_tr_b16 v[22:23], v234 offset:62592\n"
    "s_waitcnt lgkmcnt(8)\n"
    "v_mfma_f32_16x16x32_bf16 v[76:79], v[0:3], a[168:171], v[76:79]\n"
    "ds_read_b64_tr_b16 v[24:25], v235 offset:1408\n"
    "v_mfma_f32_16x16x32_bf16 v[108:111], v[0:3], a[172:175], v[108:111]\n"
    "ds_read_b64_tr_b16 v[26:27], v235 offset:6016\n"
    "v_mfma_f32_16x16x32_bf16 v[80:83], v[4:7], a[168:171], v[80:83]\n"
    "ds_read_b64_tr_b16 v[28:29], v235 offset:1440\n"
    "ds_read_b64_tr_b16 v[30:31], v235 offset:6048\n"
    "v_mfma_f32_16x16x32_bf16 v[112:115], v[4:7], a[172:175], v[112:115]\n"
    "buffer_load_dwordx4 v242, s[32:35], s63 offen offset:2048 lds\n"
    "s_waitcnt lgkmcnt(8)\n"
    "v_mfma_f32_16x16x32_bf16 v[84:87], v[8:11], a[168:171], v[84:87]\n"
    "ds_read_b64_tr_b16 v[0:1], v235 offset:1472\n"
    "v_mfma_f32_16x16x32_bf16 v[116:119], v[8:11], a[172:175], v[116:119]\n"
    "ds_read_b64_tr_b16 v[2:3], v235 offset:6080\n"
    "v_mfma_f32_16x16x32_bf16 v[88:91], v[12:15], a[168:171], v[88:91]\n"
    "ds_read_b64_tr_b16 v[4:5], v235 offset:1504\n"
    "v_mfma_f32_16x16x32_bf16 v[120:123], v[12:15], a[172:175], v[120:123]\n"
    "ds_read_b64_tr_b16 v[6:7], v235 offset:6112\n"
    "s_waitcnt lgkmcnt(8)\n"
    "v_mfma_f32_16x16x32_bf16 v[92:95], v[16:19], a[168:171], v[92:95]\n"
    "ds_read_b64_tr_b16 v[8:9], v235 offset:1536\n"
    "ds_read_b64_tr_b16 v[10:11], v235 offset:6144\n"
    "v_mfma_f32_16x16x32_bf16 v[124:127], v[16:19], a[172:175], v[124:127]\n"
    "buffer_load_dwordx4 v243, s[32:35], s63 offen offset:3072 lds\n"
    "v_mfma_f32_16x16x32_bf16 v[128:131], v[20:23], a[168:171], v[128:131]\n"
    "ds_read_b64_tr_b16 v[12:13], v235 offset:1568\n"
    "v_mfma_f32_16x16x32_bf16 v[132:135], v[20:23], a[172:175], v[132:135]\n"
    "ds_read_b64_tr_b16 v[14:15], v235 offset:6176\n"
    "s_waitcnt lgkmcnt(8)\n"
    "v_mfma_f32_16x16x32_bf16 v[64:67], v[24:27], a[176:179], v[64:67]\n"
    "ds_read_b64_tr_b16 v[16:17], v235 offset:1600\n"
    "v_mfma_f32_16x16x32_bf16 v[96:99], v[24:27], a[180:183], v[96:99]\n"
    "ds_read_b64_tr_b16 v[18:19], v235 offset:6208\n"
    "v_mfma_f32_16x16x32_bf16 v[68:71], v[28:31], a[176:179], v[68:71]\n"
    "s_add_u32 m0, s62, 0xb480\n"
    "ds_read_b64_tr_b16 v[20:21], v235 offset:1632\n"
    "ds_read_b64_tr_b16 v[22:23], v235 offset:6240\n"
    "v_mfma_f32_16x16x32_bf16 v[100:103], v[28:31], a[180:183], v[100:103]\n"
    "buffer_load_dwordx4 v244, s[32:35], s63 offen offset:0 lds\n"
    "s_waitcnt lgkmcnt(8)\n"
    "v_mfma_f32_16x16x32_bf16 v[72:75], v[0:3], a[176:179], v[72:75]\n"
    "ds_read_b64_tr_b16 v[24:25], v235 offset:1664\n"
    "v_mfma_f32_16x16x32_bf16 v[104:107], v[0:3], a[180:183], v[104:107]\n"
    "ds_read_b64_tr_b16 v[26:27], v235 offset:6272\n"
    "v_cmp_ne_u64 s[54:55], s[48:49], v[226:227]\n"
    "s_nop 1\n"
    "s_cmp_eq_u64 s[54:55], 0\n"
    "s_cbranch_scc0 .Lpoll2\n"
    ".Lpollok2:\n"
    "v_mfma_f32_16x16x32_bf16 v[76:79], v[4:7], a[176:179], v[76:79]\n"
    "ds_read_b64_tr_b16 v[28:29], v235 offset:10624\n"
    "v_mfma_f32_16x16x32_bf16 v[108:111], v[4:7], a[180:183], v[108:111]\n"
    "ds_read_b64_tr_b16 v[30:31], v235 offset:15232\n"
    "s_waitcnt lgkmcnt(8)\n"
    "v_mfma_f32_16x16x32_bf16 v[80:83], v[8:11], a[176:179], v[80:83]\n"
    "ds_read_b64_tr_b16 v[0:1], v235 offset:10656\n"
    "v_mfma_f32_16x16x32_bf16 v[112:115], v[8:11], a[180:183], v[112:115]\n"
    "ds_read_b64_tr_b16 v[2:3], v235 offset:15264\n"
    "v_mfma_f32_16x16x32_bf16 v[84:87], v[12:15], a[176:179], v[84:87]\n"
    "s_mov_b32 s72, 36864\n"
    "ds_read_b64_tr_b16 v[4:5], v235 offset:10688\n"
    "ds_read_b64_tr_b16 v[6:7], v235 offset:15296\n"
    "v_mfma_f32_16x16x32_bf16 v[116:119], v[12:15], a[180:183], v[116:119]\n"
    "buffer_load_dwordx4 a[0:3], v231, s[24:27], s72 offen offset:0 sc1\n"
    "s_waitcnt lgkmcnt(8)\n"
    "v_mfma_f32_16x16x32_bf16 v[88:91], v[16:19], a[176:179], v[88:91]\n"
    "ds_read_b64_tr_b16 v[8:9], v235 offset:10720\n"
    "v_mfma_f32_16x16x32_bf16 v[120:123], v[16:19], a[180:183], v[120:123]\n"
    "ds_read_b64_tr_b16 v[10:11], v235 offset:15328\n"
    "v_mfma_f32_16x16x32_bf16 v[92:95], v[20:23], a[176:179], v[92:95]\n"
    "ds_read_b64_tr_b16 v[12:13], v235 offset:10752\n"
    "v_mfma_f32_16x16x32_bf16 v[124:127], v[20:23], a[180:183], v[124:127]\n"
    "ds_read_b64_tr_b16 v[14:15], v235 offset:15360\n"
    "s_waitcnt lgkmcnt(8)\n"
    "v_mfma_f32_16x16x32_bf16 v[128:131], v[24:27], a[176:179], v[128:131]\n"
    "ds_read_b64_tr_b16 v[16:17], v235 offset:10784\n"
    "ds_read_b64_tr_b16 v[18:19], v235 offset:15392\n"
    "v_mfma_f32_16x16x32_bf16 v[132:135], v[24:27], a[180:183], v[132:135]\n"
    "buffer_load_dwordx4 a[4:7], v232, s[24:27], s72 offen offset:0 sc1\n"
    "v_mfma_f32_16x16x32_bf16 v[64:67], v[28:31], a[184:187], v[64:67]\n"
    "ds_read_b64_tr_b16 v[20:21], v235 offset:10816\n"
    "v_mfma_f32_16x16x32_bf16 v[96:99], v[28:31], a[188:191], v[96:99]\n"
    "ds_read_b64_tr_b16 v[22:23], v235 offset:15424\n"
    "s_waitcnt lgkmcnt(8)\n"
    "v_mfma_f32_16x16x32_bf16 v[68:71], v[0:3], a[184:187], v[68:71]\n"
    "ds_read_b64_tr_b16 v[24:25], v235 offset:10848\n"
    "v_mfma_f32_16x16x32_bf16 v[100:103], v[0:3], a[188:191], v[100:103]\n"
    "ds_read_b64_tr_b16 v[26:27], v235 offset:15456\n"
    "v_mfma_f32_16x16x32_bf16 v[72:75], v[4:7], a[184:187], v[72:75]\n"
    "ds_read_b64_tr_b16 v[28:29], v235 offset:10880\n"
    "ds_read_b64_tr_b16 v[30:31], v235 offset:15488\n"
    "v_mfma_f32_16x16x32_bf16 v[104:107], v[4:7], a[188:191], v[104:107]\n"
    "buffer_load_dwordx4 a[8:11], v231, s[24:27], s72 offen offset:1024 sc1\n"
    "s_waitcnt vmcnt(12)\n"
    "s_barrier\n"
    "s_waitcnt lgkmcnt(8)\n"
    "v_mfma_f32_16x16x32_bf16 v[76:79], v[8:11], a[184:187], v[76:79]\n"
    "ds_read_b64_tr_b16 v[0:1], v233 offset:0\n"
    "v_mfma_f32_16x16x32_bf16 v[108:111], v[8:11], a[188:191], v[108:111]\n"
    "ds_read_b64_tr_b16 v[2:3], v233 offset:4608\n"
    "v_mfma_f32_16x16x32_bf16 v[80:83], v[12:15], a[184:187], v[80:83]\n"
    "s_add_u32 m0, s62, 0x12900\n"
    "s_add_u32 s63, s63, 0x24000\n"
    "ds_read_b64_tr_b16 v[4:5], v233 offset:32\n"
    "ds_read_b64_tr_b16 v[6:7], v233 offset:4640\n"
    "v_mfma_f32_16x16x32_bf16 v[112:115], v[12:15], a[188:191], v[112:115]\n"
    "buffer_load_dwordx4 v236, s[32:35], s63 offen offset:0 lds\n"
    "s_waitcnt lgkmcnt(8)\n"
    "v_mfma_f32_16x16x32_bf16 v[84:87], v[16:19], a[184:187], v[84:87]\n"
    "ds_read_b64_tr_b16 v[8:9], v233 offset:64\n"
    "ds_read_b64_tr_b16 v[10:11], v233 offset:4672\n"
    "v_mfma_f32_16x16x32_bf16 v[116:119], v[16:19], a[188:191], v[116:119]\n"
    "buffer_load_dwordx4 a[12:15], v232, s[24:27], s72 offen offset:1024 sc1\n"
    "v_mfma_f32_16x16x32_bf16 v[88:91], v[20:23], a[184:187], v[88:91]\n"
    "ds_read_b64_tr_b16 v[12:13], v233 offset:96\n"
    "v_mfma_f32_16x16x32_bf16 v[120:123], v[20:23], a[188:191], v[120:123]\n"
    "ds_read_b64_tr_b16 v[14:15], v233 offset:4704\n"
    "s_waitcnt lgkmcnt(8)\n"
    "v_mfma_f32_16x16x32_bf16 v[92:95], v[24:27], a[184:187], v[92:95]\n"
    "ds_read_b64_tr_b16 v[16:17], v233 offset:128\n"
    "ds_read_b64_tr_b16 v[18:19], v233 offset:4736\n"
    "v_mfma_f32_16x16x32_bf16 v[124:127], v[24:27], a[188:191], v[124:127]\n"
    "buffer_load_dwordx4 v237, s[32:35], s63 offen offset:1024 lds\n"
    "v_mfma_f32_16x16x32_bf16 v[128:131], v[28:31], a[184:187], v[128:131]\n"
    "ds_read_b64_tr_b16 v[20:21], v233 offset:160\n"
    "ds_read_b64_tr_b16 v[22:23], v233 offset:4768\n"
    "v_mfma_f32_16x16x32_bf16 v[132:135], v[28:31], a[188:191], v[132:135]\n"
    "buffer_load_dwordx4 a[16:19], v231, s[24:27], s72 offen offset:2048 sc1\n"
    "s_waitcnt lgkmcnt(8)\n"
    "v_mfma_f32_16x16x32_bf16 v[64:67], v[0:3], a[192:195], v[64:67]\n"
    "ds_read_b64_tr_b16 v[24:25], v233 offset:192\n"
    "v_mfma_f32_16x16x32_bf16 v[96:99], v[0:3], a[196:199], v[96:99]\n"
    "ds_read_b64_tr_b16 v[26:27], v233 offset:4800\n"
    "v_mfma_f32_16x16x32_bf16 v[68:71], v[4:7], a[192:195], v[68:71]\n"
    "ds_read_b64_tr_b16 v[28:29], v233 offset:224\n"
    "ds_read_b64_tr_b16 v[30:31], v233 offset:4832\n"
    "v_mfma_f32_16x16x32_bf16 v[100:103], v[4:7], a[196:199], v[100:103]\n"
    "buffer_load_dwordx4 v238, s[32:35], s63 offen offset:2048 lds\n"
    "s_waitcnt lgkmcnt(8)\n"
    "v_mfma_f32_16x16x32_bf16 v[72:75], v[8:11], a[192:195], v[72:75]\n"
    "ds_read_b64_tr_b16 v[0:1], v233 offset:256\n"
    "ds_read_b64_tr_b16 v[2:3], v233 offset:4864\n"
    "v_mfma_f32_16x16x32_bf16 v[104:107], v[8:11], a[196:199], v[104:107]\n"
    "buffer_load_dwordx4 a[20:23], v232, s[24:27], s72 offen offset:2048 sc1\n"
    "v_mfma_f32_16x16x32_bf16 v[76:79], v[12:15], a[192:195], v[76:79]\n"
    "ds_read_b64_tr_b16 v[4:5], v233 offset:9216\n"
    "v_mfma_f32_16x16x32_bf16 v[108:111], v[12:15], a[196:199], v[108:111]\n"
    "ds_read_b64_tr_b16 v[6:7], v233 offset:13824\n"
    "s_waitcnt lgkmcnt(8)\n"
    "v_mfma_f32_16x16x32_bf16 v[80:83], v[16:19], a[192:195], v[80:83]\n"
    "ds_read_b64_tr_b16 v[8:9], v233 offset:9248\n"
    "ds_read_b64_tr_b16 v[10:11], v233 offset:13856\n"
    "v_mfma_f32_16x16x32_bf16 v[112:115], v[16:19], a[196:199], v[112:115]\n"
    "buffer_load_dwordx4 v239, s[32:35], s63 offen offset:3072 lds\n"
    "v_mfma_f32_16x16x32_bf16 v[84:87], v[20:23], a[192:195], v[84:87]\n"
    "ds_read_b64_tr_b16 v[12:13], v233 offset:9280\n"
    "ds_read_b64_tr_b16 v[14:15], v233 offset:13888\n"
    "v_mfma_f32_16x16x32_bf16 v[116:119], v[20:23], a[196:199], v[116:119]\n"
    "buffer_load_dwordx4 a[24:27], v231, s[24:27], s72 offen offset:3072 sc1\n"
    "s_waitcnt lgkmcnt(8)\n"
    "v_mfma_f32_16x16x32_bf16 v[88:91], v[24:27], a[192:195], v[88:91]\n"
    "ds_read_b64_tr_b16 v[16:17], v233 offset:9312\n"
    "v_mfma_f32_16x16x32_bf16 v[120:123], v[24:27], a[196:199], v[120:123]\n"
    "ds_read_b64_tr_b16 v[18:19], v233 offset:13920\n"
    "v_mfma_f32_16x16x32_bf16 v[92:95], v[28:31], a[192:195], v[92:95]\n"
    "s_add_u32 m0, s62, 0x13900\n"
    "ds_read_b64_tr_b16 v[20:21], v233 offset:9344\n"
    "ds_read_b64_tr_b16 v[22:23], v233 offset:13952\n"
    "v_mfma_f32_16x16x32_bf16 v[124:127], v[28:31], a[196:199], v[124:127]\n"
    "buffer_load_dwordx4 v240, s[32:35], s63 offen offset:0 lds\n"
    "s_waitcnt lgkmcnt(8)\n"
    "v_mfma_f32_16x16x32_bf16 v[128:131], v[0:3], a[192:195], v[128:131]\n"
    "ds_read_b64_tr_b16 v[24:25], v233 offset:9376\n"
    "ds_read_b64_tr_b16 v[26:27], v233 offset:13984\n"
    "v_mfma_f32_16x16x32_bf16 v[132:135], v[0:3], a[196:199], v[132:135]\n"
    "buffer_load_dwordx4 a[28:31], v232, s[24:27], s72 offen offset:3072 sc1\n"
    "v_mfma_f32_16x16x32_bf16 v[64:67], v[4:7], a[200:203], v[64:67]\n"
    "ds_read_b64_tr_b16 v[28:29], v233 offset:9408\n"
    "v_mfma_f32_16x16x32_bf16 v[96:99], v[4:7], a[204:207], v[96:99]\n"
    "ds_read_b64_tr_b16 v[30:31], v233 offset:14016\n"
    "s_waitcnt lgkmcnt(8)\n"
    "v_mfma_f32_16x16x32_bf16 v[68:71], v[8:11], a[200:203], v[68:71]\n"
    "ds_read_b64_tr_b16 v[0:1], v233 offset:9440\n"
    "ds_read_b64_tr_b16 v[2:3], v233 offset:14048\n"
    "v_mfma_f32_16x16x32_bf16 v[100:103], v[8:11], a[204:207], v[100:103]\n"
    "buffer_load_dwordx4 v241, s[32:35], s63 offen offset:1024 lds\n"
    "v_mfma_f32_16x16x32_bf16 v[72:75], v[12:15], a[200:203], v[72:75]\n"
    "ds_read_b64_tr_b16 v[4:5], v233 offset:9472\n"
    "ds_read_b64_tr_b16 v[6:7], v233 offset:14080\n"
    "v_mfma_f32_16x16x32_bf16 v[104:107], v[12:15], a[204:207], v[104:107]\n"
    "buffer_load_dwordx2 a[32:33], v248, s[24:27], 0 offen sc1\n"
    "s_waitcnt lgkmcnt(8)\n"
    "v_mfma_f32_16x16x32_bf16 v[76:79], v[16:19], a[200:203], v[76:79]\n"
    "ds_read_b64_tr_b16 v[8:9], v233 offset:18432\n"
    "v_mfma_f32_16x16x32_bf16 v[108:111], v[16:19], a[204:207], v[108:111]\n"
    "ds_read_b64_tr_b16 v[10:11], v233 offset:23040\n"
    "v_mfma_f32_16x16x32_bf16 v[80:83], v[20:23], a[200:203], v[80:83]\n"
    "ds_read_b64_tr_b16 v[12:13], v233 offset:18464\n"
    "ds_read_b64_tr_b16 v[14:15], v233 offset:23072\n"
    "v_mfma_f32_16x16x32_bf16 v[112:115], v[20:23], a[204:207], v[112:115]\n"
    "buffer_load_dwordx4 v242, s[32:35], s63 offen offset:2048 lds\n"
    "s_waitcnt lgkmcnt(8)\n"
    "v_mfma_f32_16x16x32_bf16 v[84:87], v[24:27], a[200:203], v[84:87]\n"
    "ds_read_b64_tr_b16 v[16:17], v233 offset:18496\n"
    "ds_read_b64_tr_b16 v[18:19], v233 offset:23104\n"
    "v_mfma_f32_16x16x32_bf16 v[116:119], v[24:27], a[204:207], v[116:119]\n"
    "buffer_load_dwordx2 a[36:37], v249, s[24:27], 0 offen sc1\n"
    "v_mfma_f32_16x16x32_bf16 v[88:91], v[28:31], a[200:203], v[88:91]\n"
    "ds_read_b64_tr_b16 v[20:21], v233 offset:18528\n"
    "v_mfma_f32_16x16x32_bf16 v[120:123], v[28:31], a[204:207], v[120:123]\n"
    "ds_read_b64_tr_b16 v[22:23], v233 offset:23136\n"
    "s_waitcnt lgkmcnt(8)\n"
    "v_mfma_f32_16x16x32_bf16 v[92:95], v[0:3], a[200:203], v[92:95]\n"
    "ds_read_b64_tr_b16 v[24:25], v233 offset:18560\n"
    "ds_read_b64_tr_b16 v[26:27], v233 offset:23168\n"
    "v_mfma_f32_16x16x32_bf16 v[124:127], v[0:3], a[204:207], v[124:127]\n"
    "buffer_load_dwordx4 v243, s[32:35], s63 offen offset:3072 lds\n"
    "v_mfma_f32_16x16x32_bf16 v[128:131], v[4:7], a[200:203], v[128:131]\n"
    "ds_read_b64_tr_b16 v[28:29], v233 offset:18592\n"
    "v_mfma_f32_16x16x32_bf16 v[132:135], v[4:7], a[204:207], v[132:135]\n"
    "ds_read_b64_tr_b16 v[30:31], v233 offset:23200\n"
    "s_waitcnt lgkmcnt(8)\n"
    "v_mfma_f32_16x16x32_bf16 v[64:67], v[8:11], a[208:211], v[64:67]\n"
    "ds_read_b64_tr_b16 v[0:1], v233 offset:18624\n"
    "v_mfma_f32_16x16x32_bf16 v[96:99], v[8:11], a[212:215], v[96:99]\n"
    "ds_read_b64_tr_b16 v[2:3], v233 offset:23232\n"
    "v_mfma_f32_16x16x32_bf16 v[68:71], v[12:15], a[208:211], v[68:71]\n"
    "s_add_u32 m0, s62, 0x14900\n"
    "ds_read_b64_tr_b16 v[4:5], v233 offset:18656\n"
    "ds_read_b64_tr_b16 v[6:7], v233 offset:23264\n"
    "v_mfma_f32_16x16x32_bf16 v[100:103], v[12:15], a[212:215], v[100:103]\n"
    "buffer_load_dwordx4 v244, s[32:35], s63 offen offset:0 lds\n"
    "s_waitcnt lgkmcnt(8)\n"
    "v_mfma_f32_16x16x32_bf16 v[72:75], v[16:19], a[208:211], v[72:75]\n"
    "ds_read_b64_tr_b16 v[8:9], v233 offset:18688\n"
    "v_mfma_f32_16x16x32_bf16 v[104:107], v[16:19], a[212:215], v[104:107]\n"
    "ds_read_b64_tr_b16 v[10:11], v233 offset:23296\n"
    "v_mfma_f32_16x16x32_bf16 v[76:79], v[20:23], a[208:211], v[76:79]\n"
    "ds_read_b64_tr_b16 v[12:13], v233 offset:27648\n"
    "v_mfma_f32_16x16x32_bf16 v[108:111], v[20:23], a[212:215], v[108:111]\n"
    "ds_read_b64_tr_b16 v[14:15], v233 offset:32256\n"
    "s_waitcnt lgkmcnt(8)\n"
    "v_mfma_f32_16x16x32_bf16 v[80:83], v[24:27], a[208:211], v[80:83]\n"
    "ds_read_b64_tr_b16 v[16:17], v233 offset:27680\n"
    "v_mfma_f32_16x16x32_bf16 v[112:115], v[24:27], a[212:215], v[112:115]\n"
    "ds_read_b64_tr_b16 v[18:19], v233 offset:32288\n"
    "v_mfma_f32_16x16x32_bf16 v[84:87], v[28:31], a[208:211], v[84:87]\n"
    "ds_read_b64_tr_b16 v[20:21], v233 offset:27712\n"
    "s_and_b32 s15, s64, 0xf3\n"
    "s_lshl_b32 s15, s15, 3\n"
    "s_add_u32 s15, s15, 1024\n"
    "v_and_b32 v225, 3, v192\n"
    "v_lshl_add_u32 v225, v225, 5, s15\n"
    "v_mfma_f32_16x16x32_bf16 v[116:119], v[28:31], a[212:215], v[116:119]\n"
    "ds_read_b64_tr_b16 v[22:23], v233 offset:32320\n"
    "s_waitcnt lgkmcnt(8)\n"
    "v_mfma_f32_16x16x32_bf16 v[88:91], v[0:3], a[208:211], v[88:91]\n"
    "ds_read_b64_tr_b16 v[24:25], v233 offset:27744\n"
    "ds_read_b64_tr_b16 v[26:27], v233 offset:32352\n"
    "v_mfma_f32_16x16x32_bf16 v[120:123], v[0:3], a[212:215], v[120:123]\n"
    "global_load_dwordx2 v[226:227], v225, s[50:51] sc1\n"
    "ds_read_b32 v204, v205 offset:0\n"
    "ds_read_b32 v205, v205 offset:64\n"
    "v_mfma_f32_16x16x32_bf16 v[92:95], v[4:7], a[208:211], v[92:95]\n"
    "ds_read_b64_tr_b16 v[28:29], v233 offset:27776\n"
    "v_mfma_f32_16x16x32_bf16 v[124:127], v[4:7], a[212:215], v[124:127]\n"
    "ds_read_b64_tr_b16 v[30:31], v233 offset:32384\n"
    "s_waitcnt lgkmcnt(10)\n"
    "v_mfma_f32_16x16x32_bf16 v[128:131], v[8:11], a[208:211], v[128:131]\n"
    "ds_read_b64_tr_b16 v[0:1], v233 offset:27808\n"
    "v_mfma_f32_16x16x32_bf16 v[132:135], v[8:11], a[212:215], v[132:135]\n"
    "ds_read_b64_tr_b16 v[2:3], v233 offset:32416\n"
    "v_mfma_f32_16x16x32_bf16 v[64:67], v[12:15], a[216:219], v[64:67]\n"
    "ds_read_b64_tr_b16 v[4:5], v233 offset:27840\n"
    "v_mfma_f32_16x16x32_bf16 v[96:99], v[12:15], a[220:223], v[96:99]\n"
    "ds_read_b64_tr_b16 v[6:7], v233 offset:32448\n"
    "s_waitcnt lgkmcnt(10)\n"
    "v_mfma_f32_16x16x32_bf16 v[68:71], v[16:19], a[216:219], v[68:71]\n"
    "ds_read_b64_tr_b16 v[8:9], v233 offset:27872\n"
    "v_mfma_f32_16x16x32_bf16 v[100:103], v[16:19], a[220:223], v[100:103]\n"
    "ds_read_b64_tr_b16 v[10:11], v233 offset:32480\n"
    "v_mfma_f32_16x16x32_bf16 v[72:75], v[20:23], a[216:219], v[72:75]\n"
    "ds_read_b64_tr_b16 v[12:13], v233 offset:27904\n"
    "v_mfma_f32_16x16x32_bf16 v[104:107], v[20:23], a[220:223], v[104:107]\n"
    "ds_read_b64_tr_b16 v[14:15], v233 offset:32512\n"
    "s_waitcnt vmcnt(20)\n"
    "s_barrier\n"
    "s_waitcnt lgkmcnt(8)\n"
    "v_mfma_f32_16x16x32_bf16 v[76:79], v[24:27], a[216:219], v[76:79]\n"
    "ds_read_b64_tr_b16 v[16:17], v233 offset:38016\n"
    "v_mfma_f32_16x16x32_bf16 v[108:111], v[24:27], a[220:223], v[108:111]\n"
    "ds_read_b64_tr_b16 v[18:19], v233 offset:42624\n"
    "v_mfma_f32_16x16x32_bf16 v[80:83], v[28:31], a[216:219], v[80:83]\n"
    "s_add_u32 m0, s62, 0x1bd80\n"
    "s_mul_i32 s63, s9, 41472\n"
    "ds_read_b64_tr_b16 v[20:21], v233 offset:38048\n"
    "ds_read_b64_tr_b16 v[22:23], v233 offset:42656\n"
    "v_mfma_f32_16x16x32_bf16 v[112:115], v[28:31], a[220:223], v[112:115]\n"
    "buffer_load_dwordx4 v193, s[36:39], s63 offen offset:0 lds\n"
    "s_waitcnt lgkmcnt(8)\n"
    "v_mfma_f32_16x16x32_bf16 v[84:87], v[0:3], a[216:219], v[84:87]\n"
    "ds_read_b64_tr_b16 v[24:25], v233 offset:38080\n"
    "v_mfma_f32_16x16x32_bf16 v[116:119], v[0:3], a[220:223], v[116:119]\n"
    "ds_read_b64_tr_b16 v[26:27], v233 offset:42688\n"
    "v_mfma_f32_16x16x32_bf16 v[88:91], v[4:7], a[216:219], v[88:91]\n"
    "ds_read_b64_tr_b16 v[28:29], v233 offset:38112\n"
    "v_mfma_f32_16x16x32_bf16 v[120:123], v[4:7], a[220:223], v[120:123]\n"
    "ds_read_b64_tr_b16 v[30:31], v233 offset:42720\n"
    "s_waitcnt lgkmcnt(8)\n"
    "v_mfma_f32_16x16x32_bf16 v[92:95], v[8:11], a[216:219], v[92:95]\n"
    "ds_read_b64_tr_b16 v[0:1], v233 offset:38144\n"
    "ds_read_b64_tr_b16 v[2:3], v233 offset:42752\n"
    "v_mfma_f32_16x16x32_bf16 v[124:127], v[8:11], a[220:223], v[124:127]\n"
    "buffer_load_dwordx4 v194, s[36:39], s63 offen offset:1024 lds\n"
    "v_mfma_f32_16x16x32_bf16 v[128:131], v[12:15], a[216:219], v[128:131]\n"
    "ds_read_b64_tr_b16 v[4:5], v233 offset:38176\n"
    "v_mfma_f32_16x16x32_bf16 v[132:135], v[12:15], a[220:223], v[132:135]\n"
    "ds_read_b64_tr_b16 v[6:7], v233 offset:42784\n"
    "s_waitcnt lgkmcnt(8)\n"
    "v_mfma_f32_16x16x32_bf16 v[64:67], v[16:19], a[224:227], v[64:67]\n"
    "ds_read_b64_tr_b16 v[8:9], v233 offset:38208\n"
    "v_mfma_f32_16x16x32_bf16 v[96:99], v[16:19], a[228:231], v[96:99]\n"
    "ds_read_b64_tr_b16 v[10:11], v233 offset:42816\n"
    "v_mfma_f32_16x16x32_bf16 v[68:71], v[20:23], a[224:227], v[68:71]\n"
    "ds_read_b64_tr_b16 v[12:13], v233 offset:38240\n"
    "ds_read_b64_tr_b16 v[14:15], v233 offset:42848\n"
    "v_mfma_f32_16x16x32_bf16 v[100:103], v[20:23], a[228:231], v[100:103]\n"
    "buffer_load_dwordx4 v195, s[36:39], s63 offen offset:2048 lds\n"
    "s_waitcnt lgkmcnt(8)\n"
    "v_mfma_f32_16x16x32_bf16 v[72:75], v[24:27], a[224:227], v[72:75]\n"
    "ds_read_b64_tr_b16 v[16:17], v233 offset:38272\n"
    "v_mfma_f32_16x16x32_bf16 v[104:107], v[24:27], a[228:231], v[104:107]\n"
    "ds_read_b64_tr_b16 v[18:19], v233 offset:42880\n"
    "v_mfma_f32_16x16x32_bf16 v[76:79], v[28:31], a[224:227], v[76:79]\n"
    "ds_read_b64_tr_b16 v[20:21], v233 offset:47232\n"
    "v_mfma_f32_16x16x32_bf16 v[108:111], v[28:31], a[228:231], v[108:111]\n"
    "ds_read_b64_tr_b16 v[22:23], v233 offset:51840\n"
    "s_waitcnt lgkmcnt(8)\n"
    "v_mfma_f32_16x16x32_bf16 v[80:83], v[0:3], a[224:227], v[80:83]\n"
    "ds_read_b64_tr_b16 v[24:25], v233 offset:47264\n"
    "ds_read_b64_tr_b16 v[26:27], v233 offset:51872\n"
    "v_mfma_f32_16x16x32_bf16 v[112:115], v[0:3], a[228:231], v[112:115]\n"
    "buffer_load_dwordx4 v196, s[36:39], s63 offen offset:3072 lds\n"
    "v_mfma_f32_16x16x32_bf16 v[84:87], v[4:7], a[224:227], v[84:87]\n"
    "ds_read_b64_tr_b16 v[28:29], v233 offset:47296\n"
    "v_mfma_f32_16x16x32_bf16 v[116:119], v[4:7], a[228:231], v[116:119]\n"
    "ds_read_b64_tr_b16 v[30:31], v233 offset:51904\n"
    "s_waitcnt lgkmcnt(8)\n"
    "v_mfma_f32_16x16x32_bf16 v[88:91], v[8:11], a[224:227], v[88:91]\n"
    "ds_read_b64_tr_b16 v[0:1], v233 offset:47328\n"
    "v_mfma_f32_16x16x32_bf16 v[120:123], v[8:11], a[228:231], v[120:123]\n"
    "ds_read_b64_tr_b16 v[2:3], v233 offset:51936\n"
    "v_mfma_f32_16x16x32_bf16 v[92:95], v[12:15], a[224:227], v[92:95]\n"
    "s_add_u32 m0, s62, 0x1cd80\n"
    "ds_read_b64_tr_b16 v[4:5], v233 offset:47360\n"
    "ds_read_b64_tr_b16 v[6:7], v233 offset:51968\n"
    "v_mfma_f32_16x16x32_bf16 v[124:127], v[12:15], a[228:231], v[124:127]\n"
    "buffer_load_dwordx4 v197, s[36:39], s63 offen offset:0 lds\n"
    "s_waitcnt lgkmcnt(8)\n"
    "v_mfma_f32_16x16x32_bf16 v[128:131], v[16:19], a[224:227], v[128:131]\n"
    "ds_read_b64_tr_b16 v[8:9], v233 offset:47392\n"
    "v_mfma_f32_16x16x32_bf16 v[132:135], v[16:19], a[228:231], v[132:135]\n"
    "ds_read_b64_tr_b16 v[10:11], v233 offset:52000\n"
    "v_mfma_f32_16x16x32_bf16 v[64:67], v[20:23], a[232:235], v[64:67]\n"
    "ds_read_b64_tr_b16 v[12:13], v233 offset:47424\n"
    "v_mfma_f32_16x16x32_bf16 v[96:99], v[20:23], a[236:239], v[96:99]\n"
    "ds_read_b64_tr_b16 v[14:15], v233 offset:52032\n"
    "s_waitcnt lgkmcnt(8)\n"
    "v_mfma_f32_16x16x32_bf16 v[68:71], v[24:27], a[232:235], v[68:71]\n"
    "ds_read_b64_tr_b16 v[16:17], v233 offset:47456\n"
    "ds_read_b64_tr_b16 v[18:19], v233 offset:52064\n"
    "v_mfma_f32_16x16x32_bf16 v[100:103], v[24:27], a[236:239], v[100:103]\n"
    "buffer_load_dwordx4 v198, s[36:39], s63 offen offset:1024 lds\n"
    "v_mfma_f32_16x16x32_bf16 v[72:75], v[28:31], a[232:235], v[72:75]\n"
    "ds_read_b64_tr_b16 v[20:21], v233 offset:47488\n"
    "v_mfma_f32_16x16x32_bf16 v[104:107], v[28:31], a[236:239], v[104:107]\n"
    "ds_read_b64_tr_b16 v[22:23], v233 offset:52096\n"
    "s_waitcnt lgkmcnt(8)\n"
    "v_mfma_f32_16x16x32_bf16 v[76:79], v[0:3], a[232:235], v[76:79]\n"
    "ds_read_b64_tr_b16 v[24:25], v233 offset:56448\n"
    "v_mfma_f32_16x16x32_bf16 v[108:111], v[0:3], a[236:239], v[108:111]\n"
    "ds_read_b64_tr_b16 v[26:27], v233 offset:61056\n"
    "v_mfma_f32_16x16x32_bf16 v[80:83], v[4:7], a[232:235], v[80:83]\n"
    "ds_read_b64_tr_b16 v[28:29], v233 offset:56480\n"
    "ds_read_b64_tr_b16 v[30:31], v233 offset:61088\n"
    "v_mfma_f32_16x16x32_bf16 v[112:115], v[4:7], a[236:239], v[112:115]\n"
    "buffer_load_dwordx4 v199, s[36:39], s63 offen offset:2048 lds\n"
    "s_waitcnt lgkmcnt(8)\n"
    "v_mfma_f32_16x16x32_bf16 v[84:87], v[8:11], a[232:235], v[84:87]\n"
    "ds_read_b64_tr_b16 v[0:1], v233 offset:56512\n"
    "v_mfma_f32_16x16x32_bf16 v[116:119], v[8:11], a[236:239], v[116:119]\n"
    "ds_read_b64_tr_b16 v[2:3], v233 offset:61120\n"
    "v_mfma_f32_16x16x32_bf16 v[88:91], v[12:15], a[232:235], v[88:91]\n"
    "ds_read_b64_tr_b16 v[4:5], v233 offset:56544\n"
    "v_mfma_f32_16x16x32_bf16 v[120:123], v[12:15], a[236:239], v[120:123]\n"
    "ds_read_b64_tr_b16 v[6:7], v233 offset:61152\n"
    "s_waitcnt lgkmcnt(8)\n"
    "v_mfma_f32_16x16x32_bf16 v[92:95], v[16:19], a[232:235], v[92:95]\n"
    "ds_read_b64_tr_b16 v[8:9], v233 offset:56576\n"
    "ds_read_b64_tr_b16 v[10:11], v233 offset:61184\n"
    "v_mfma_f32_16x16x32_bf16 v[124:127], v[16:19], a[236:239], v[124:127]\n"
    "buffer_load_dwordx4 v200, s[36:39], s63 offen offset:3072 lds\n"
    "v_mfma_f32_16x16x32_bf16 v[128:131], v[20:23], a[232:235], v[128:131]\n"
    "ds_read_b64_tr_b16 v[12:13], v233 offset:56608\n"
    "v_mfma_f32_16x16x32_bf16 v[132:135], v[20:23], a[236:239], v[132:135]\n"
    "ds_read_b64_tr_b16 v[14:15], v233 offset:61216\n"
    "s_waitcnt lgkmcnt(8)\n"
    "v_mfma_f32_16x16x32_bf16 v[64:67], v[24:27], a[240:243], v[64:67]\n"
    "ds_read_b64_tr_b16 v[16:17], v233 offset:56640\n"
    "v_mfma_f32_16x16x32_bf16 v[96:99], v[24:27], a[244:247], v[96:99]\n"
    "ds_read_b64_tr_b16 v[18:19], v233 offset:61248\n"
    "v_mfma_f32_16x16x32_bf16 v[68:71], v[28:31], a[240:243], v[68:71]\n"
    "s_add_u32 m0, s62, 0x1dd80\n"
    "ds_read_b64_tr_b16 v[20:21], v233 offset:56672\n"
    "ds_read_b64_tr_b16 v[22:23], v233 offset:61280\n"
    "v_mfma_f32_16x16x32_bf16 v[100:103], v[28:31], a[244:247], v[100:103]\n"
    "buffer_load_dwordx4 v201, s[36:39], s63 offen offset:0 lds\n"
    "s_waitcnt lgkmcnt(8)\n"
    "v_mfma_f32_16x16x32_bf16 v[72:75], v[0:3], a[240:243], v[72:75]\n"
    "ds_read_b64_tr_b16 v[24:25], v233 offset:56704\n"
    "v_mfma_f32_16x16x32_bf16 v[104:107], v[0:3], a[244:247], v[104:107]\n"
    "ds_read_b64_tr_b16 v[26:27], v233 offset:61312\n"
    "v_mfma_f32_16x16x32_bf16 v[76:79], v[4:7], a[240:243], v[76:79]\n"
    "ds_read_b64_tr_b16 v[28:29], v234 offset:128\n"
    "v_mfma_f32_16x16x32_bf16 v[108:111], v[4:7], a[244:247], v[108:111]\n"
    "ds_read_b64_tr_b16 v[30:31], v234 offset:4736\n"
    "s_waitcnt lgkmcnt(8)\n"
    "v_mfma_f32_16x16x32_bf16 v[80:83], v[8:11], a[240:243], v[80:83]\n"
    "ds_read_b64_tr_b16 v[0:1], v234 offset:160\n"
    "v_mfma_f32_16x16x32_bf16 v[112:115], v[8:11], a[244:247], v[112:115]\n"
    "ds_read_b64_tr_b16 v[2:3], v234 offset:4768\n"
    "v_mfma_f32_16x16x32_bf16 v[84:87], v[12:15], a[240:243], v[84:87]\n"
    "ds_read_b64_tr_b16 v[4:5], v234 offset:192\n"
    "v_mfma_f32_16x16x32_bf16 v[116:119], v[12:15], a[244:247], v[116:119]\n"
    "ds_read_b64_tr_b16 v[6:7], v234 offset:4800\n"
    "s_waitcnt lgkmcnt(8)\n"
    "v_mfma_f32_16x16x32_bf16 v[88:91], v[16:19], a[240:243], v[88:91]\n"
    "ds_read_b64_tr_b16 v[8:9], v234 offset:224\n"
    "v_mfma_f32_16x16x32_bf16 v[120:123], v[16:19], a[244:247], v[120:123]\n"
    "ds_read_b64_tr_b16 v[10:11], v234 offset:4832\n"
    "v_mfma_f32_16x16x32_bf16 v[92:95], v[20:23], a[240:243], v[92:95]\n"
    "ds_read_b64_tr_b16 v[12:13], v234 offset:256\n"
    "v_mfma_f32_16x16x32_bf16 v[124:127], v[20:23], a[244:247], v[124:127]\n"
    "ds_read_b64_tr_b16 v[14:15], v234 offset:4864\n"
    "s_waitcnt lgkmcnt(8)\n"
    "v_mfma_f32_16x16x32_bf16 v[128:131], v[24:27], a[240:243], v[128:131]\n"
    "ds_read_b64_tr_b16 v[16:17], v234 offset:288\n"
    "v_mfma_f32_16x16x32_bf16 v[132:135], v[24:27], a[244:247], v[132:135]\n"
    "ds_read_b64_tr_b16 v[18:19], v234 offset:4896\n"
    "v_mfma_f32_16x16x32_bf16 v[64:67], v[28:31], a[248:251], v[64:67]\n"
    "ds_read_b64_tr_b16 v[20:21], v234 offset:320\n"
    "v_mfma_f32_16x16x32_bf16 v[96:99], v[28:31], a[252:255], v[96:99]\n"
    "ds_read_b64_tr_b16 v[22:23], v234 offset:4928\n"
    "s_waitcnt lgkmcnt(8)\n"
    "v_mfma_f32_16x16x32_bf16 v[68:71], v[0:3], a[248:251], v[68:71]\n"
    "ds_read_b64_tr_b16 v[24:25], v234 offset:352\n"
    "v_mfma_f32_16x16x32_bf16 v[100:103], v[0:3], a[252:255], v[100:103]\n"
    "ds_read_b64_tr_b16 v[26:27], v234 offset:4960\n"
    "v_mfma_f32_16x16x32_bf16 v[72:75], v[4:7], a[248:251], v[72:75]\n"
    "ds_read_b64_tr_b16 v[28:29], v234 offset:384\n"
    "v_mfma_f32_16x16x32_bf16 v[104:107], v[4:7], a[252:255], v[104:107]\n"
    "ds_read_b64_tr_b16 v[30:31], v234 offset:4992\n"
    "s_waitcnt vmcnt(10)\n"
    "s_barrier\n"
    "s_waitcnt lgkmcnt(8)\n"
    "v_mfma_f32_16x16x32_bf16 v[76:79], v[8:11], a[248:251], v[76:79]\n"
    "ds_read_b64_tr_b16 v[0:1], v234 offset:10496\n"
    "v_mfma_f32_16x16x32_bf16 v[108:111], v[8:11], a[252:255], v[108:111]\n"
    "ds_read_b64_tr_b16 v[2:3], v234 offset:15104\n"
    "v_mfma_f32_16x16x32_bf16 v[80:83], v[12:15], a[248:251], v[80:83]\n"
    "s_mul_i32 s15, s9, 2176\n"
    "s_add_u32 m0, s15, 0x6300\n"
    "ds_read_b64_tr_b16 v[4:5], v234 offset:10528\n"
    "ds_read_b64_tr_b16 v[6:7], v234 offset:15136\n"
    "v_mfma_f32_16x16x32_bf16 v[112:115], v[12:15], a[252:255], v[112:115]\n"
    "buffer_load_dwordx4 v202, s[36:39], 0 offen offset:0 lds\n"
    "s_waitcnt lgkmcnt(8)\n"
    "v_mfma_f32_16x16x32_bf16 v[84:87], v[16:19], a[248:251], v[84:87]\n"
    "ds_read_b64_tr_b16 v[8:9], v234 offset:10560\n"
    "v_mfma_f32_16x16x32_bf16 v[116:119], v[16:19], a[252:255], v[116:119]\n"
    "ds_read_b64_tr_b16 v[10:11], v234 offset:15168\n"
    "v_mfma_f32_16x16x32_bf16 v[88:91], v[20:23], a[248:251], v[88:91]\n"
    "ds_read_b64_tr_b16 v[12:13], v234 offset:10592\n"
    "v_mfma_f32_16x16x32_bf16 v[120:123], v[20:23], a[252:255], v[120:123]\n"
    "ds_read_b64_tr_b16 v[14:15], v234 offset:15200\n"
    "s_waitcnt lgkmcnt(8)\n"
    "v_mfma_f32_16x16x32_bf16 v[92:95], v[24:27], a[248:251], v[92:95]\n"
    "ds_read_b64_tr_b16 v[16:17], v234 offset:10624\n"
    "ds_read_b64_tr_b16 v[18:19], v234 offset:15232\n"
    "v_mfma_f32_16x16x32_bf16 v[124:127], v[24:27], a[252:255], v[124:127]\n"
    "buffer_load_dwordx4 v203, s[36:39], 0 offen offset:1024 lds\n"
    "v_mfma_f32_16x16x32_bf16 v[128:131], v[28:31], a[248:251], v[128:131]\n"
    "ds_read_b64_tr_b16 v[20:21], v234 offset:10656\n"
    "v_mfma_f32_16x16x32_bf16 v[132:135], v[28:31], a[252:255], v[132:135]\n"
    "ds_read_b64_tr_b16 v[22:23], v234 offset:15264\n"
    "s_waitcnt lgkmcnt(8)\n"
    "v_mfma_f32_16x16x32_bf16 v[64:67], v[0:3], v[152:155], v[64:67]\n"
    "ds_read_b64_tr_b16 v[24:25], v234 offset:10688\n"
    "v_mfma_f32_16x16x32_bf16 v[96:99], v[0:3], v[156:159], v[96:99]\n"
    "ds_read_b64_tr_b16 v[26:27], v234 offset:15296\n"
    "v_mfma_f32_16x16x32_bf16 v[68:71], v[4:7], v[152:155], v[68:71]\n"
    "ds_read_b64_tr_b16 v[28:29], v234 offset:10720\n"
    "v_mfma_f32_16x16x32_bf16 v[100:103], v[4:7], v[156:159], v[100:103]\n"
    "ds_read_b64_tr_b16 v[30:31], v234 offset:15328\n"
    "s_waitcnt lgkmcnt(8)\n"
    "v_mfma_f32_16x16x32_bf16 v[72:75], v[8:11], v[152:155], v[72:75]\n"
    "ds_read_b64_tr_b16 v[0:1], v234 offset:10752\n"
    "v_mfma_f32_16x16x32_bf16 v[104:107], v[8:11], v[156:159], v[104:107]\n"
    "ds_read_b64_tr_b16 v[2:3], v234 offset:15360\n"
    "v_mfma_f32_16x16x32_bf16 v[76:79], v[12:15], v[152:155], v[76:79]\n"
    "ds_read_b64_tr_b16 v[4:5], v234 offset:19712\n"
    "v_mfma_f32_16x16x32_bf16 v[108:111], v[12:15], v[156:159], v[108:111]\n"
    "ds_read_b64_tr_b16 v[6:7], v234 offset:24320\n"
    "s_waitcnt lgkmcnt(8)\n"
    "v_mfma_f32_16x16x32_bf16 v[80:83], v[16:19], v[152:155], v[80:83]\n"
    "ds_read_b64_tr_b16 v[8:9], v234 offset:19744\n"
    "v_mfma_f32_16x16x32_bf16 v[112:115], v[16:19], v[156:159], v[112:115]\n"
    "ds_read_b64_tr_b16 v[10:11], v234 offset:24352\n"
    "v_mfma_f32_16x16x32_bf16 v[84:87], v[20:23], v[152:155], v[84:87]\n"
    "ds_read_b64_tr_b16 v[12:13], v234 offset:19776\n"
    "v_mfma_f32_16x16x32_bf16 v[116:119], v[20:23], v[156:159], v[116:119]\n"
    "ds_read_b64_tr_b16 v[14:15], v234 offset:24384\n"
    "s_waitcnt lgkmcnt(8)\n"
    "v_mfma_f32_16x16x32_bf16 v[88:91], v[24:27], v[152:155], v[88:91]\n"
    "ds_read_b64_tr_b16 v[16:17], v234 offset:19808\n"
    "v_mfma_f32_16x16x32_bf16 v[120:123], v[24:27], v[156:159], v[120:123]\n"
    "ds_read_b64_tr_b16 v[18:19], v234 offset:24416\n"
    "v_mfma_f32_16x16x32_bf16 v[92:95], v[28:31], v[152:155], v[92:95]\n"
    "ds_read_b64_tr_b16 v[20:21], v234 offset:19840\n"
    "v_mfma_f32_16x16x32_bf16 v[124:127], v[28:31], v[156:159], v[124:127]\n"
    "ds_read_b64_tr_b16 v[22:23], v234 offset:24448\n"
    "s_waitcnt lgkmcnt(8)\n"
    "v_mfma_f32_16x16x32_bf16 v[128:131], v[0:3], v[152:155], v[128:131]\n"
    "ds_read_b64_tr_b16 v[24:25], v234 offset:19872\n"
    "v_mfma_f32_16x16x32_bf16 v[132:135], v[0:3], v[156:159], v[132:135]\n"
    "ds_read_b64_tr_b16 v[26:27], v234 offset:24480\n"
    "v_mfma_f32_16x16x32_bf16 v[64:67], v[4:7], v[160:163], v[64:67]\n"
    "ds_read_b64_tr_b16 v[28:29], v234 offset:19904\n"
    "v_mfma_f32_16x16x32_bf16 v[96:99], v[4:7], v[164:167], v[96:99]\n"
    "ds_read_b64_tr_b16 v[30:31], v234 offset:24512\n"
    "s_waitcnt lgkmcnt(8)\n"
    "v_mfma_f32_16x16x32_bf16 v[68:71], v[8:11], v[160:163], v[68:71]\n"
    "ds_read_b64_tr_b16 v[0:1], v234 offset:19936\n"
    "v_mfma_f32_16x16x32_bf16 v[100:103], v[8:11], v[164:167], v[100:103]\n"
    "ds_read_b64_tr_b16 v[2:3], v234 offset:24544\n"
    "v_mfma_f32_16x16x32_bf16 v[72:75], v[12:15], v[160:163], v[72:75]\n"
    "ds_read_b64_tr_b16 v[4:5], v234 offset:19968\n"
    "v_mfma_f32_16x16x32_bf16 v[104:107], v[12:15], v[164:167], v[104:107]\n"
    "ds_read_b64_tr_b16 v[6:7], v234 offset:24576\n"
    "s_waitcnt lgkmcnt(8)\n"
    "v_mfma_f32_16x16x32_bf16 v[76:79], v[16:19], v[160:163], v[76:79]\n"
    "ds_read_b64_tr_b16 v[8:9], v234 offset:28928\n"
    "v_mfma_f32_16x16x32_bf16 v[108:111], v[16:19], v[164:167], v[108:111]\n"
    "ds_read_b64_tr_b16 v[10:11], v234 offset:33536\n"
    "v_mfma_f32_16x16x32_bf16 v[80:83], v[20:23], v[160:163], v[80:83]\n"
    "ds_read_b64_tr_b16 v[12:13], v234 offset:28960\n"
    "v_mfma_f32_16x16x32_bf16 v[112:115], v[20:23], v[164:167], v[112:115]\n"
    "ds_read_b64_tr_b16 v[14:15], v234 offset:33568\n"
    "s_waitcnt lgkmcnt(8)\n"
    "v_mfma_f32_16x16x32_bf16 v[84:87], v[24:27], v[160:163], v[84:87]\n"
    "ds_read_b64_tr_b16 v[16:17], v234 offset:28992\n"
    "v_mfma_f32_16x16x32_bf16 v[116:119], v[24:27], v[164:167], v[116:119]\n"
    "ds_read_b64_tr_b16 v[18:19], v234 offset:33600\n"
    "v_mfma_f32_16x16x32_bf16 v[88:91], v[28:31], v[160:163], v[88:91]\n"
    "ds_read_b64_tr_b16 v[20:21], v234 offset:29024\n"
    "v_mfma_f32_16x16x32_bf16 v[120:123], v[28:31], v[164:167], v[120:123]\n"
    "ds_read_b64_tr_b16 v[22:23], v234 offset:33632\n"
    "s_waitcnt lgkmcnt(8)\n"
    "v_mfma_f32_16x16x32_bf16 v[92:95], v[0:3], v[160:163], v[92:95]\n"
    "ds_read_b64_tr_b16 v[24:25], v234 offset:29056\n"
    "v_mfma_f32_16x16x32_bf16 v[124:127], v[0:3], v[164:167], v[124:127]\n"
    "ds_read_b64_tr_b16 v[26:27], v234 offset:33664\n"
    "v_mfma_f32_16x16x32_bf16 v[128:131], v[4:7], v[160:163], v[128:131]\n"
    "ds_read_b64_tr_b16 v[28:29], v234 offset:29088\n"
    "v_mfma_f32_16x16x32_bf16 v[132:135], v[4:7], v[164:167], v[132:135]\n"
    "ds_read_b64_tr_b16 v[30:31], v234 offset:33696\n"
    "s_waitcnt lgkmcnt(8)\n"
    "v_mfma_f32_16x16x32_bf16 v[64:67], v[8:11], v[168:171], v[64:67]\n"
    "ds_read_b64_tr_b16 v[0:1], v234 offset:29120\n"
    "v_mfma_f32_16x16x32_bf16 v[96:99], v[8:11], v[172:175], v[96:99]\n"
    "ds_read_b64_tr_b16 v[2:3], v234 offset:33728\n"
    "v_mfma_f32_16x16x32_bf16 v[68:71], v[12:15], v[168:171], v[68:71]\n"
    "ds_read_b64_tr_b16 v[4:5], v234 offset:29152\n"
    "v_mfma_f32_16x16x32_bf16 v[100:103], v[12:15], v[172:175], v[100:103]\n"
    "ds_read_b64_tr_b16 v[6:7], v234 offset:33760\n"
    "s_waitcnt lgkmcnt(8)\n"
    "v_mfma_f32_16x16x32_bf16 v[72:75], v[16:19], v[168:171], v[72:75]\n"
    "ds_read_b64_tr_b16 v[8:9], v234 offset:29184\n"
    "v_mfma_f32_16x16x32_bf16 v[104:107], v[16:19], v[172:175], v[104:107]\n"
    "ds_read_b64_tr_b16 v[10:11], v234 offset:33792\n"
    "v_mfma_f32_16x16x32_bf16 v[76:79], v[20:23], v[168:171], v[76:79]\n"
    "ds_read_b64_tr_b16 v[12:13], v234 offset:38144\n"
    "v_mfma_f32_16x16x32_bf16 v[108:111], v[20:23], v[172:175], v[108:111]\n"
    "ds_read_b64_tr_b16 v[14:15], v234 offset:42752\n"
    "s_waitcnt lgkmcnt(8)\n"
    "v_mfma_f32_16x16x32_bf16 v[80:83], v[24:27], v[168:171], v[80:83]\n"
    "ds_read_b64_tr_b16 v[16:17], v234 offset:38176\n"
    "v_mfma_f32_16x16x32_bf16 v[112:115], v[24:27], v[172:175], v[112:115]\n"
    "ds_read_b64_tr_b16 v[18:19], v234 offset:42784\n"
    "s_waitcnt vmcnt(11)\n"
    "v_cmp_ne_u64 s[54:55], s[48:49], v[226:227]\n"
    "s_nop 1\n"
    "s_cmp_eq_u64 s[54:55], 0\n"
    "s_cbranch_scc0 .Lpoll3\n"
    ".Lpollok3:\n"
    "v_mfma_f32_16x16x32_bf16 v[84:87], v[28:31], v[168:171], v[84:87]\n"
    "ds_read_b64_tr_b16 v[20:21], v234 offset:38208\n"
    "v_mfma_f32_16x16x32_bf16 v[116:119], v[28:31], v[172:175], v[116:119]\n"
    "ds_read_b64_tr_b16 v[22:23], v234 offset:42816\n"
    "s_waitcnt lgkmcnt(8)\n"
    "v_mfma_f32_16x16x32_bf16 v[88:91], v[0:3], v[168:171], v[88:91]\n"
    "ds_read_b64_tr_b16 v[24:25], v234 offset:38240\n"
    "v_mfma_f32_16x16x32_bf16 v[120:123], v[0:3], v[172:175], v[120:123]\n"
    "ds_read_b64_tr_b16 v[26:27], v234 offset:42848\n"
    "v_mfma_f32_16x16x32_bf16 v[92:95], v[4:7], v[168:171], v[92:95]\n"
    "ds_read_b64_tr_b16 v[28:29], v234 offset:38272\n"
    "v_mfma_f32_16x16x32_bf16 v[124:127], v[4:7], v[172:175], v[124:127]\n"
    "ds_read_b64_tr_b16 v[30:31], v234 offset:42880\n"
    "s_waitcnt lgkmcnt(8)\n"
    "v_mfma_f32_16x16x32_bf16 v[128:131], v[8:11], v[168:171], v[128:131]\n"
    "ds_read_b64_tr_b16 v[0:1], v234 offset:38304\n"
    "v_mfma_f32_16x16x32_bf16 v[132:135], v[8:11], v[172:175], v[132:135]\n"
    "ds_read_b64_tr_b16 v[2:3], v234 offset:42912\n"
    "v_mfma_f32_16x16x32_bf16 v[64:67], v[12:15], v[176:179], v[64:67]\n"
    "ds_read_b64_tr_b16 v[4:5], v234 offset:38336\n"
    "v_mfma_f32_16x16x32_bf16 v[96:99], v[12:15], v[180:183], v[96:99]\n"
    "ds_read_b64_tr_b16 v[6:7], v234 offset:42944\n"
    "s_waitcnt lgkmcnt(8)\n"
    "v_mfma_f32_16x16x32_bf16 v[68:71], v[16:19], v[176:179], v[68:71]\n"
    "ds_read_b64_tr_b16 v[8:9], v234 offset:38368\n"
    "v_mfma_f32_16x16x32_bf16 v[100:103], v[16:19], v[180:183], v[100:103]\n"
    "ds_read_b64_tr_b16 v[10:11], v234 offset:42976\n"
    "v_mfma_f32_16x16x32_bf16 v[72:75], v[20:23], v[176:179], v[72:75]\n"
    "ds_read_b64_tr_b16 v[12:13], v234 offset:38400\n"
    "v_mfma_f32_16x16x32_bf16 v[104:107], v[20:23], v[180:183], v[104:107]\n"
    "ds_read_b64_tr_b16 v[14:15], v234 offset:43008\n"
    "s_waitcnt vmcnt(2)\n"
    "s_barrier\n"
    "s_waitcnt lgkmcnt(8)\n"
    "v_mfma_f32_16x16x32_bf16 v[76:79], v[24:27], v[176:179], v[76:79]\n"
    "ds_read_b64_tr_b16 v[16:17], v234 offset:48512\n"
    "v_mul_f32 v64, v204, v64\n"
    "v_mul_f32 v65, v204, v65\n"
    "v_mul_f32 v66, v204, v66\n"
    "v_mfma_f32_16x16x32_bf16 v[108:111], v[24:27], v[180:183], v[108:111]\n"
    "ds_read_b64_tr_b16 v[18:19], v234 offset:53120\n"
    "v_mul_f32 v67, v204, v67\n"
    "v_mul_f32 v96, v205, v96\n"
    "v_mfma_f32_16x16x32_bf16 v[80:83], v[28:31], v[176:179], v[80:83]\n"
    "ds_read_b64_tr_b16 v[20:21], v234 offset:48544\n"
    "v_mul_f32 v97, v205, v97\n"
    "v_mul_f32 v98, v205, v98\n"
    "v_mul_f32 v99, v205, v99\n"
    "v_mfma_f32_16x16x32_bf16 v[112:115], v[28:31], v[180:183], v[112:115]\n"
    "ds_read_b64_tr_b16 v[22:23], v234 offset:53152\n"
    "v_mul_f32 v68, v204, v68\n"
    "v_mul_f32 v69, v204, v69\n"
    "s_waitcnt lgkmcnt(8)\n"
    "v_mfma_f32_16x16x32_bf16 v[84:87], v[0:3], v[176:179], v[84:87]\n"
    "ds_read_b64_tr_b16 v[24:25], v234 offset:48576\n"
    "v_mul_f32 v70, v204, v70\n"
    "v_mul_f32 v71, v204, v71\n"
    "v_mul_f32 v100, v205, v100\n"
    "v_mfma_f32_16x16x32_bf16 v[116:119], v[0:3], v[180:183], v[116:119]\n"
    "ds_read_b64_tr_b16 v[26:27], v234 offset:53184\n"
    "v_mul_f32 v101, v205, v101\n"
    "v_mul_f32 v102, v205, v102\n"
    "v_mfma_f32_16x16x32_bf16 v[88:91], v[4:7], v[176:179], v[88:91]\n"
    "ds_read_b64_tr_b16 v[28:29], v234 offset:48608\n"
    "v_mul_f32 v103, v205, v103\n"
    "v_mul_f32 v72, v204, v72\n"
    "v_mul_f32 v73, v204, v73\n"
    "v_mfma_f32_16x16x32_bf16 v[120:123], v[4:7], v[180:183], v[120:123]\n"
    "ds_read_b64_tr_b16 v[30:31], v234 offset:53216\n"
    "v_mul_f32 v74, v204, v74\n"
    "v_mul_f32 v75, v204, v75\n"
    "s_waitcnt lgkmcnt(8)\n"
    "v_mfma_f32_16x16x32_bf16 v[92:95], v[8:11], v[176:179], v[92:95]\n"
    "ds_read_b64_tr_b16 v[0:1], v234 offset:48640\n"
    "v_mul_f32 v104, v205, v104\n"
    "v_mul_f32 v105, v205, v105\n"
    "v_mul_f32 v106, v205, v106\n"
    "v_mfma_f32_16x16x32_bf16 v[124:127], v[8:11], v[180:183], v[124:127]\n"
    "ds_read_b64_tr_b16 v[2:3], v234 offset:53248\n"
    "v_mul_f32 v107, v205, v107\n"
    "v_mul_f32 v76, v204, v76\n"
    "v_mfma_f32_16x16x32_bf16 v[128:131], v[12:15], v[176:179], v[128:131]\n"
    "ds_read_b64_tr_b16 v[4:5], v234 offset:48672\n"
    "v_mul_f32 v77, v204, v77\n"
    "v_mul_f32 v78, v204, v78\n"
    "v_mul_f32 v79, v204, v79\n"
    "v_mfma_f32_16x16x32_bf16 v[132:135], v[12:15], v[180:183], v[132:135]\n"
    "ds_read_b64_tr_b16 v[6:7], v234 offset:53280\n"
    "v_mul_f32 v108, v205, v108\n"
    "v_mul_f32 v109, v205, v109\n"
    "s_waitcnt lgkmcnt(8)\n"
    "v_mfma_f32_16x16x32_bf16 v[64:67], v[16:19], a[0:3], v[64:67]\n"
    "ds_read_b64_tr_b16 v[8:9], v234 offset:48704\n"
    "v_mul_f32 v110, v205, v110\n"
    "v_mul_f32 v111, v205, v111\n"
    "v_mul_f32 v80, v204, v80\n"
    "v_mfma_f32_16x16x32_bf16 v[96:99], v[16:19], a[4:7], v[96:99]\n"
    "ds_read_b64_tr_b16 v[10:11], v234 offset:53312\n"
    "v_mul_f32 v81, v204, v81\n"
    "v_mul_f32 v82, v204, v82\n"
    "v_mfma_f32_16x16x32_bf16 v[68:71], v[20:23], a[0:3], v[68:71]\n"
    "ds_read_b64_tr_b16 v[12:13], v234 offset:48736\n"
    "v_mul_f32 v83, v204, v83\n"
    "v_mul_f32 v112, v205, v112\n"
    "v_mul_f32 v113, v205, v113\n"
    "v_mfma_f32_16x16x32_bf16 v[100:103], v[20:23], a[4:7], v[100:103]\n"
    "ds_read_b64_tr_b16 v[14:15], v234 offset:53344\n"
    "v_mul_f32 v114, v205, v114\n"
    "v_mul_f32 v115, v205, v115\n"
    "s_waitcnt lgkmcnt(8)\n"
    "v_mfma_f32_16x16x32_bf16 v[72:75], v[24:27], a[0:3], v[72:75]\n"
    "ds_read_b64_tr_b16 v[16:17], v234 offset:48768\n"
    "v_mul_f32 v84, v204, v84\n"
    "v_mul_f32 v85, v204, v85\n"
    "v_mul_f32 v86, v204, v86\n"
    "v_mfma_f32_16x16x32_bf16 v[104:107], v[24:27], a[4:7], v[104:107]\n"
    "ds_read_b64_tr_b16 v[18:19], v234 offset:53376\n"
    "v_mul_f32 v87, v204, v87\n"
    "v_mul_f32 v116, v205, v116\n"
    "v_mfma_f32_16x16x32_bf16 v[76:79], v[28:31], a[0:3], v[76:79]\n"
    "ds_read_b64_tr_b16 v[20:21], v234 offset:57728\n"
    "v_mul_f32 v117, v205, v117\n"
    "v_mul_f32 v118, v205, v118\n"
    "v_mul_f32 v119, v205, v119\n"
    "v_mfma_f32_16x16x32_bf16 v[108:111], v[28:31], a[4:7], v[108:111]\n"
    "ds_read_b64_tr_b16 v[22:23], v234 offset:62336\n"
    "v_mul_f32 v88, v204, v88\n"
    "v_mul_f32 v89, v204, v89\n"
    "s_waitcnt lgkmcnt(8)\n"
    "v_mfma_f32_16x16x32_bf16 v[80:83], v[0:3], a[0:3], v[80:83]\n"
    "ds_read_b64_tr_b16 v[24:25], v234 offset:57760\n"
    "v_mul_f32 v90, v204, v90\n"
    "v_mul_f32 v91, v204, v91\n"
    "v_mul_f32 v120, v205, v120\n"
    "v_mfma_f32_16x16x32_bf16 v[112:115], v[0:3], a[4:7], v[112:115]\n"
    "ds_read_b64_tr_b16 v[26:27], v234 offset:62368\n"
    "v_mul_f32 v121, v205, v121\n"
    "v_mul_f32 v122, v205, v122\n"
    "v_mfma_f32_16x16x32_bf16 v[84:87], v[4:7], a[0:3], v[84:87]\n"
    "ds_read_b64_tr_b16 v[28:29], v234 offset:57792\n"
    "v_mul_f32 v123, v205, v123\n"
    "v_mul_f32 v92, v204, v92\n"
    "v_mul_f32 v93, v204, v93\n"
    "v_mfma_f32_16x16x32_bf16 v[116:119], v[4:7], a[4:7], v[116:119]\n"
    "ds_read_b64_tr_b16 v[30:31], v234 offset:62400\n"
    "v_mul_f32 v94, v204, v94\n"
    "v_mul_f32 v95, v204, v95\n"
    "s_waitcnt lgkmcnt(8)\n"
    "v_mfma_f32_16x16x32_bf16 v[88:91], v[8:11], a[0:3], v[88:91]\n"
    "ds_read_b64_tr_b16 v[0:1], v234 offset:57824\n"
    "v_mul_f32 v124, v205, v124\n"
    "v_mul_f32 v125, v205, v125\n"
    "v_mul_f32 v126, v205, v126\n"
    "v_mul_f32 v127, v205, v127\n"
    "v_mfma_f32_16x16x32_bf16 v[120:123], v[8:11], a[4:7], v[120:123]\n"
    "ds_read_b64_tr_b16 v[2:3], v234 offset:62432\n"
    "v_mul_f32 v128, v204, v128\n"
    "v_mul_f32 v129, v204, v129\n"
    "v_mfma_f32_16x16x32_bf16 v[92:95], v[12:15], a[0:3], v[92:95]\n"
    "ds_read_b64_tr_b16 v[4:5], v234 offset:57856\n"
    "v_mul_f32 v130, v204, v130\n"
    "v_mul_f32 v131, v204, v131\n"
    "v_mul_f32 v132, v205, v132\n"
    "v_mul_f32 v133, v205, v133\n"
    "v_mul_f32 v134, v205, v134\n"
    "v_mul_f32 v135, v205, v135\n"
    "v_mfma_f32_16x16x32_bf16 v[124:127], v[12:15], a[4:7], v[124:127]\n"
    "ds_read_b64_tr_b16 v[6:7], v234 offset:62464\n"
    "s_waitcnt lgkmcnt(8)\n"
    "v_mfma_f32_16x16x32_bf16 v[128:131], v[16:19], a[0:3], v[128:131]\n"
    "ds_read_b64_tr_b16 v[8:9], v234 offset:57888\n"
    "v_mfma_f32_16x16x32_bf16 v[132:135], v[16:19], a[4:7], v[132:135]\n"
    "ds_read_b64_tr_b16 v[10:11], v234 offset:62496\n"
    "v_mfma_f32_16x16x32_bf16 v[64:67], v[20:23], a[8:11], v[64:67]\n"
    "ds_read_b64_tr_b16 v[12:13], v234 offset:57920\n"
    "v_mfma_f32_16x16x32_bf16 v[96:99], v[20:23], a[12:15], v[96:99]\n"
    "ds_read_b64_tr_b16 v[14:15], v234 offset:62528\n"
    "s_waitcnt lgkmcnt(8)\n"
    "v_mfma_f32_16x16x32_bf16 v[68:71], v[24:27], a[8:11], v[68:71]\n"
    "ds_read_b64_tr_b16 v[16:17], v234 offset:57952\n"
    "v_mfma_f32_16x16x32_bf16 v[100:103], v[24:27], a[12:15], v[100:103]\n"
    "ds_read_b64_tr_b16 v[18:19], v234 offset:62560\n"
    "v_mfma_f32_16x16x32_bf16 v[72:75], v[28:31], a[8:11], v[72:75]\n"
    "ds_read_b64_tr_b16 v[20:21], v234 offset:57984\n"
    "v_mfma_f32_16x16x32_bf16 v[104:107], v[28:31], a[12:15], v[104:107]\n"
    "ds_read_b64_tr_b16 v[22:23], v234 offset:62592\n"
    "s_waitcnt lgkmcnt(8)\n"
    "v_mfma_f32_16x16x32_bf16 v[76:79], v[0:3], a[8:11], v[76:79]\n"
    "ds_read_b64_tr_b16 v[24:25], v235 offset:1408\n"
    "v_mfma_f32_16x16x32_bf16 v[108:111], v[0:3], a[12:15], v[108:111]\n"
    "ds_read_b64_tr_b16 v[26:27], v235 offset:6016\n"
    "v_mfma_f32_16x16x32_bf16 v[80:83], v[4:7], a[8:11], v[80:83]\n"
    "ds_read_b64_tr_b16 v[28:29], v235 offset:1440\n"
    "v_mfma_f32_16x16x32_bf16 v[112:115], v[4:7], a[12:15], v[112:115]\n"
    "ds_read_b64_tr_b16 v[30:31], v235 offset:6048\n"
    "s_waitcnt lgkmcnt(8)\n"
    "v_mfma_f32_16x16x32_bf16 v[84:87], v[8:11], a[8:11], v[84:87]\n"
    "ds_read_b64_tr_b16 v[0:1], v235 offset:1472\n"
    "v_mfma_f32_16x16x32_bf16 v[116:119], v[8:11], a[12:15], v[116:119]\n"
    "ds_read_b64_tr_b16 v[2:3], v235 offset:6080\n"
    "v_mfma_f32_16x16x32_bf16 v[88:91], v[12:15], a[8:11], v[88:91]\n"
    "ds_read_b64_tr_b16 v[4:5], v235 offset:1504\n"
    "v_mfma_f32_16x16x32_bf16 v[120:123], v[12:15], a[12:15], v[120:123]\n"
    "ds_read_b64_tr_b16 v[6:7], v235 offset:6112\n"
    "s_waitcnt lgkmcnt(8)\n"
    "v_mfma_f32_16x16x32_bf16 v[92:95], v[16:19], a[8:11], v[92:95]\n"
    "ds_read_b64_tr_b16 v[8:9], v235 offset:1536\n"
    "v_mfma_f32_16x16x32_bf16 v[124:127], v[16:19], a[12:15], v[124:127]\n"
    "ds_read_b64_tr_b16 v[10:11], v235 offset:6144\n"
    "v_mfma_f32_16x16x32_bf16 v[128:131], v[20:23], a[8:11], v[128:131]\n"
    "ds_read_b64_tr_b16 v[12:13], v235 offset:1568\n"
    "v_mfma_f32_16x16x32_bf16 v[132:135], v[20:23], a[12:15], v[132:135]\n"
    "ds_read_b64_tr_b16 v[14:15], v235 offset:6176\n"
    "s_waitcnt lgkmcnt(8)\n"
    "v_mfma_f32_16x16x32_bf16 v[64:67], v[24:27], a[16:19], v[64:67]\n"
    "ds_read_b64_tr_b16 v[16:17], v235 offset:1600\n"
    "v_mfma_f32_16x16x32_bf16 v[96:99], v[24:27], a[20:23], v[96:99]\n"
    "ds_read_b64_tr_b16 v[18:19], v235 offset:6208\n"
    "v_mfma_f32_16x16x32_bf16 v[68:71], v[28:31], a[16:19], v[68:71]\n"
    "ds_read_b64_tr_b16 v[20:21], v235 offset:1632\n"
    "v_mfma_f32_16x16x32_bf16 v[100:103], v[28:31], a[20:23], v[100:103]\n"
    "ds_read_b64_tr_b16 v[22:23], v235 offset:6240\n"
    "s_waitcnt lgkmcnt(8)\n"
    "v_mfma_f32_16x16x32_bf16 v[72:75], v[0:3], a[16:19], v[72:75]\n"
    "ds_read_b64_tr_b16 v[24:25], v235 offset:1664\n"
    "v_mfma_f32_16x16x32_bf16 v[104:107], v[0:3], a[20:23], v[104:107]\n"
    "ds_read_b64_tr_b16 v[26:27], v235 offset:6272\n"
    "v_mfma_f32_16x16x32_bf16 v[76:79], v[4:7], a[16:19], v[76:79]\n"
    "ds_read_b64_tr_b16 v[28:29], v235 offset:10624\n"
    "v_mfma_f32_16x16x32_bf16 v[108:111], v[4:7], a[20:23], v[108:111]\n"
    "ds_read_b64_tr_b16 v[30:31], v235 offset:15232\n"
    "s_waitcnt lgkmcnt(8)\n"
    "v_mfma_f32_16x16x32_bf16 v[80:83], v[8:11], a[16:19], v[80:83]\n"
    "ds_read_b64_tr_b16 v[0:1], v235 offset:10656\n"
    "v_mfma_f32_16x16x32_bf16 v[112:115], v[8:11], a[20:23], v[112:115]\n"
    "ds_read_b64_tr_b16 v[2:3], v235 offset:15264\n"
    "v_mfma_f32_16x16x32_bf16 v[84:87], v[12:15], a[16:19], v[84:87]\n"
    "ds_read_b64_tr_b16 v[4:5], v235 offset:10688\n"
    "v_mfma_f32_16x16x32_bf16 v[116:119], v[12:15], a[20:23], v[116:119]\n"
    "ds_read_b64_tr_b16 v[6:7], v235 offset:15296\n"
    "s_waitcnt lgkmcnt(8)\n"
    "v_mfma_f32_16x16x32_bf16 v[88:91], v[16:19], a[16:19], v[88:91]\n"
    "ds_read_b64_tr_b16 v[8:9], v235 offset:10720\n"
    "v_mfma_f32_16x16x32_bf16 v[120:123], v[16:19], a[20:23], v[120:123]\n"
    "ds_read_b64_tr_b16 v[10:11], v235 offset:15328\n"
    "v_mfma_f32_16x16x32_bf16 v[92:95], v[20:23], a[16:19], v[92:95]\n"
    "ds_read_b64_tr_b16 v[12:13], v235 offset:10752\n"
    "v_mfma_f32_16x16x32_bf16 v[124:127], v[20:23], a[20:23], v[124:127]\n"
    "ds_read_b64_tr_b16 v[14:15], v235 offset:15360\n"
    "s_waitcnt lgkmcnt(8)\n"
    "v_mfma_f32_16x16x32_bf16 v[128:131], v[24:27], a[16:19], v[128:131]\n"
    "ds_read_b64_tr_b16 v[16:17], v235 offset:10784\n"
    "v_mfma_f32_16x16x32_bf16 v[132:135], v[24:27], a[20:23], v[132:135]\n"
    "ds_read_b64_tr_b16 v[18:19], v235 offset:15392\n"
    "v_mfma_f32_16x16x32_bf16 v[64:67], v[28:31], a[24:27], v[64:67]\n"
    "ds_read_b64_tr_b16 v[20:21], v235 offset:10816\n"
    "v_mfma_f32_16x16x32_bf16 v[96:99], v[28:31], a[28:31], v[96:99]\n"
    "ds_read_b64_tr_b16 v[22:23], v235 offset:15424\n"
    "s_waitcnt lgkmcnt(8)\n"
    "v_mfma_f32_16x16x32_bf16 v[68:71], v[0:3], a[24:27], v[68:71]\n"
    "ds_read_b64_tr_b16 v[24:25], v235 offset:10848\n"
    "v_mfma_f32_16x16x32_bf16 v[100:103], v[0:3], a[28:31], v[100:103]\n"
    "ds_read_b64_tr_b16 v[26:27], v235 offset:15456\n"
    "v_mfma_f32_16x16x32_bf16 v[72:75], v[4:7], a[24:27], v[72:75]\n"
    "ds_read_b64_tr_b16 v[28:29], v235 offset:10880\n"
    "v_mfma_f32_16x16x32_bf16 v[104:107], v[4:7], a[28:31], v[104:107]\n"
    "ds_read_b64_tr_b16 v[30:31], v235 offset:15488\n"
    "s_waitcnt vmcnt(0)\n"
    "s_barrier\n"
    "s_waitcnt lgkmcnt(8)\n"
    "v_mfma_f32_16x16x32_bf16 v[76:79], v[8:11], a[24:27], v[76:79]\n"
    "ds_read_b64_tr_b16 v[0:1], v206 offset:25344\n"
    "v_mfma_f32_16x16x32_bf16 v[108:111], v[8:11], a[28:31], v[108:111]\n"
    "v_mfma_f32_16x16x32_bf16 v[80:83], v[12:15], a[24:27], v[80:83]\n"
    "ds_read_b64_tr_b16 v[4:5], v206 offset:25376\n"
    "v_mfma_f32_16x16x32_bf16 v[112:115], v[12:15], a[28:31], v[112:115]\n"
    "s_waitcnt lgkmcnt(6)\n"
    "v_mfma_f32_16x16x32_bf16 v[84:87], v[16:19], a[24:27], v[84:87]\n"
    "ds_read_b64_tr_b16 v[8:9], v206 offset:25408\n"
    "v_mfma_f32_16x16x32_bf16 v[116:119], v[16:19], a[28:31], v[116:119]\n"
    "v_mfma_f32_16x16x32_bf16 v[88:91], v[20:23], a[24:27], v[88:91]\n"
    "ds_read_b64_tr_b16 v[12:13], v206 offset:25440\n"
    "v_mfma_f32_16x16x32_bf16 v[120:123], v[20:23], a[28:31], v[120:123]\n"
    "s_waitcnt lgkmcnt(4)\n"
    "v_mfma_f32_16x16x32_bf16 v[92:95], v[24:27], a[24:27], v[92:95]\n"
    "ds_read_b64_tr_b16 v[16:17], v206 offset:25472\n"
    "v_mfma_f32_16x16x32_bf16 v[124:127], v[24:27], a[28:31], v[124:127]\n"
    "v_mfma_f32_16x16x32_bf16 v[128:131], v[28:31], a[24:27], v[128:131]\n"
    "ds_read_b64_tr_b16 v[20:21], v206 offset:25504\n"
    "v_mfma_f32_16x16x32_bf16 v[132:135], v[28:31], a[28:31], v[132:135]\n"
    "s_waitcnt lgkmcnt(4)\n"
    "v_mfma_f32_16x16x16_bf16 v[64:67], v[0:1], a[32:33], v[64:67]\n"
    "ds_read_b64_tr_b16 v[24:25], v206 offset:25536\n"
    "v_mfma_f32_16x16x16_bf16 v[96:99], v[0:1], a[36:37], v[96:99]\n"
    "v_mfma_f32_16x16x16_bf16 v[68:71], v[4:5], a[32:33], v[68:71]\n"
    "ds_read_b64_tr_b16 v[28:29], v206 offset:25568\n"
    "v_mfma_f32_16x16x16_bf16 v[100:103], v[4:5], a[36:37], v[100:103]\n"
    "s_waitcnt lgkmcnt(4)\n"
    "v_mfma_f32_16x16x16_bf16 v[72:75], v[8:9], a[32:33], v[72:75]\n"
    "ds_read_b64_tr_b16 v[0:1], v206 offset:25600\n"
    "v_mfma_f32_16x16x16_bf16 v[104:107], v[8:9], a[36:37], v[104:107]\n"
    "v_mfma_f32_16x16x16_bf16 v[76:79], v[12:13], a[32:33], v[76:79]\n"
    "v_mfma_f32_16x16x16_bf16 v[108:111], v[12:13], a[36:37], v[108:111]\n"
    "s_waitcnt lgkmcnt(3)\n"
    "v_mfma_f32_16x16x16_bf16 v[80:83], v[16:17], a[32:33], v[80:83]\n"
    "v_mfma_f32_16x16x16_bf16 v[112:115], v[16:17], a[36:37], v[112:115]\n"
    "v_mfma_f32_16x16x16_bf16 v[84:87], v[20:21], a[32:33], v[84:87]\n"
    "v_mfma_f32_16x16x16_bf16 v[116:119], v[20:21], a[36:37], v[116:119]\n"
    "s_waitcnt lgkmcnt(1)\n"
    "v_mfma_f32_16x16x16_bf16 v[88:91], v[24:25], a[32:33], v[88:91]\n"
    "v_mfma_f32_16x16x16_bf16 v[120:123], v[24:25], a[36:37], v[120:123]\n"
    "v_mfma_f32_16x16x16_bf16 v[92:95], v[28:29], a[32:33], v[92:95]\n"
    "v_mfma_f32_16x16x16_bf16 v[124:127], v[28:29], a[36:37], v[124:127]\n"
    "s_waitcnt lgkmcnt(0)\n"
    "v_mfma_f32_16x16x16_bf16 v[128:131], v[0:1], a[32:33], v[128:131]\n"
    "v_mfma_f32_16x16x16_bf16 v[132:135], v[0:1], a[36:37], v[132:135]\n"
    "s_and_b32 s15, s64, 0xf3\n"
    "s_lshl_b32 s54, s9, 2\n"
    "s_or_b32 s15, s15, s54\n"
    "s_lshl_b32 s15, s15, 2\n"
    "s_add_u32 s74, s15, s11\n"
    "s_mul_i32 s75, s74, 9216\n"
    "s_cmp_eq_u32 s9, s11\n"
    "s_cbranch_scc1 .Lown\n"
    "v_cvt_pk_bf16_f32 v0, v64, v65\n"
    "v_cvt_pk_bf16_f32 v1, v66, v67\n"
    "v_cvt_pk_bf16_f32 v2, v68, v69\n"
    "v_cvt_pk_bf16_f32 v3, v70, v71\n"
    "buffer_store_dwordx4 v[0:3], v246, s[44:47], s75 offen offset:0 sc1\n"
    "v_cvt_pk_bf16_f32 v4, v72, v73\n"
    "v_cvt_pk_bf16_f32 v5, v74, v75\n"
    "v_cvt_pk_bf16_f32 v6, v76, v77\n"
    "v_cvt_pk_bf16_f32 v7, v78, v79\n"
    "buffer_store_dwordx4 v[4:7], v246, s[44:47], s75 offen offset:1024 sc1\n"
    "v_cvt_pk_bf16_f32 v8, v80, v81\n"
    "v_cvt_pk_bf16_f32 v9, v82, v83\n"
    "v_cvt_pk_bf16_f32 v10, v84, v85\n"
    "v_cvt_pk_bf16_f32 v11, v86, v87\n"
    "buffer_store_dwordx4 v[8:11], v246, s[44:47], s75 offen offset:2048 sc1\n"
    "v_cvt_pk_bf16_f32 v12, v88, v89\n"
    "v_cvt_pk_bf16_f32 v13, v90, v91\n"
    "v_cvt_pk_bf16_f32 v14, v92, v93\n"
    "v_cvt_pk_bf16_f32 v15, v94, v95\n"
    "buffer_store_dwordx4 v[12:15], v246, s[44:47], s75 offen offset:3072 sc1\n"
    "v_cvt_pk_bf16_f32 v16, v96, v97\n"
    "v_cvt_pk_bf16_f32 v17, v98, v99\n"
    "v_cvt_pk_bf16_f32 v18, v100, v101\n"
    "v_cvt_pk_bf16_f32 v19, v102, v103\n"
    "s_add_u32 s75, s75, 4096\n"
    "buffer_store_dwordx4 v[16:19], v246, s[44:47], s75 offen offset:0 sc1\n"
    "v_cvt_pk_bf16_f32 v20, v104, v105\n"
    "v_cvt_pk_bf16_f32 v21, v106, v107\n"
    "v_cvt_pk_bf16_f32 v22, v108, v109\n"
    "v_cvt_pk_bf16_f32 v23, v110, v111\n"
    "buffer_store_dwordx4 v[20:23], v246, s[44:47], s75 offen offset:1024 sc1\n"
    "v_cvt_pk_bf16_f32 v24, v112, v113\n"
    "v_cvt_pk_bf16_f32 v25, v114, v115\n"
    "v_cvt_pk_bf16_f32 v26, v116, v117\n"
    "v_cvt_pk_bf16_f32 v27, v118, v119\n"
    "buffer_store_dwordx4 v[24:27], v246, s[44:47], s75 offen offset:2048 sc1\n"
    "v_cvt_pk_bf16_f32 v28, v120, v121\n"
    "v_cvt_pk_bf16_f32 v29, v122, v123\n"
    "v_cvt_pk_bf16_f32 v30, v124, v125\n"
    "v_cvt_pk_bf16_f32 v31, v126, v127\n"
    "buffer_store_dwordx4 v[28:31], v246, s[44:47], s75 offen offset:3072 sc1\n"
    "v_cvt_pk_bf16_f32 v32, v128, v129\n"
    "v_cvt_pk_bf16_f32 v33, v130, v131\n"
    "v_cvt_pk_bf16_f32 v34, v132, v133\n"
    "v_cvt_pk_bf16_f32 v35, v134, v135\n"
    "s_add_u32 s75, s75, 4096\n"
    "buffer_store_dwordx4 v[32:35], v246, s[44:47], s75 offen offset:0 sc1\n"
    ".Lownr:\n"
    "s_lshl_b32 s15, s8, 2\n"
    "s_add_u32 s15, s15, s9\n"
    "s_cmp_eq_u32 s15, 3\n"
    "s_mov_b32 s15, 0x8000\n"
    "s_cselect_b32 s15, 1024, s15\n"
    "v_lshl_add_u32 v148, v192, 5, s15\n"
    "buffer_load_dwordx4 v[140:143], v148, s[68:71], 0 offen offset:0 sc1\n"
    "buffer_load_dwordx4 v[144:147], v148, s[68:71], 0 offen offset:16 sc1\n"
    "s_lshl_b32 s15, s9, 12\n"
    "s_add_u32 s15, s15, 76032\n"
    "v_add_u32 v196, s15, v246\n"
    "v_add_u32 v197, 92416, v246\n"
    "v_and_b32 v184, 15, v192\n"
    "v_mul_u32_u24 v184, 1152, v184\n"
    "v_lshrrev_b32 v185, 5, v192\n"
    "v_lshl_add_u32 v184, v185, 4, v184\n"
    "v_bfe_u32 v185, v192, 4, 1\n"
    "v_lshl_add_u32 v186, v185, 5, v184\n"
    "s_mul_i32 s15, s13, 18432\n"
    "s_lshl_b32 s55, s14, 7\n"
    "s_add_u32 s15, s15, s55\n"
    "v_add_u32 v199, s15, v186\n"
    "v_mul_u32_u24 v185, 18432, v185\n"
    "s_cmp_eq_u32 s9, 0\n"
    "s_mov_b32 s54, 0x80000000\n"
    "s_cselect_b32 s54, 256, s54\n"
    "v_add3_u32 v200, v184, v185, s54\n"
    "s_barrier\n"
    "buffer_load_dwordx4 v[96:99], v208, s[44:47], s81 offen offset:0 sc1\n"
    "buffer_load_dwordx4 v[100:103], v208, s[44:47], s81 offen offset:1024 sc1\n"
    "buffer_load_dwordx4 v[104:107], v209, s[44:47], s81 offen offset:0 sc1\n"
    "buffer_load_dwordx4 v[108:111], v208, s[44:47], s82 offen offset:0 sc1\n"
    "buffer_load_dwordx4 v[112:115], v208, s[44:47], s82 offen offset:1024 sc1\n"
    "buffer_load_dwordx4 v[116:119], v209, s[44:47], s82 offen offset:0 sc1\n"
    "buffer_load_dwordx4 v[120:123], v208, s[44:47], s83 offen offset:0 sc1\n"
    "buffer_load_dwordx4 v[124:127], v208, s[44:47], s83 offen offset:1024 sc1\n"
    "buffer_load_dwordx4 v[128:131], v209, s[44:47], s83 offen offset:0 sc1\n"
    "ds_read_b128 v[64:67], v196 offset:0\n"
    "ds_read_b128 v[68:71], v196 offset:1024\n"
    "ds_read_b128 v[72:75], v196 offset:2048\n"
    "ds_read_b128 v[76:79], v196 offset:3072\n"
    "ds_read_b128 v[80:83], v197 offset:0\n"
    "ds_read_b128 v[84:87], v197 offset:1024\n"
    "v_mov_b32 v184, 0x3f80\n"
    "v_mov_b32 v185, 0x3f800000\n"
    "s_waitcnt vmcnt(6)\n"
    "v_max3_u32 v186, v96, v97, v98\n"
    "v_max3_u32 v187, v99, v100, v101\n"
    "v_max3_u32 v186, v186, v102, v103\n"
    "v_max3_u32 v187, v187, v104, v105\n"
    "v_max3_u32 v186, v186, v106, v107\n"
    "v_max_u32 v186, v186, v187\n"
    "v_cmp_eq_u32 vcc, -1, v186\n"
    "s_nop 1\n"
    "s_cmp_eq_u64 vcc, 0\n"
    "s_cbranch_scc0 .Lin0\n"
    ".Linok0:\n"
    "s_waitcnt lgkmcnt(5)\n"
    "v_dot2_f32_bf16 v64, v96, v184, v64\n"
    "v_dot2_f32_bf16 v65, v96, v185, v65\n"
    "v_dot2_f32_bf16 v66, v97, v184, v66\n"
    "v_dot2_f32_bf16 v67, v97, v185, v67\n"
    "s_waitcnt lgkmcnt(4)\n"
    "v_dot2_f32_bf16 v68, v98, v184, v68\n"
    "v_dot2_f32_bf16 v69, v98, v185, v69\n"
    "v_dot2_f32_bf16 v70, v99, v184, v70\n"
    "v_dot2_f32_bf16 v71, v99, v185, v71\n"
    "s_waitcnt lgkmcnt(3)\n"
    "v_dot2_f32_bf16 v72, v100, v184, v72\n"
    "v_dot2_f32_bf16 v73, v100, v185, v73\n"
    "v_dot2_f32_bf16 v74, v101, v184, v74\n"
    "v_dot2_f32_bf16 v75, v101, v185, v75\n"
    "s_waitcnt lgkmcnt(2)\n"
    "v_dot2_f32_bf16 v76, v102, v184, v76\n"
    "v_dot2_f32_bf16 v77, v102, v185, v77\n"
    "v_dot2_f32_bf16 v78, v103, v184, v78\n"
    "v_dot2_f32_bf16 v79, v103, v185, v79\n"
    "s_waitcnt lgkmcnt(1)\n"
    "v_dot2_f32_bf16 v80, v104, v184, v80\n"
    "v_dot2_f32_bf16 v81, v104, v185, v81\n"
    "v_dot2_f32_bf16 v82, v105, v184, v82\n"
    "v_dot2_f32_bf16 v83, v105, v185, v83\n"
    "s_waitcnt lgkmcnt(0)\n"
    "v_dot2_f32_bf16 v84, v106, v184, v84\n"
    "v_dot2_f32_bf16 v85, v106, v185, v85\n"
    "v_dot2_f32_bf16 v86, v107, v184, v86\n"
    "v_dot2_f32_bf16 v87, v107, v185, v87\n"
    "s_waitcnt vmcnt(3)\n"
    "v_max3_u32 v186, v108, v109, v110\n"
    "v_max3_u32 v187, v111, v112, v113\n"
    "v_max3_u32 v186, v186, v114, v115\n"
    "v_max3_u32 v187, v187, v116, v117\n"
    "v_max3_u32 v186, v186, v118, v119\n"
    "v_max_u32 v186, v186, v187\n"
    "v_cmp_eq_u32 vcc, -1, v186\n"
    "s_nop 1\n"
    "s_cmp_eq_u64 vcc, 0\n"
    "s_cbranch_scc0 .Lin1\n"
    ".Linok1:\n"
    "v_dot2_f32_bf16 v64, v108, v184, v64\n"
    "v_dot2_f32_bf16 v65, v108, v185, v65\n"
    "v_dot2_f32_bf16 v66, v109, v184, v66\n"
    "v_dot2_f32_bf16 v67, v109, v185, v67\n"
    "v_dot2_f32_bf16 v68, v110, v184, v68\n"
    "v_dot2_f32_bf16 v69, v110, v185, v69\n"
    "v_dot2_f32_bf16 v70, v111, v184, v70\n"
    "v_dot2_f32_bf16 v71, v111, v185, v71\n"
    "v_dot2_f32_bf16 v72, v112, v184, v72\n"
    "v_dot2_f32_bf16 v73, v112, v185, v73\n"
    "v_dot2_f32_bf16 v74, v113, v184, v74\n"
    "v_dot2_f32_bf16 v75, v113, v185, v75\n"
    "v_dot2_f32_bf16 v76, v114, v184, v76\n"
    "v_dot2_f32_bf16 v77, v114, v185, v77\n"
    "v_dot2_f32_bf16 v78, v115, v184, v78\n"
    "v_dot2_f32_bf16 v79, v115, v185, v79\n"
    "v_dot2_f32_bf16 v80, v116, v184, v80\n"
    "v_dot2_f32_bf16 v81, v116, v185, v81\n"
    "v_dot2_f32_bf16 v82, v117, v184, v82\n"
    "v_dot2_f32_bf16 v83, v117, v185, v83\n"
    "v_dot2_f32_bf16 v84, v118, v184, v84\n"
    "v_dot2_f32_bf16 v85, v118, v185, v85\n"
    "v_dot2_f32_bf16 v86, v119, v184, v86\n"
    "v_dot2_f32_bf16 v87, v119, v185, v87\n"
    "s_waitcnt vmcnt(0)\n"
    "v_max3_u32 v186, v120, v121, v122\n"
    "v_max3_u32 v187, v123, v124, v125\n"
    "v_max3_u32 v186, v186, v126, v127\n"
    "v_max3_u32 v187, v187, v128, v129\n"
    "v_max3_u32 v186, v186, v130, v131\n"
    "v_max_u32 v186, v186, v187\n"
    "v_cmp_eq_u32 vcc, -1, v186\n"
    "s_nop 1\n"
    "s_cmp_eq_u64 vcc, 0\n"
    "s_cbranch_scc0 .Lin2\n"
    ".Linok2:\n"
    "v_dot2_f32_bf16 v64, v120, v184, v64\n"
    "v_dot2_f32_bf16 v65, v120, v185, v65\n"
    "v_dot2_f32_bf16 v66, v121, v184, v66\n"
    "v_dot2_f32_bf16 v67, v121, v185, v67\n"
    "v_dot2_f32_bf16 v68, v122, v184, v68\n"
    "v_dot2_f32_bf16 v69, v122, v185, v69\n"
    "v_dot2_f32_bf16 v70, v123, v184, v70\n"
    "v_dot2_f32_bf16 v71, v123, v185, v71\n"
    "v_dot2_f32_bf16 v72, v124, v184, v72\n"
    "v_dot2_f32_bf16 v73, v124, v185, v73\n"
    "v_dot2_f32_bf16 v74, v125, v184, v74\n"
    "v_dot2_f32_bf16 v75, v125, v185, v75\n"
    "v_dot2_f32_bf16 v76, v126, v184, v76\n"
    "v_dot2_f32_bf16 v77, v126, v185, v77\n"
    "v_dot2_f32_bf16 v78, v127, v184, v78\n"
    "v_dot2_f32_bf16 v79, v127, v185, v79\n"
    "v_dot2_f32_bf16 v80, v128, v184, v80\n"
    "v_dot2_f32_bf16 v81, v128, v185, v81\n"
    "v_dot2_f32_bf16 v82, v129, v184, v82\n"
    "v_dot2_f32_bf16 v83, v129, v185, v83\n"
    "v_dot2_f32_bf16 v84, v130, v184, v84\n"
    "v_dot2_f32_bf16 v85, v130, v185, v85\n"
    "v_dot2_f32_bf16 v86, v131, v184, v86\n"
    "v_dot2_f32_bf16 v87, v131, v185, v87\n"
    "v_cvt_pk_bf16_f32 v40, v64, v65\n"
    "v_cvt_pk_bf16_f32 v41, v66, v67\n"
    "v_cvt_pk_bf16_f32 v42, v68, v69\n"
    "v_cvt_pk_bf16_f32 v43, v70, v71\n"
    "v_cvt_pk_bf16_f32 v44, v72, v73\n"
    "v_cvt_pk_bf16_f32 v45, v74, v75\n"
    "v_cvt_pk_bf16_f32 v46, v76, v77\n"
    "v_cvt_pk_bf16_f32 v47, v78, v79\n"
    "v_cvt_pk_bf16_f32 v48, v80, v81\n"
    "v_cvt_pk_bf16_f32 v49, v82, v83\n"
    "v_cvt_pk_bf16_f32 v50, v84, v85\n"
    "v_cvt_pk_bf16_f32 v51, v86, v87\n"
    "v_permlane16_swap_b32 v40, v42\n"
    "v_permlane16_swap_b32 v41, v43\n"
    "buffer_store_dwordx4 v[40:43], v199, s[40:43], 0 offen offset:0\n"
    "v_permlane16_swap_b32 v44, v46\n"
    "v_permlane16_swap_b32 v45, v47\n"
    "buffer_store_dwordx4 v[44:47], v199, s[40:43], 0 offen offset:64\n"
    "v_permlane16_swap_b32 v48, v50\n"
    "v_permlane16_swap_b32 v49, v51\n"
    "buffer_store_dwordx4 v[48:51], v200, s[40:43], 0 offen offset:0\n"
    "s_lshl_b32 s15, s8, 2\n"
    "s_add_u32 s15, s15, s9\n"
    "s_cmp_eq_u32 s15, 3\n"
    "s_cbranch_scc1 .Lepoch\n"
    "s_endpgm\n"
    ".Lepoch:\n"
    "s_mov_b32 s73, 40000\n"
    ".Lepochl:\n"
    "v_cmp_ne_u64 s[54:55], s[48:49], v[140:141]\n"
    "v_cmp_ne_u64 vcc, s[48:49], v[142:143]\n"
    "s_nop 1\n"
    "s_or_b64 s[54:55], vcc, s[54:55]\n"
    "v_cmp_ne_u64 vcc, s[48:49], v[144:145]\n"
    "s_nop 1\n"
    "s_or_b64 s[54:55], vcc, s[54:55]\n"
    "v_cmp_ne_u64 vcc, s[48:49], v[146:147]\n"
    "s_nop 1\n"
    "s_or_b64 s[54:55], vcc, s[54:55]\n"
    "s_cbranch_scc0 .Lepochok\n"
    "s_sleep 8\n"
    "buffer_load_dwordx4 v[140:143], v148, s[68:71], 0 offen offset:0 sc1\n"
    "buffer_load_dwordx4 v[144:147], v148, s[68:71], 0 offen offset:16 sc1\n"
    "s_waitcnt vmcnt(0)\n"
    "s_sub_u32 s73, s73, 1\n"
    "s_cbranch_scc0 .Lepochl\n"
    ".Lepochok:\n"
    "s_mov_b32 s15, 0\n"
    "v_lshl_add_u32 v225, v192, 24, s15\n"
    "v_mov_b32 v226, s48\n"
    "v_mov_b32 v227, s49\n"
    "buffer_store_dwordx2 v[226:227], v225, s[68:71], 0 offen sc1\n"
    "s_endpgm\n"
    ".Lmainend:\n"
    "s_nop 15\n"
    "s_nop 15\n"
    ".Lpoll0:\n"
    "s_nop 4\n"
    "s_mov_b32 s73, 40000\n"
    ".Lpolll0:\n"
    "s_nop 4\n"
    "s_sleep 4\n"
    "global_load_dwordx2 v[226:227], v225, s[50:51] sc1\n"
    "s_waitcnt vmcnt(0)\n"
    "v_cmp_ne_u64 s[54:55], s[48:49], v[226:227]\n"
    "s_nop 1\n"
    "s_cmp_eq_u64 s[54:55], 0\n"
    "s_cbranch_scc1 .Lpollok0\n"
    "s_sub_u32 s73, s73, 1\n"
    "s_cbranch_scc0 .Lpolll0\n"
    "s_branch .Lpollok0\n"
    ".Lpoll1:\n"
    "s_nop 4\n"
    "s_mov_b32 s73, 40000\n"
    ".Lpolll1:\n"
    "s_nop 4\n"
    "s_sleep 4\n"
    "global_load_dwordx2 v[226:227], v225, s[50:51] sc1\n"
    "s_waitcnt vmcnt(0)\n"
    "v_cmp_ne_u64 s[54:55], s[48:49], v[226:227]\n"
    "s_nop 1\n"
    "s_cmp_eq_u64 s[54:55], 0\n"
    "s_cbranch_scc1 .Lpollok1\n"
    "s_sub_u32 s73, s73, 1\n"
    "s_cbranch_scc0 .Lpolll1\n"
    "s_branch .Lpollok1\n"
    ".Lpoll2:\n"
    "s_nop 4\n"
    "s_mov_b32 s73, 40000\n"
    ".Lpolll2:\n"
    "s_nop 4\n"
    "s_sleep 4\n"
    "global_load_dwordx2 v[226:227], v225, s[50:51] sc1\n"
    "s_waitcnt vmcnt(0)\n"
    "v_cmp_ne_u64 s[54:55], s[48:49], v[226:227]\n"
    "s_nop 1\n"
    "s_cmp_eq_u64 s[54:55], 0\n"
    "s_cbranch_scc1 .Lpollok2\n"
    "s_sub_u32 s73, s73, 1\n"
    "s_cbranch_scc0 .Lpolll2\n"
    "s_branch .Lpollok2\n"
    ".Lpoll3:\n"
    "s_nop 4\n"
    "s_mov_b32 s73, 40000\n"
    ".Lpolll3:\n"
    "s_nop 4\n"
    "s_sleep 4\n"
    "global_load_dwordx2 v[226:227], v225, s[50:51] sc1\n"
    "s_waitcnt vmcnt(0)\n"
    "v_cmp_ne_u64 s[54:55], s[48:49], v[226:227]\n"
    "s_nop 1\n"
    "s_cmp_eq_u64 s[54:55], 0\n"
    "s_cbranch_scc1 .Lpollok3\n"
    "s_sub_u32 s73, s73, 1\n"
    "s_cbranch_scc0 .Lpolll3\n"
    "s_branch .Lpollok3\n"
    ".Lown:\n"
    "s_nop 4\n"
    "v_add_u32 v184, 76032, v246\n"
    "ds_write_b128 v184, v[64:67] offset:0\n"
    "ds_write_b128 v184, v[68:71] offset:1024\n"
    "ds_write_b128 v184, v[72:75] offset:2048\n"
    "ds_write_b128 v184, v[76:79] offset:3072\n"
    "ds_write_b128 v184, v[80:83] offset:4096\n"
    "ds_write_b128 v184, v[84:87] offset:5120\n"
    "ds_write_b128 v184, v[88:91] offset:6144\n"
    "ds_write_b128 v184, v[92:95] offset:7168\n"
    "ds_write_b128 v184, v[96:99] offset:8192\n"
    "ds_write_b128 v184, v[100:103] offset:9216\n"
    "ds_write_b128 v184, v[104:107] offset:10240\n"
    "ds_write_b128 v184, v[108:111] offset:11264\n"
    "ds_write_b128 v184, v[112:115] offset:12288\n"
    "ds_write_b128 v184, v[116:119] offset:13312\n"
    "ds_write_b128 v184, v[120:123] offset:14336\n"
    "ds_write_b128 v184, v[124:127] offset:15360\n"
    "ds_write_b128 v184, v[128:131] offset:16384\n"
    "ds_write_b128 v184, v[132:135] offset:17408\n"
    "s_waitcnt lgkmcnt(0)\n"
    "s_branch .Lownr\n"
    ".Lin0:\n"
    "s_nop 4\n"
    "s_mov_b32 s73, 60000\n"
    ".Linl0:\n"
    "s_nop 4\n"
    "buffer_load_dwordx4 v[96:99], v208, s[44:47], s81 offen offset:0 sc1\n"
    "buffer_load_dwordx4 v[100:103], v208, s[44:47], s81 offen offset:1024 sc1\n"
    "buffer_load_dwordx4 v[104:107], v209, s[44:47], s81 offen offset:0 sc1\n"
    "s_waitcnt vmcnt(0)\n"
    "v_max3_u32 v186, v96, v97, v98\n"
    "v_max3_u32 v187, v99, v100, v101\n"
    "v_max3_u32 v186, v186, v102, v103\n"
    "v_max3_u32 v187, v187, v104, v105\n"
    "v_max3_u32 v186, v186, v106, v107\n"
    "v_max_u32 v186, v186, v187\n"
    "v_cmp_eq_u32 vcc, -1, v186\n"
    "s_nop 1\n"
    "s_cmp_eq_u64 vcc, 0\n"
    "s_cbranch_scc1 .Linok0\n"
    "s_sub_u32 s73, s73, 1\n"
    "s_cbranch_scc0 .Linl0\n"
    "s_branch .Linok0\n"
    ".Lin1:\n"
    "s_nop 4\n"
    "s_mov_b32 s73, 60000\n"
    ".Linl1:\n"
    "s_nop 4\n"
    "buffer_load_dwordx4 v[108:111], v208, s[44:47], s82 offen offset:0 sc1\n"
    "buffer_load_dwordx4 v[112:115], v208, s[44:47], s82 offen offset:1024 sc1\n"
    "buffer_load_dwordx4 v[116:119], v209, s[44:47], s82 offen offset:0 sc1\n"
    "s_waitcnt vmcnt(0)\n"
    "v_max3_u32 v186, v108, v109, v110\n"
    "v_max3_u32 v187, v111, v112, v113\n"
    "v_max3_u32 v186, v186, v114, v115\n"
    "v_max3_u32 v187, v187, v116, v117\n"
    "v_max3_u32 v186, v186, v118, v119\n"
    "v_max_u32 v186, v186, v187\n"
    "v_cmp_eq_u32 vcc, -1, v186\n"
    "s_nop 1\n"
    "s_cmp_eq_u64 vcc, 0\n"
    "s_cbranch_scc1 .Linok1\n"
    "s_sub_u32 s73, s73, 1\n"
    "s_cbranch_scc0 .Linl1\n"
    "s_branch .Linok1\n"
    ".Lin2:\n"
    "s_nop 4\n"
    "s_mov_b32 s73, 60000\n"
    ".Linl2:\n"
    "s_nop 4\n"
    "buffer_load_dwordx4 v[120:123], v208, s[44:47], s83 offen offset:0 sc1\n"
    "buffer_load_dwordx4 v[124:127], v208, s[44:47], s83 offen offset:1024 sc1\n"
    "buffer_load_dwordx4 v[128:131], v209, s[44:47], s83 offen offset:0 sc1\n"
    "s_waitcnt vmcnt(0)\n"
    "v_max3_u32 v186, v120, v121, v122\n"
    "v_max3_u32 v187, v123, v124, v125\n"
    "v_max3_u32 v186, v186, v126, v127\n"
    "v_max3_u32 v187, v187, v128, v129\n"
    "v_max3_u32 v186, v186, v130, v131\n"
    "v_max_u32 v186, v186, v187\n"
    "v_cmp_eq_u32 vcc, -1, v186\n"
    "s_nop 1\n"
    "s_cmp_eq_u64 vcc, 0\n"
    "s_cbranch_scc1 .Linok2\n"
    "s_sub_u32 s73, s73, 1\n"
    "s_cbranch_scc0 .Linl2\n"
    "s_branch .Linok2\n"
    ".Lfb:\n"
    "s_nop 4\n"
    "s_mov_b64 s[90:91], 0\n"
    "v_and_b32 v146, 31, v192\n"
    "v_subrev_u32 v147, 4, v146\n"
    "v_cmp_gt_u32 vcc, 20, v146\n"
    "s_nop 1\n"
    "v_cndmask_b32 v147, v147, v146, vcc\n"
    "v_min_u32 v147, 20, v147\n"
    "v_lshlrev_b32 v147, 1, v147\n"
    "v_lshrrev_b32 v144, 5, v192\n"
    "v_mul_u32_u24 v144, 192, v144\n"
    "v_add_u32 v147, v147, v144\n"
    "v_add_u32 v147, 4096, v147\n"
    "v_lshlrev_b32 v146, 3, v146\n"
    "v_add_u32 v146, 10240, v146\n"
    "v_mov_b32 v144, 0xff800000\n"
    "v_mov_b32 v145, 0xff800000\n"
    "v_mov_b32 v32, 0\n"
    "v_mov_b32 v33, 0\n"
    "v_mov_b32 v34, 0\n"
    "v_mov_b32 v35, 0\n"
    "v_mov_b32 v36, 0\n"
    "v_mov_b32 v37, 0\n"
    "v_mov_b32 v38, 0\n"
    "v_mov_b32 v39, 0\n"
    "v_mov_b32 v40, 0\n"
    "v_mov_b32 v41, 0\n"
    "v_mov_b32 v42, 0\n"
    "v_mov_b32 v43, 0\n"
    "v_mov_b32 v44, 0\n"
    "v_mov_b32 v45, 0\n"
    "v_mov_b32 v46, 0\n"
    "v_mov_b32 v47, 0\n"
    "v_mov_b32 v48, 0\n"
    "v_mov_b32 v49, 0\n"
    "v_mov_b32 v50, 0\n"
    "v_mov_b32 v51, 0\n"
    "v_mov_b32 v52, 0\n"
    "v_mov_b32 v53, 0\n"
    "v_mov_b32 v54, 0\n"
    "v_mov_b32 v55, 0\n"
    "v_mov_b32 v56, 0\n"
    "v_mov_b32 v57, 0\n"
    "v_mov_b32 v58, 0\n"
    "v_mov_b32 v59, 0\n"
    "v_mov_b32 v60, 0\n"
    "v_mov_b32 v61, 0\n"
    "v_mov_b32 v62, 0\n"
    "v_mov_b32 v63, 0\n"
    "s_mov_b32 s73, 0\n"
    ".Lfbl0:\n"
    "s_nop 4\n"
    "s_lshr_b32 s15, s73, 2\n"
    "s_mul_i32 s15, s15, 12288\n"
    "s_and_b32 s54, s73, 3\n"
    "s_lshl_b32 s96, s54, 10\n"
    "s_add_u32 s96, s96, s15\n"
    "buffer_load_dwordx4 v[128:131], v246, s[28:31], s96 offen sc1\n"
    "s_lshl_b32 s96, s54, 8\n"
    "s_add_u32 s96, s96, s15\n"
    "buffer_load_dwordx2 v[136:137], v146, s[28:31], s96 offen sc1\n"
    "s_waitcnt vmcnt(0)\n"
    "v_mfma_f32_32x32x4_2b_bf16 v[64:95], v[136:137], v[220:221], 0\n"
    "s_nop 15\n"
    "s_nop 1\n"
    "v_mfma_f32_32x32x16_bf16 v[64:79], v[128:131], v[212:215], v[64:79]\n"
    "v_mfma_f32_32x32x16_bf16 v[80:95], v[128:131], v[216:219], v[80:95]\n"
    "s_nop 10\n"
    "v_max_f32 v144, v144, v64\n"
    "v_max_f32 v144, v144, v65\n"
    "v_max_f32 v144, v144, v66\n"
    "v_max_f32 v144, v144, v67\n"
    "v_max_f32 v144, v144, v68\n"
    "v_max_f32 v144, v144, v69\n"
    "v_max_f32 v144, v144, v70\n"
    "v_max_f32 v144, v144, v71\n"
    "v_max_f32 v144, v144, v72\n"
    "v_max_f32 v144, v144, v73\n"
    "v_max_f32 v144, v144, v74\n"
    "v_max_f32 v144, v144, v75\n"
    "v_max_f32 v144, v144, v76\n"
    "v_max_f32 v144, v144, v77\n"
    "v_max_f32 v144, v144, v78\n"
    "v_max_f32 v144, v144, v79\n"
    "v_max_f32 v145, v145, v80\n"
    "v_max_f32 v145, v145, v81\n"
    "v_max_f32 v145, v145, v82\n"
    "v_max_f32 v145, v145, v83\n"
    "v_max_f32 v145, v145, v84\n"
    "v_max_f32 v145, v145, v85\n"
    "v_max_f32 v145, v145, v86\n"
    "v_max_f32 v145, v145, v87\n"
    "v_max_f32 v145, v145, v88\n"
    "v_max_f32 v145, v145, v89\n"
    "v_max_f32 v145, v145, v90\n"
    "v_max_f32 v145, v145, v91\n"
    "v_max_f32 v145, v145, v92\n"
    "v_max_f32 v145, v145, v93\n"
    "v_max_f32 v145, v145, v94\n"
    "v_max_f32 v145, v145, v95\n"
    "s_nop 15\n"
    "s_nop 15\n"
    "s_add_u32 s73, s73, 1\n"
    "s_cmp_lt_u32 s73, 64\n"
    "s_cbranch_scc1 .Lfbl0\n"
    "v_mov_b32 v64, v144\n"
    "s_nop 1\n"
    "v_permlane32_swap_b32 v64, v144\n"
    "v_max_f32 v144, v64, v144\n"
    "v_mov_b32 v64, v145\n"
    "s_nop 1\n"
    "v_permlane32_swap_b32 v64, v145\n"
    "v_max_f32 v145, v64, v145\n"
    "s_mov_b32 s73, 0\n"
    ".Lfbl1:\n"
    "s_nop 4\n"
    "s_lshr_b32 s15, s73, 2\n"
    "s_mul_i32 s15, s15, 12288\n"
    "s_and_b32 s54, s73, 3\n"
    "s_lshl_b32 s96, s54, 10\n"
    "s_add_u32 s96, s96, s15\n"
    "buffer_load_dwordx4 v[128:131], v246, s[28:31], s96 offen sc1\n"
    "s_lshl_b32 s96, s54, 8\n"
    "s_add_u32 s96, s96, s15\n"
    "buffer_load_dwordx2 v[136:137], v146, s[28:31], s96 offen sc1\n"
    "s_mul_i32 s97, s54, 1536\n"
    "s_add_u32 s97, s97, s15\n"
    "buffer_load_ushort v96, v147, s[28:31], s97 offen offset:0 sc1\n"
    "buffer_load_ushort v97, v147, s[28:31], s97 offen offset:48 sc1\n"
    "buffer_load_ushort v98, v147, s[28:31], s97 offen offset:96 sc1\n"
    "buffer_load_ushort v99, v147, s[28:31], s97 offen offset:144 sc1\n"
    "buffer_load_ushort v100, v147, s[28:31], s97 offen offset:384 sc1\n"
    "buffer_load_ushort v101, v147, s[28:31], s97 offen offset:432 sc1\n"
    "buffer_load_ushort v102, v147, s[28:31], s97 offen offset:480 sc1\n"
    "buffer_load_ushort v103, v147, s[28:31], s97 offen offset:528 sc1\n"
    "buffer_load_ushort v104, v147, s[28:31], s97 offen offset:768 sc1\n"
    "buffer_load_ushort v105, v147, s[28:31], s97 offen offset:816 sc1\n"
    "buffer_load_ushort v106, v147, s[28:31], s97 offen offset:864 sc1\n"
    "buffer_load_ushort v107, v147, s[28:31], s97 offen offset:912 sc1\n"
    "buffer_load_ushort v108, v147, s[28:31], s97 offen offset:1152 sc1\n"
    "buffer_load_ushort v109, v147, s[28:31], s97 offen offset:1200 sc1\n"
    "buffer_load_ushort v110, v147, s[28:31], s97 offen offset:1248 sc1\n"
    "buffer_load_ushort v111, v147, s[28:31], s97 offen offset:1296 sc1\n"
    "s_waitcnt vmcnt(0)\n"
    "v_lshl_or_b32 v184, v97, 16, v96\n"
    "v_lshl_or_b32 v185, v99, 16, v98\n"
    "v_lshl_or_b32 v186, v101, 16, v100\n"
    "v_lshl_or_b32 v187, v103, 16, v102\n"
    "v_lshl_or_b32 v188, v105, 16, v104\n"
    "v_lshl_or_b32 v189, v107, 16, v106\n"
    "v_lshl_or_b32 v190, v109, 16, v108\n"
    "v_lshl_or_b32 v191, v111, 16, v110\n"
    "v_mfma_f32_32x32x4_2b_bf16 v[64:95], v[136:137], v[220:221], 0\n"
    "s_nop 15\n"
    "s_nop 1\n"
    "v_mfma_f32_32x32x16_bf16 v[64:79], v[128:131], v[212:215], v[64:79]\n"
    "v_mfma_f32_32x32x16_bf16 v[80:95], v[128:131], v[216:219], v[80:95]\n"
    "s_nop 10\n"
    "v_sub_f32 v64, v64, v144\n"
    "v_sub_f32 v65, v65, v144\n"
    "v_sub_f32 v66, v66, v144\n"
    "v_sub_f32 v67, v67, v144\n"
    "v_sub_f32 v68, v68, v144\n"
    "v_sub_f32 v69, v69, v144\n"
    "v_sub_f32 v70, v70, v144\n"
    "v_sub_f32 v71, v71, v144\n"
    "v_sub_f32 v72, v72, v144\n"
    "v_sub_f32 v73, v73, v144\n"
    "v_sub_f32 v74, v74, v144\n"
    "v_sub_f32 v75, v75, v144\n"
    "v_sub_f32 v76, v76, v144\n"
    "v_sub_f32 v77, v77, v144\n"
    "v_sub_f32 v78, v78, v144\n"
    "v_sub_f32 v79, v79, v144\n"
    "v_sub_f32 v80, v80, v145\n"
    "v_sub_f32 v81, v81, v145\n"
    "v_sub_f32 v82, v82, v145\n"
    "v_sub_f32 v83, v83, v145\n"
    "v_sub_f32 v84, v84, v145\n"
    "v_sub_f32 v85, v85, v145\n"
    "v_sub_f32 v86, v86, v145\n"
    "v_sub_f32 v87, v87, v145\n"
    "v_sub_f32 v88, v88, v145\n"
    "v_sub_f32 v89, v89, v145\n"
    "v_sub_f32 v90, v90, v145\n"
    "v_sub_f32 v91, v91, v145\n"
    "v_sub_f32 v92, v92, v145\n"
    "v_sub_f32 v93, v93, v145\n"
    "v_sub_f32 v94, v94, v145\n"
    "v_sub_f32 v95, v95, v145\n"
    "v_exp_f32 v64, v64\n"
    "v_exp_f32 v65, v65\n"
    "v_exp_f32 v66, v66\n"
    "v_exp_f32 v67, v67\n"
    "v_exp_f32 v68, v68\n"
    "v_exp_f32 v69, v69\n"
    "v_exp_f32 v70, v70\n"
    "v_exp_f32 v71, v71\n"
    "v_exp_f32 v72, v72\n"
    "v_exp_f32 v73, v73\n"
    "v_exp_f32 v74, v74\n"
    "v_exp_f32 v75, v75\n"
    "v_exp_f32 v76, v76\n"
    "v_exp_f32 v77, v77\n"
    "v_exp_f32 v78, v78\n"
    "v_exp_f32 v79, v79\n"
    "v_exp_f32 v80, v80\n"
    "v_exp_f32 v81, v81\n"
    "v_exp_f32 v82, v82\n"
    "v_exp_f32 v83, v83\n"
    "v_exp_f32 v84, v84\n"
    "v_exp_f32 v85, v85\n"
    "v_exp_f32 v86, v86\n"
    "v_exp_f32 v87, v87\n"
    "v_exp_f32 v88, v88\n"
    "v_exp_f32 v89, v89\n"
    "v_exp_f32 v90, v90\n"
    "v_exp_f32 v91, v91\n"
    "v_exp_f32 v92, v92\n"
    "v_exp_f32 v93, v93\n"
    "v_exp_f32 v94, v94\n"
    "v_exp_f32 v95, v95\n"
    "v_cvt_pk_bf16_f32 v0, v64, v65\n"
    "v_cvt_pk_bf16_f32 v1, v66, v67\n"
    "v_cvt_pk_bf16_f32 v2, v68, v69\n"
    "v_cvt_pk_bf16_f32 v3, v70, v71\n"
    "v_cvt_pk_bf16_f32 v4, v72, v73\n"
    "v_cvt_pk_bf16_f32 v5, v74, v75\n"
    "v_cvt_pk_bf16_f32 v6, v76, v77\n"
    "v_cvt_pk_bf16_f32 v7, v78, v79\n"
    "v_cvt_pk_bf16_f32 v8, v80, v81\n"
    "v_cvt_pk_bf16_f32 v9, v82, v83\n"
    "v_cvt_pk_bf16_f32 v10, v84, v85\n"
    "v_cvt_pk_bf16_f32 v11, v86, v87\n"
    "v_cvt_pk_bf16_f32 v12, v88, v89\n"
    "v_cvt_pk_bf16_f32 v13, v90, v91\n"
    "v_cvt_pk_bf16_f32 v14, v92, v93\n"
    "v_cvt_pk_bf16_f32 v15, v94, v95\n"
    "v_mfma_f32_32x32x16_bf16 v[32:47], v[184:187], v[0:3], v[32:47]\n"
    "v_mfma_f32_32x32x16_bf16 v[32:47], v[188:191], v[4:7], v[32:47]\n"
    "v_mfma_f32_32x32x16_bf16 v[48:63], v[184:187], v[8:11], v[48:63]\n"
    "v_mfma_f32_32x32x16_bf16 v[48:63], v[188:191], v[12:15], v[48:63]\n"
    "s_nop 15\n"
    "s_nop 15\n"
    "s_add_u32 s73, s73, 1\n"
    "s_cmp_lt_u32 s73, 64\n"
    "s_cbranch_scc1 .Lfbl1\n"
    "s_branch .Lfbr\n"
    :
    : "s"(__builtin_amdgcn_kernarg_segment_ptr()), "s"(blockIdx.x), "s"(wave + (unsigned)(size_t)smem)
    : "v0", "v1", "v2", "v3", "v4", "v5", "v6", "v7", "v8", "v9", "v10", "v11", "v12", "v13", "v14", "v15", "v16", "v17", "v18", "v19", "v20", "v21", "v22", "v23", "v24", "v25", "v26", "v27", "v28", "v29", "v30", "v31", "v32", "v33", "v34", "v35", "v36", "v37", "v38", "v39", "v40", "v41", "v42", "v43", "v44", "v45", "v46", "v47", "v48", "v49", "v50", "v51", "v52", "v53", "v54", "v55", "v56", "v57", "v58", "v59", "v60", "v61", "v62", "v63", "v64", "v65", "v66", "v67", "v68", "v69", "v70", "v71", "v72", "v73", "v74", "v75", "v76", "v77", "v78", "v79", "v80", "v81", "v82", "v83", "v84", "v85", "v86", "v87", "v88", "v89", "v90", "v91", "v92", "v93", "v94", "v95", "v96", "v97", "v98", "v99", "v100", "v101", "v102", "v103", "v104", "v105", "v106", "v107", "v108", "v109", "v110", "v111", "v112", "v113", "v114", "v115", "v116", "v117", "v118", "v119", "v120", "v121", "v122", "v123", "v124", "v125", "v126", "v127", "v128", "v129", "v130", "v131", "v132", "v133", "v134", "v135", "v136", "v137", "v138", "v139", "v140", "v141", "v142", "v143", "v144", "v145", "v146", "v147", "v148", "v149", "v150", "v151", "v152", "v153", "v154", "v155", "v156", "v157", "v158", "v159", "v160", "v161", "v162", "v163", "v164", "v165", "v166", "v167", "v168", "v169", "v170", "v171", "v172", "v173", "v174", "v175", "v176", "v177", "v178", "v179", "v180", "v181", "v182", "v183", "v184", "v185", "v186", "v187", "v188", "v189", "v190", "v191", "v192", "v193", "v194", "v195", "v196", "v197", "v198", "v199", "v200", "v201", "v202", "v203", "v204", "v205", "v206", "v207", "v208", "v209", "v210", "v211", "v212", "v213", "v214", "v215", "v216", "v217", "v218", "v219", "v220", "v221", "v222", "v223", "v224", "v225", "v226", "v227", "v228", "v229", "v230", "v231", "v232", "v233", "v234", "v235", "v236", "v237", "v238", "v239", "v240", "v241", "v242", "v243", "v244", "v245", "v246", "v247", "v248", "v249", "v250", "v251", "v252", "v253", "v254", "v255", "a0", "a1", "a2", "a3", "a4", "a5", "a6", "a7", "a8", "a9", "a10", "a11", "a12", "a13", "a14", "a15", "a16", "a17", "a18", "a19", "a20", "a21", "a22", "a23", "a24", "a25", "a26", "a27", "a28", "a29", "a30", "a31", "a32", "a33", "a34", "a35", "a36", "a37", "a38", "a39", "a40", "a41", "a42", "a43", "a44", "a45", "a46", "a47", "a48", "a49", "a50", "a51", "a52", "a53", "a54", "a55", "a56", "a57", "a58", "a59", "a60", "a61", "a62", "a63", "a64", "a65", "a66", "a67", "a68", "a69", "a70", "a71", "a72", "a73", "a74", "a75", "a76", "a77", "a78", "a79", "a80", "a81", "a82", "a83", "a84", "a85", "a86", "a87", "a88", "a89", "a90", "a91", "a92", "a93", "a94", "a95", "a96", "a97", "a98", "a99", "a100", "a101", "a102", "a103", "a104", "a105", "a106", "a107", "a108", "a109", "a110", "a111", "a112", "a113", "a114", "a115", "a116", "a117", "a118", "a119", "a120", "a121", "a122", "a123", "a124", "a125", "a126", "a127", "a128", "a129", "a130", "a131", "a132", "a133", "a134", "a135", "a136", "a137", "a138", "a139", "a140", "a141", "a142", "a143", "a144", "a145", "a146", "a147", "a148", "a149", "a150", "a151", "a152", "a153", "a154", "a155", "a156", "a157", "a158", "a159", "a160", "a161", "a162", "a163", "a164", "a165", "a166", "a167", "a168", "a169", "a170", "a171", "a172", "a173", "a174", "a175", "a176", "a177", "a178", "a179", "a180", "a181", "a182", "a183", "a184", "a185", "a186", "a187", "a188", "a189", "a190", "a191", "a192", "a193", "a194", "a195", "a196", "a197", "a198", "a199", "a200", "a201", "a202", "a203", "a204", "a205", "a206", "a207", "a208", "a209", "a210", "a211", "a212", "a213", "a214", "a215", "a216", "a217", "a218", "a219", "a220", "a221", "a222", "a223", "a224", "a225", "a226", "a227", "a228", "a229", "a230", "a231", "a232", "a233", "a234", "a235", "a236", "a237", "a238", "a239", "a240", "a241", "a242", "a243", "a244", "a245", "a246", "a247", "a248", "a249", "a250", "a251", "a252", "a253", "a254", "a255", "s4", "s5", "s6", "s7", "s8", "s9", "s10", "s11", "s12", "s13", "s14", "s15", "s16", "s17", "s18", "s19", "s20", "s21", "s22", "s23", "s24", "s25", "s26", "s27", "s28", "s29", "s30", "s31", "s32", "s33", "s34", "s35", "s36", "s37", "s38", "s39", "s40", "s41", "s42", "s43", "s44", "s45", "s46", "s47", "s48", "s49", "s50", "s51", "s52", "s53", "s54", "s55", "s56", "s57", "s58", "s59", "s60", "s61", "s62", "s63", "s64", "s65", "s66", "s67", "s68", "s69", "s70", "s71", "s72", "s73", "s74", "s75", "s76", "s77", "s78", "s79", "s80", "s81", "s82", "s83", "s84", "s85", "s86", "s87", "s88", "s89", "s90", "s91", "s92", "s93", "s94", "s95", "s96", "s97", "s98", "s99", "s100", "s101", "vcc", "scc", "memory");
}

extern "C" void kernel_launch(void* const* d_in, const int* in_sizes, int n_in, void* d_out, int out_size, void* d_ws,
                              size_t ws_size, hipStream_t stream) {
  fused_mix_kernel<<<256, 256, 0, stream>>>(d_in[0], d_in[1], d_in[2], d_in[3], d_in[4], d_in[6], d_in[7], d_in[8],
                                            d_in[9], d_out, d_ws);
}
```
